```python
import torch

def _softmax(x, axis=-1):
    return torch.softmax(x, dim=axis)

def _gelu(x):
    return torch.nn.functional.gelu(x, approximate='tanh')

def _silu(x):
    return torch.nn.functional.silu(x)

def _sigmoid(x):
    return torch.sigmoid(x)

def _layernorm(x, axis=-1, eps=1e-05):
    m = x.mean(dim=axis, keepdim=True)
    v = x.var(dim=axis, keepdim=True, unbiased=False)
    return (x - m) / torch.sqrt(v + eps)

def _scatter_add(tgt, idx, src, axis=0):
    return tgt.clone().index_add_(axis, idx, src)

def _segment_sum(src, seg, n):
    out = torch.zeros((n,) + src.shape[1:], dtype=src.dtype)
    return out.index_add_(0, seg, src)

def _pad(x, pw):
    flat = tuple((v for pair in reversed(pw) for v in pair))
    return torch.nn.functional.pad(x, flat)
kernel_name = 'adaLN_cond__sliding_block_cached__geglu_ffn__decode'

def setup_inputs():

    _w = lambda a: (lambda t: t.to(torch.bfloat16) if t.is_floating_point() else t)(torch.as_tensor(a))

    def _A_setup():
        B, Sq, D = (1, 1, 576)
        Hq, Hkv, Sk, Dh = (32, 16, 65536, 128)
        assert Hq % Hkv == 0, (Hq, Hkv)
        return {'x': _w((lambda *s: torch.randn(*s, dtype=torch.bfloat16) * 0.05)(B, Sq, D)), 'k_cache': _w((lambda *s: torch.randn(*s, dtype=torch.bfloat16) * 3.2)(B, Hkv, Sk, Dh)), 'v_cache': _w((lambda *s: torch.randn(*s, dtype=torch.bfloat16) * 0.4)(B, Hkv, Sk, Dh)), 'wq': _w((lambda *s: torch.randn(*s, dtype=torch.bfloat16) * 3.2)(D, Hq * Dh) * D ** (-0.5)), 'wo': _w((lambda *s: torch.randn(*s, dtype=torch.bfloat16) * 0.4)(Hq * Dh, D) * (Hq * Dh) ** (-0.5))}
    _A_in = _A_setup()

    def _B_setup():
        B, S, D, F = (1, 1, 576, 4608)
        return {'x': _w((lambda *s: torch.randn(*s, dtype=torch.bfloat16) * 0.05)(B, S, D)), 'w_gate': _w((lambda *s: torch.randn(*s, dtype=torch.bfloat16) * 0.05)(D, F)), 'w_up': _w((lambda *s: torch.randn(*s, dtype=torch.bfloat16) * 0.05)(D, F)), 'w_down': _w((lambda *s: torch.randn(*s, dtype=torch.bfloat16) * 0.05)(F, D))}
    _B_in = _B_setup()
    return {'A_x': _A_in['x'], 'A_k_cache': _A_in['k_cache'], 'A_v_cache': _A_in['v_cache'], 'A_wq': _A_in['wq'], 'A_wo': _A_in['wo'], 'B_x': _B_in['x'], 'B_w_gate': _B_in['w_gate'], 'B_w_up': _B_in['w_up'], 'B_w_down': _B_in['w_down'], 'c': _w((lambda *s: torch.randn(*s, dtype=torch.bfloat16) * 0.1)(1, 576) * 8.0), 'w_mod': _w((lambda *s: torch.randn(*s, dtype=torch.bfloat16) * 0.1)(576, 6 * 576) * 576 ** (-0.5) * 8.0)}

def reference(A_x, A_k_cache, A_v_cache, A_wq, A_wo, B_x, B_w_gate, B_w_up, B_w_down, c, w_mod):

    def _A_ref(x, k_cache, v_cache, wq, wo):
        B, Sq, D = x.shape
        Hq, Dh = (32, 128)
        Hkv, Sk = (k_cache.shape[1], k_cache.shape[2])
        W = min(4096, max(256, Sk // 4))
        rep = Hq // Hkv
        q = (lambda x, *d: x.permute(*d))((x @ wq).reshape(B, Sq, Hq, Dh), 0, 2, 1, 3)
        k = (lambda x, n, axis: torch.repeat_interleave(x, n, dim=axis))(k_cache[:, :, Sk - W:, :], rep, axis=1)
        v = (lambda x, n, axis: torch.repeat_interleave(x, n, dim=axis))(v_cache[:, :, Sk - W:, :], rep, axis=1)
        scores = torch.einsum('bhqd,bhkd->bhqk', q, k) * Dh ** (-0.5)
        p = _softmax(scores, axis=-1)
        o = torch.einsum('bhqk,bhkd->bhqd', p, v)
        return (lambda x, *d: x.permute(*d))(o, 0, 2, 1, 3).reshape(B, Sq, Hq * Dh) @ wo

    def _B_ref(x, w_gate, w_up, w_down):
        return _gelu(x @ w_gate) * (x @ w_up) @ w_down
    x = A_x
    mod = (_silu(c) @ w_mod).reshape(c.shape[0], 6, 1, -1)
    sa, ca, ga, sb, cb, gb = (mod[:, 0].clone(), mod[:, 1].clone(), mod[:, 2].clone(), mod[:, 3].clone(), mod[:, 4].clone(), mod[:, 5].clone())
    h = _layernorm(x, axis=-1) * (1 + ca) + sa
    x = x + ga * _A_ref(h, A_k_cache, A_v_cache, A_wq, A_wo)
    h = _layernorm(x, axis=-1) * (1 + cb) + sb
    x = x + gb * _B_ref(h, B_w_gate, B_w_up, B_w_down)
    return x
```

```cpp
#include <hip/hip_runtime.h>
#include <hip/hip_bf16.h>
#include <stdint.h>

typedef unsigned short u16;
__device__ __forceinline__ float bf2f(u16 b){ return __uint_as_float(((unsigned)b)<<16); }
__device__ __forceinline__ u16 f2bf(float f){ unsigned u=__float_as_uint(f); u += 0x7FFFu + ((u>>16)&1u); return (u16)(u>>16); }
__device__ __forceinline__ float rbf(float f){ return bf2f(f2bf(f)); }

constexpr int D=576, HQ=32, HKV=16, SK=65536, DH=128, W=4096, F=4608;

__global__ void k_gemv(const float* in, const u16* Wt, float* out, int K, int N){
  int n = blockIdx.x*64 + (threadIdx.x&63);
  int kq = threadIdx.x>>6;
  __shared__ float red[4][64];
  float acc=0.f;
  if(n<N) for(int k=kq;k<K;k+=4) acc += in[k]*bf2f(Wt[(size_t)k*N+n]);
  red[kq][threadIdx.x&63]=acc;
  __syncthreads();
  if(kq==0 && n<N) out[n]=rbf(red[0][threadIdx.x]+red[1][threadIdx.x]+red[2][threadIdx.x]+red[3][threadIdx.x]);
}
__global__ void k_silu(const u16* c, float* o){ int i=threadIdx.x+blockIdx.x*blockDim.x; if(i<D){ float x=bf2f(c[i]); o[i]=rbf(x/(1.f+__expf(-x))); } }

__global__ void k_ln(const float* x, const float* shift, const float* scale, float* h){
  __shared__ float red[16];
  int t=threadIdx.x;
  float v = x[t];
  float s=v; for(int o=32;o;o>>=1) s+=__shfl_xor(s,o);
  if((t&63)==0) red[t>>6]=s; __syncthreads();
  float tot=0; for(int i=0;i<9;i++) tot+=red[i];
  float mean_f = tot/576.f; float m=rbf(mean_f);
  __syncthreads();
  float d=(v-mean_f); float q=d*d; for(int o=32;o;o>>=1) q+=__shfl_xor(q,o);
  if((t&63)==0) red[t>>6]=q; __syncthreads();
  float tq=0; for(int i=0;i<9;i++) tq+=red[i];
  float var=rbf(tq/576.f);
  float xm=rbf(v-m);
  float den=rbf(sqrtf(rbf(var+1e-5f)));
  float ln=rbf(xm/den);
  h[t]=rbf(rbf(ln*rbf(1.f+scale[t]))+shift[t]);
}
__global__ void k_cvt(const u16* a, float* o, int n){ int i=threadIdx.x+blockIdx.x*blockDim.x; if(i<n) o[i]=bf2f(a[i]); }

__global__ void k_attn(const float* q, const u16* kc, const u16* vc, float* o){
  int h=blockIdx.x; int g=h/2;
  __shared__ float sc[W]; __shared__ float qs[DH]; __shared__ float red[8];
  int t=threadIdx.x;
  if(t<DH) qs[t]=q[h*DH+t];
  __syncthreads();
  const u16* kb=kc+((size_t)g*SK+(SK-W))*DH;
  const u16* vb=vc+((size_t)g*SK+(SK-W))*DH;
  float mx=-INFINITY;
  for(int j=t;j<W;j+=256){ float a=0; for(int d=0;d<DH;d++) a+=qs[d]*bf2f(kb[(size_t)j*DH+d]); a=rbf(rbf(a)*0.08838834764831845f); sc[j]=a; mx=fmaxf(mx,a);}
  for(int o_=32;o_;o_>>=1) mx=fmaxf(mx,__shfl_xor(mx,o_));
  if((t&63)==0) red[t>>6]=mx; __syncthreads();
  mx=fmaxf(fmaxf(red[0],red[1]),fmaxf(red[2],red[3])); __syncthreads();
  float s=0; for(int j=t;j<W;j+=256){ float e=__expf(sc[j]-mx); sc[j]=e; s+=e; }
  for(int o_=32;o_;o_>>=1) s+=__shfl_xor(s,o_);
  if((t&63)==0) red[t>>6]=s; __syncthreads();
  s=red[0]+red[1]+red[2]+red[3];
  float inv=1.f/s;
  for(int j=t;j<W;j+=256) sc[j]=rbf(sc[j]*inv);
  __syncthreads();
  if(t<DH){ float a=0; for(int j=0;j<W;j++) a+=sc[j]*bf2f(vb[(size_t)j*DH+t]); o[h*DH+t]=rbf(a);}
}
__global__ void k_res(const float* x, const float* gate, const float* y, float* o){ int t=threadIdx.x+blockIdx.x*blockDim.x; if(t<D) o[t]=rbf(x[t]+rbf(gate[t]*y[t])); }
__global__ void k_act(const float* g, const float* u, float* a){ int t=threadIdx.x+blockIdx.x*blockDim.x; if(t<F){ float x=g[t]; float ge=0.5f*x*(1.f+tanhf(0.7978845608028654f*(x+0.044715f*x*x*x))); a[t]=rbf(rbf(ge)*u[t]); } }
__global__ void k_out(const float* x, u16* o){ int t=threadIdx.x+blockIdx.x*blockDim.x; if(t<D) o[t]=f2bf(x[t]); }

extern "C" void kernel_launch(void* const* d_in, const int* in_sizes, int n_in, void* d_out, int out_size, void* d_ws, size_t ws_size, hipStream_t stream){
  const u16* x=(const u16*)d_in[0]; const u16* kc=(const u16*)d_in[1]; const u16* vc=(const u16*)d_in[2];
  const u16* wq=(const u16*)d_in[3]; const u16* wo=(const u16*)d_in[4];
  const u16* wg=(const u16*)d_in[6]; const u16* wu=(const u16*)d_in[7]; const u16* wd=(const u16*)d_in[8];
  const u16* c=(const u16*)d_in[9]; const u16* wm=(const u16*)d_in[10];
  float* ws=(float*)d_ws;
  float* sc=ws; float* mod=ws+1024; float* xf=ws+8192; float* h=ws+9216; float* q=ws+10240; float* o=ws+16384; float* attn=ws+24576; float* x1=ws+25600; float* h2=ws+26624; float* g=ws+32768; float* u=ws+40960; float* act=ws+49152; float* y=ws+57344; float* x2=ws+58368;
  k_silu<<<3,256,0,stream>>>(c,sc);
  k_gemv<<<3456/64,256,0,stream>>>(sc,wm,mod,D,3456);
  k_cvt<<<3,256,0,stream>>>(x,xf,D);
  k_ln<<<1,576,0,stream>>>(xf,mod+0,mod+576,h);
  k_gemv<<<4096/64,256,0,stream>>>(h,wq,q,D,4096);
  k_attn<<<32,256,0,stream>>>(q,kc,vc,o);
  k_gemv<<<9,256,0,stream>>>(o,wo,attn,4096,D);
  k_res<<<3,256,0,stream>>>(xf,mod+2*576,attn,x1);
  k_ln<<<1,576,0,stream>>>(x1,mod+3*576,mod+4*576,h2);
  k_gemv<<<F/64,256,0,stream>>>(h2,wg,g,D,F);
  k_gemv<<<F/64,256,0,stream>>>(h2,wu,u,D,F);
  k_act<<<F/256,256,0,stream>>>(g,u,act);
  k_gemv<<<9,256,0,stream>>>(act,wd,y,F,D);
  k_res<<<3,256,0,stream>>>(x1,mod+5*576,y,x2);
  k_out<<<3,256,0,stream>>>(x2,(u16*)d_out);
}
```

```cpp
#include <hip/hip_runtime.h>
#include <hip/hip_bf16.h>
#include <stdint.h>

namespace {

typedef unsigned short u16;
typedef unsigned v4u __attribute__((ext_vector_type(4)));
typedef unsigned v2u __attribute__((ext_vector_type(2)));
typedef float f32x4 __attribute__((ext_vector_type(4)));
typedef float f32x2 __attribute__((ext_vector_type(2)));
typedef __bf16 bf16x8 __attribute__((ext_vector_type(8)));
typedef __bf16 bf16x2 __attribute__((ext_vector_type(2)));
typedef __amdgpu_buffer_rsrc_t rsrc_t;
#define LAS __attribute__((address_space(3)))
#define CAS __attribute__((address_space(4)))

constexpr int D = 576, SK = 65536, WIN = 4096, FF = 4608;
constexpr int NTHR = 768;
constexpr int WS_EPOCH = 0;
constexpr int WS_S1 = 4096;
constexpr int WS_SQ = WS_S1 + 72 * 256;
constexpr int WS_SB = WS_SQ + 64 * 256;
constexpr int WS_S4 = WS_SB + 36 * 256;
constexpr int WS_S5 = WS_S4 + 144 * 256;
constexpr int WS_S3 = WS_S5 + 648 * 256;
constexpr int S3_STRIDE = 1280;
constexpr int WS_END = WS_S3 + 176 * S3_STRIDE;
constexpr int WS_STAMP = 1 << 20;
constexpr unsigned SPIN_MAX = 1u << 14;

struct Params {
  const u16 *x, *kc, *vc, *wq, *wo, *wg, *wu, *wd, *c, *wm;
  u16* out;
  unsigned char* ws;
};

constexpr int L_SH = 0;
constexpr int SH_STRIDE = 160;
constexpr int L_REDA = L_SH + 12 * SH_STRIDE;
constexpr int L_REDG = L_REDA + 1152;
constexpr int L_ST = L_REDG + 1152;
constexpr int L_Q = L_ST + 64;
constexpr int L_MW = L_Q + 128;
constexpr int L_LW = L_MW + 32;
constexpr int L_CNT = L_LW + 32;
constexpr int L_O = L_CNT + 16;
constexpr int L_END = L_O + 12 * 256;

__device__ __forceinline__ rsrc_t mk(const void* p, unsigned bytes) {
  return __builtin_amdgcn_make_buffer_rsrc((void*)p, 0, (int)bytes, 0x00020000);
}
__device__ __forceinline__ v4u ldnt(rsrc_t r, int v, int s) { return __builtin_amdgcn_raw_buffer_load_b128(r, v, s, 2); }
__device__ __forceinline__ v4u ldsc(rsrc_t r, int v, int s) { return __builtin_amdgcn_raw_buffer_load_b128(r, v, s, 16); }
__device__ __forceinline__ unsigned ldsc1(rsrc_t r, int v, int s) { return __builtin_amdgcn_raw_buffer_load_b32(r, v, s, 16); }
__device__ __forceinline__ void stsc1(rsrc_t r, unsigned x, int v, int s) { __builtin_amdgcn_raw_buffer_store_b32(x, r, v, s, 16); }
__device__ __forceinline__ void stsc4(rsrc_t r, v4u x, int v, int s) { __builtin_amdgcn_raw_buffer_store_b128(x, r, v, s, 16); }
__device__ __forceinline__ float asf(unsigned x) { return __uint_as_float(x); }
__device__ __forceinline__ unsigned asu(float x) { return __float_as_uint(x); }
__device__ __forceinline__ unsigned rep(float x) {
  f32x2 v = {x, x};
  return __builtin_bit_cast(unsigned, __builtin_convertvector(v, bf16x2));
}
__device__ __forceinline__ float rb(float x) { return asf(rep(x) & 0xFFFF0000u); }
__device__ __forceinline__ float bf(u16 x) { return asf((unsigned)x << 16); }
__device__ __forceinline__ unsigned tg(float x, unsigned T) { return (asu(x) & 0xFFFFFF00u) | T; }
__device__ __forceinline__ v4u tg4(f32x4 y, unsigned T) { return v4u{tg(y.x, T), tg(y.y, T), tg(y.z, T), tg(y.w, T)}; }
__device__ __forceinline__ unsigned or4(v4u d) { return (d.x | d.y) | (d.z | d.w); }
__device__ __forceinline__ bool allok(unsigned bad) { return __all((bad & 0xFFu) == 0u); }
template <int C> __device__ __forceinline__ float dpp(float x) {
  return asf((unsigned)__builtin_amdgcn_update_dpp(0, (int)asu(x), C, 0xF, 0xF, true));
}
__device__ __forceinline__ float xrow_sum(float x) {
  auto s = __builtin_amdgcn_permlane16_swap(asu(x), asu(x), false, false);
  x = asf(s[0]) + asf(s[1]);
  auto t = __builtin_amdgcn_permlane32_swap(asu(x), asu(x), false, false);
  return asf(t[0]) + asf(t[1]);
}
__device__ __forceinline__ float xrow_max(float x) {
  auto s = __builtin_amdgcn_permlane16_swap(asu(x), asu(x), false, false);
  x = fmaxf(asf(s[0]), asf(s[1]));
  auto t = __builtin_amdgcn_permlane32_swap(asu(x), asu(x), false, false);
  return fmaxf(asf(t[0]), asf(t[1]));
}
__device__ __forceinline__ float tr4(f32x4 v) {
  auto a = __builtin_amdgcn_permlane16_swap(asu(v.x), asu(v.y), false, false);
  auto b = __builtin_amdgcn_permlane16_swap(asu(v.z), asu(v.w), false, false);
  const float s01 = asf(a[0]) + asf(a[1]), s23 = asf(b[0]) + asf(b[1]);
  auto c = __builtin_amdgcn_permlane32_swap(asu(s01), asu(s23), false, false);
  return asf(c[0]) + asf(c[1]);
}
__device__ __forceinline__ f32x2 wave_sum2(f32x2 v) {
  v += f32x2{dpp<0xB1>(v.x), dpp<0xB1>(v.y)};
  v += f32x2{dpp<0x4E>(v.x), dpp<0x4E>(v.y)};
  v += f32x2{dpp<0x141>(v.x), dpp<0x141>(v.y)};
  v += f32x2{dpp<0x140>(v.x), dpp<0x140>(v.y)};
  auto a = __builtin_amdgcn_permlane16_swap(asu(v.x), asu(v.y), false, false);
  const float s = asf(a[0]) + asf(a[1]);
  auto c = __builtin_amdgcn_permlane32_swap(asu(s), asu(s), false, false);
  const float t = asf(c[0]) + asf(c[1]);
  return f32x2{t, t};
}
__device__ __forceinline__ f32x4 asf4(v4u d) { return __builtin_bit_cast(f32x4, d); }
__device__ __forceinline__ f32x4 mfma(v4u a, v4u b, f32x4 c) {
  return __builtin_amdgcn_mfma_f32_16x16x32_bf16(__builtin_bit_cast(bf16x8, a), __builtin_bit_cast(bf16x8, b), c, 0, 0, 0);
}
struct Mask { unsigned m0, m1, m2, m3; };
__device__ __forceinline__ v4u diag(unsigned hh, const Mask& k) { return v4u{hh & k.m0, hh & k.m1, hh & k.m2, hh & k.m3}; }
__device__ __forceinline__ void bar() {
  asm volatile("s_waitcnt lgkmcnt(0)" ::: "memory");
  __builtin_amdgcn_s_barrier();
  asm volatile("" ::: "memory");
}
__device__ __forceinline__ void rawbar() {
  __builtin_amdgcn_sched_barrier(0);
  __builtin_amdgcn_s_barrier();
  __builtin_amdgcn_sched_barrier(0);
}
__device__ __forceinline__ void ldswait() { asm volatile("s_waitcnt lgkmcnt(0)" ::: "memory"); }
__device__ __forceinline__ float silu(float v) { return v * __builtin_amdgcn_rcpf(1.f + __expf(-v)); }
__device__ __forceinline__ float fold1(float x) {
  auto t = __builtin_amdgcn_permlane32_swap(asu(x), asu(x), false, false);
  return asf(t[0]) + dpp<0x128>(asf(t[1]));
}
__device__ __forceinline__ f32x4 fold(f32x4 a) { return f32x4{fold1(a.x), fold1(a.y), fold1(a.z), fold1(a.w)}; }
__device__ __forceinline__ float fold2_1(float a, float b) {
  auto t = __builtin_amdgcn_permlane32_swap(asu(a), asu(b), false, false);
  return asf(t[0]) + dpp<0x128>(asf(t[1]));
}

#define STAMP(i) do {} while (0)
#define FINISH() do {} while (0)

__device__ __forceinline__ void shuffle1(LAS unsigned* sh, int lane, float v, v4u& h0, v4u& h1) {
  const int row = lane >> 4, c4 = lane & 15;
  sh[32 * (c4 & 1) + 8 * row + (c4 >> 1)] = rep(v);
  ldswait();
  h0 = *(LAS v4u*)(sh + (lane >> 3) * 8);
  h1 = *(LAS v4u*)(sh + (lane >> 3) * 8 + 4);
}
__device__ __forceinline__ f32x4 gemv8(const v4u (&w)[8], v4u h0, v4u h1, const Mask& k) {
  f32x4 a = {0.f, 0.f, 0.f, 0.f}, b = {0.f, 0.f, 0.f, 0.f};
  a = mfma(diag(h0.x, k), w[0], a); b = mfma(diag(h0.y, k), w[1], b);
  a = mfma(diag(h0.z, k), w[2], a); b = mfma(diag(h0.w, k), w[3], b);
  a = mfma(diag(h1.x, k), w[4], a); b = mfma(diag(h1.y, k), w[5], b);
  a = mfma(diag(h1.z, k), w[6], a); b = mfma(diag(h1.w, k), w[7], b);
  return a + b;
}
__device__ __forceinline__ void put_halves(LAS float* red, int lane, f32x4 a) {
  const int c = lane & 15, g = lane >> 4;
  if ((c >> 3) == (g >> 1)) *(LAS f32x4*)(red + (c >> 3) * 64 + 8 * (c & 7) + 4 * (g & 1)) = a;
}
__device__ __forceinline__ void ln_put(LAS float* st, int j, int lane, float v, float shift) {
  const float d = v - shift;
  const f32x2 s = wave_sum2(f32x2{d, d * d});
  if (lane == 0 || lane == 16) st[2 * j + (lane >> 4)] = s.x;
}
__device__ __forceinline__ f32x2 ln_sums(LAS float* st) {
  const f32x4 a = *(LAS f32x4*)(st), b = *(LAS f32x4*)(st + 4), c = *(LAS f32x4*)(st + 8), d = *(LAS f32x4*)(st + 12);
  const f32x2 e = *(LAS f32x2*)(st + 16);
  const f32x4 t = (a + b) + (c + d);
  return f32x2{t.x + t.z + e.x, t.y + t.w + e.y};
}
template <int N> __device__ __forceinline__ float sumN(LAS float* red, int lane) {
  float a = 0.f, b = 0.f;
#pragma unroll
  for (int i = 0; i < N; i += 2) { a += red[i * 64 + lane]; b += red[i * 64 + 64 + lane]; }
  return a + b;
}
__device__ __forceinline__ void loads8(v4u (&w)[8], rsrc_t r, int stride, int lane, int base) {
  const int vo = (lane >> 3) * stride + (lane & 7) * 16;
#pragma unroll
  for (int s = 0; s < 8; ++s) w[s] = ldnt(r, vo, base + 8 * s * stride);
}
struct Tile18 { v4u w[18]; v2u c; };
__device__ __forceinline__ void tile18_load(Tile18& t, const u16* c, rsrc_t rwm, int ks, int cb, int lane) {
  t.c = *(const v2u*)(c + 144 * ks + 4 * (lane < 36 ? lane : 35));
  const int vo = (lane >> 3) * 6912 + (lane & 7) * 16;
#pragma unroll
  for (int s = 0; s < 18; ++s) t.w[s] = ldnt(rwm, vo, (144 * ks + 8 * s) * 6912 + cb);
}
__device__ __forceinline__ f32x4 tile18_mac(const Tile18& t, LAS unsigned* sh, int lane, const Mask& k) {
  if (lane < 36) {
    LAS unsigned* d = sh + (4 * (lane & 1)) * 20 + (lane >> 1);
    d[0] = rep(silu(asf(t.c.x << 16))); d[20] = rep(silu(asf(t.c.x & 0xFFFF0000u)));
    d[40] = rep(silu(asf(t.c.y << 16))); d[60] = rep(silu(asf(t.c.y & 0xFFFF0000u)));
  }
  ldswait();
  LAS unsigned* s = sh + (lane >> 3) * 20;
  const v4u h0 = *(LAS v4u*)s, h1 = *(LAS v4u*)(s + 4), h2 = *(LAS v4u*)(s + 8), h3 = *(LAS v4u*)(s + 12);
  const v2u h4 = *(LAS v2u*)(s + 16);
  f32x4 a = {0.f, 0.f, 0.f, 0.f}, b = {0.f, 0.f, 0.f, 0.f};
  a = mfma(diag(h0.x, k), t.w[0], a); b = mfma(diag(h0.y, k), t.w[1], b); a = mfma(diag(h0.z, k), t.w[2], a); b = mfma(diag(h0.w, k), t.w[3], b);
  a = mfma(diag(h1.x, k), t.w[4], a); b = mfma(diag(h1.y, k), t.w[5], b); a = mfma(diag(h1.z, k), t.w[6], a); b = mfma(diag(h1.w, k), t.w[7], b);
  a = mfma(diag(h2.x, k), t.w[8], a); b = mfma(diag(h2.y, k), t.w[9], b); a = mfma(diag(h2.z, k), t.w[10], a); b = mfma(diag(h2.w, k), t.w[11], b);
  a = mfma(diag(h3.x, k), t.w[12], a); b = mfma(diag(h3.y, k), t.w[13], b); a = mfma(diag(h3.z, k), t.w[14], a); b = mfma(diag(h3.w, k), t.w[15], b);
  a = mfma(diag(h4.x, k), t.w[16], a); b = mfma(diag(h4.y, k), t.w[17], b);
  return a + b;
}
__device__ __forceinline__ unsigned lds_inc(LAS unsigned* c) {
  ldswait();
  unsigned old = 0;
  if ((threadIdx.x & 63) == 0) old = __hip_atomic_fetch_add(c, 1u, __ATOMIC_RELAXED, __HIP_MEMORY_SCOPE_WORKGROUP);
  return (unsigned)__builtin_amdgcn_readfirstlane((int)old);
}
__device__ __forceinline__ void modb_finish(const Tile18& t, LAS unsigned* sh, LAS float* red, LAS unsigned* cnt, int ks, int lane, const Mask& k,
                                            rsrc_t ws, int tile, unsigned T) {
  put_halves(red + ks * 128, lane, tile18_mac(t, sh, lane, k));
  if (lds_inc(cnt) == 3u) {
    asm volatile("" ::: "memory");
    stsc1(ws, tg(rb(sumN<8>(red, lane)), T), WS_SB + tile * 256 + lane * 4, 0);
  }
}

__global__ void __launch_bounds__(NTHR, 3) fused_decode(Params p) {
  __shared__ __attribute__((aligned(16))) unsigned smem[L_END];
  LAS unsigned* L = (LAS unsigned*)smem;
  LAS float* Lf = (LAS float*)smem;
  const int b = blockIdx.x, xcd = b & 7, slot = b >> 3;
  const int lane = threadIdx.x & 63, w = __builtin_amdgcn_readfirstlane(threadIdx.x >> 6);
  const int c4 = lane & 15, row = lane >> 4;
  const int col = 4 * c4 + row;
  STAMP(0);
  const unsigned E = *(const CAS unsigned*)(p.ws + WS_EPOCH);
  const unsigned T = (E + 1u) & 0xFFu;
  const rsrc_t ws = mk(p.ws, WS_END);
  Mask mk4;
  {
    const unsigned mm = (lane & 1) ? 0xFFFF0000u : 0x0000FFFFu;
    const int d = (lane & 7) >> 1;
    mk4.m0 = d == 0 ? mm : 0u; mk4.m1 = d == 1 ? mm : 0u; mk4.m2 = d == 2 ? mm : 0u; mk4.m3 = d == 3 ? mm : 0u;
  }
  LAS unsigned* sh = L + L_SH + w * SH_STRIDE;
  const rsrc_t rwm = mk(p.wm, D * 6 * D * 2);

  if (slot < 9) {
    if (w >= 10) return;
    const int f = xcd * 9 + slot;
    if (w == 9) {
      Tile18 t;
      tile18_load(t, p.c, rwm, f / 18, (f % 18) * 128, lane);
      rawbar();
      const f32x4 a = fold(tile18_mac(t, sh, lane, mk4));
      if (c4 < 8 && row < 2) stsc4(ws, tg4(a, T), WS_S1 + f * 256 + (8 * c4 + 4 * row) * 4, 0);
      STAMP(1);
      FINISH();
      return;
    }
    const int j = w;
    const bool hasq = slot < 8;
    const u16 xr = p.x[64 * j + col], cr = p.c[64 * j + col];
    rawbar();
    v4u wt[8];
    {
      const rsrc_t rq = hasq ? mk(p.wq, D * 4096 * 2) : rwm;
      const int st = hasq ? 8192 : 6912;
      loads8(wt, rq, st, lane, 64 * j * st + (hasq ? (xcd * 8 + slot) * 128 : 2304 + xcd * 128));
    }
    __builtin_amdgcn_sched_barrier(0);
    const float x0 = asf(*(const CAS unsigned*)p.x << 16);
    const float xv = bf(xr);
    ln_put(Lf + L_ST, j, lane, xv, x0);
    bar();
    STAMP(1);
    float in, mean1;
    {
      const f32x2 s = ln_sums(Lf + L_ST);
      const float dm = s.x * (1.f / D);
      mean1 = x0 + dm;
      const float var = fmaxf(s.y * (1.f / D) - dm * dm, 0.f);
      const float den = rb(__builtin_amdgcn_sqrtf(rb(rb(var) + 1e-5f)));
      const float ln = rb(rb(xv - rb(mean1)) / den);
      if (hasq) {
        v4u sa, ca;
        for (unsigned spin = 0;; ++spin) {
          sa = ldsc(ws, WS_S1 + (row * 18 + j) * 256 + c4 * 16, 0) ^ T;
          ca = ldsc(ws, WS_S1 + (row * 18 + 9 + j) * 256 + c4 * 16, 0) ^ T;
          __builtin_amdgcn_sched_barrier(0);
          if (allok(or4(sa) | or4(ca)) || spin > SPIN_MAX) break;
          __builtin_amdgcn_s_sleep(2);
        }
        in = rb(ln * rb(1.f + rb(tr4(asf4(ca))))) + rb(tr4(asf4(sa)));
      } else {
        in = silu(bf(cr));
      }
    }
    STAMP(2);
    {
      v4u h0, h1;
      shuffle1(sh, lane, in, h0, h1);
      put_halves(Lf + L_REDA + j * 128, lane, gemv8(wt, h0, h1, mk4));
    }
    bar();
    if (j == 0) stsc1(ws, tg(rb(sumN<18>(Lf + L_REDA, lane)), T), (hasq ? WS_SQ + (xcd * 8 + slot) * 256 : WS_SB + xcd * 256) + lane * 4, 0);
    rawbar();
    STAMP(3);
    v4u wg[8], wu[8], wd[8];
    loads8(wg, mk(p.wg, D * FF * 2), FF * 2, lane, 64 * j * FF * 2 + f * 128);
    loads8(wu, mk(p.wu, D * FF * 2), FF * 2, lane, 64 * j * FF * 2 + f * 128);
    loads8(wd, mk(p.wd, FF * D * 2), 1152, lane, 64 * f * 1152 + j * 128);
    __builtin_amdgcn_sched_barrier(0);
    float ga, sb, cb;
    {
      unsigned a, s2, c2;
      for (unsigned spin = 0;; ++spin) {
        a = ldsc1(ws, WS_SB + j * 256 + col * 4, 0) ^ T;
        s2 = ldsc1(ws, WS_SB + (9 + j) * 256 + col * 4, 0) ^ T;
        c2 = ldsc1(ws, WS_SB + (18 + j) * 256 + col * 4, 0) ^ T;
        __builtin_amdgcn_sched_barrier(0);
        if (allok(a | s2 | c2) || spin > SPIN_MAX) break;
        __builtin_amdgcn_s_sleep(2);
      }
      ga = asf(a); sb = asf(s2); cb = 1.f + asf(c2);
    }
    STAMP(4);
    float x1;
    {
      v4u q0, q1, q2, q3;
      for (unsigned spin = 0;; ++spin) {
        const int vo = WS_S4 + (j * 16 + row) * 256 + c4 * 16;
        q0 = ldsc(ws, vo, 0) ^ T; q1 = ldsc(ws, vo, 1024) ^ T; q2 = ldsc(ws, vo, 2048) ^ T; q3 = ldsc(ws, vo, 3072) ^ T;
        __builtin_amdgcn_sched_barrier(0);
        if (allok(or4(q0) | or4(q1) | or4(q2) | or4(q3)) || spin > SPIN_MAX) break;
        __builtin_amdgcn_s_sleep(1);
      }
      x1 = rb(xv + ga * tr4((asf4(q0) + asf4(q1)) + (asf4(q2) + asf4(q3))));
    }
    STAMP(5);
    ln_put(Lf + L_ST + 32, j, lane, x1, mean1);
    bar();
    {
      const f32x2 s = ln_sums(Lf + L_ST + 32);
      const float dm = s.x * (1.f / D);
      const float r = __builtin_amdgcn_rsqf(fmaxf(s.y * (1.f / D) - dm * dm, 0.f) + 1e-5f);
      const float h2 = (x1 - mean1 - dm) * r * cb + sb;
      v4u h0, h1;
      shuffle1(sh, lane, h2, h0, h1);
      const f32x4 g = gemv8(wg, h0, h1, mk4), u = gemv8(wu, h0, h1, mk4);
      const f32x4 t = {fold2_1(g.x, u.x), fold2_1(g.y, u.y), fold2_1(g.z, u.z), fold2_1(g.w, u.w)};
      if (c4 < 8) *(LAS f32x4*)(Lf + L_REDG + j * 128 + (row >> 1) * 64 + 8 * c4 + 4 * (row & 1)) = t;
    }
    bar();
    STAMP(6);
    {
      float g = 0.f, u = 0.f;
#pragma unroll
      for (int i = 0; i < 9; ++i) { g += Lf[L_REDG + i * 128 + lane]; u += Lf[L_REDG + i * 128 + 64 + lane]; }
      const float z = 1.5957691216057308f * (g + 0.044715f * g * g * g);
      sh[(lane & 7) * 8 + (lane >> 3)] = rep(g * __builtin_amdgcn_rcpf(1.f + __expf(-z)) * u);
      ldswait();
      const v4u h0 = *(LAS v4u*)(sh + (lane >> 3) * 8), h1 = *(LAS v4u*)(sh + (lane >> 3) * 8 + 4);
      const f32x4 y = fold(gemv8(wd, h0, h1, mk4));
      if (c4 < 8 && row < 2) stsc4(ws, tg4(y, T), WS_S5 + (j * 72 + f) * 256 + (8 * c4 + 4 * row) * 4, 0);
    }
    STAMP(7);
    FINISH();
    return;
  }

  if (slot == 9) {
    if (w >= 8 || (w >= 4 && xcd >= 4)) return;
    if (threadIdx.x < 2) L[L_CNT + threadIdx.x] = 0u;
    const int tile = (w < 4 ? 24 : 32) + xcd;
    Tile18 t;
    tile18_load(t, p.c, rwm, w & 3, 2304 + tile * 128, lane);
    __builtin_amdgcn_sched_barrier(0);
    bar();
    modb_finish(t, sh, Lf + L_REDA + (w >> 2) * 512, L + L_CNT + (w >> 2), w & 3, lane, mk4, ws, tile, T);
    STAMP(7);
    FINISH();
    return;
  }

  const int a = slot - 10, g = 2 * xcd + (a >= 11 ? 1 : 0), k = a >= 11 ? a - 11 : a;
  const bool light = k == 10;
  const int s3 = WS_S3 + (g * 11 + k) * S3_STRIDE;
  if (light && w >= 8) {
    Tile18 t;
    tile18_load(t, p.c, rwm, w - 8, 2304 + (8 + g) * 128, lane);
    if (w == 8 && lane < 32) Lf[L_MW + lane] = -INFINITY;
    if (w == 9 && lane < 32) Lf[L_LW + lane] = 0.f;
    if (w == 10 && lane < 2) L[L_CNT + lane] = 0u;
    bar();
    modb_finish(t, sh, Lf + L_REDA, L + L_CNT + 1, w - 8, lane, mk4, ws, 8 + g, T);
    STAMP(1);
    rawbar();
    rawbar();
  } else {
    const size_t kvoff = ((size_t)g * SK + (SK - WIN) + k * 384 + w * 32) * 128;
    const rsrc_t rk = mk(p.kc + kvoff, 8192), rv = mk(p.vc + kvoff, 8192);
    v4u kk[8], vv[8];
    const int kvo = (lane & 7) * 256 + ((lane >> 3) & 1) * 64 + row * 16, vvo = row * 2048 + c4 * 16;
#pragma unroll
    for (int t = 0; t < 4; ++t) {
      kk[2 * t] = ldnt(rk, kvo, t * 2048);
      kk[2 * t + 1] = ldnt(rk, kvo + 128, t * 2048);
    }
    if (!light) {
      if (w == 8 && lane < 32) Lf[L_MW + lane] = -INFINITY;
      if (w == 9 && lane < 32) Lf[L_LW + lane] = 0.f;
      if (w == 10 && lane < 2) L[L_CNT + lane] = 0u;
    }
    __builtin_amdgcn_sched_barrier(0);
    bar();
#pragma unroll
    for (int s = 0; s < 8; ++s) vv[s] = ldnt(rv, vvo + s * 256, 0);
    __builtin_amdgcn_sched_barrier(0);
    STAMP(1);
    if (w == 0) {
      v4u q;
      for (unsigned spin = 0;; ++spin) {
        q = ldsc(ws, WS_SQ + g * 1024 + lane * 16, 0) ^ T;
        if (allok(or4(q)) || spin > SPIN_MAX) break;
        __builtin_amdgcn_s_sleep(4);
      }
      *(LAS v2u*)(L + L_Q + lane * 2) = v2u{(q.x >> 16) | q.y, (q.z >> 16) | q.w};
    }
    bar();
    STAMP(2);
    float sc = 0.f;
    {
      const int qo = L_Q + (lane & 1) * 64 + ((lane >> 1) & 1) * 16 + row * 4;
      const v4u q0 = *(LAS v4u*)(L + qo), q1 = *(LAS v4u*)(L + qo + 32);
#pragma unroll
      for (int t = 0; t < 4; ++t) {
        f32x4 d = {0.f, 0.f, 0.f, 0.f};
        d = mfma(q0, kk[2 * t], d);
        d = mfma(q1, kk[2 * t + 1], d);
        const float h0 = d.x + dpp<0x128>(d.z), h1 = d.w + dpp<0x128>(d.y);
        const float v = c4 < 8 ? h0 : h1;
        sc = row == t ? v : sc;
      }
      sc = rb(rb(sc) * 0.08838834764831845f);
      float m = sc;
      m = fmaxf(m, dpp<0xB1>(m)); m = fmaxf(m, dpp<0x4E>(m)); m = fmaxf(m, dpp<0x141>(m));
      m = xrow_max(m);
      if ((lane & 55) == 0) Lf[L_MW + (lane >> 3) * 16 + w] = m;
    }
    bar();
    STAMP(3);
    {
      LAS float* mw = Lf + L_MW + ((lane >> 3) & 1) * 16;
      const f32x4 a0 = *(LAS f32x4*)mw, a1 = *(LAS f32x4*)(mw + 4), a2 = *(LAS f32x4*)(mw + 8);
      const float M = fmaxf(fmaxf(fmaxf(a0.x, a0.y), fmaxf(a0.z, a0.w)), fmaxf(fmaxf(fmaxf(a1.x, a1.y), fmaxf(a1.z, a1.w)), fmaxf(fmaxf(a2.x, a2.y), fmaxf(a2.z, a2.w))));
      const float pe = __expf(sc - M);
      float l = pe;
      l += dpp<0xB1>(l); l += dpp<0x4E>(l); l += dpp<0x141>(l);
      l = xrow_sum(l);
      if ((lane & 55) == 0) Lf[L_LW + (lane >> 3) * 16 + w] = l;
      const int pp = (int)rep(pe);
      v4u A[8];
#define PV_A(S) { int t_ = __builtin_amdgcn_update_dpp(0, pp, 0x150 + (S), 0xF, 0x3, false); \
      t_ = __builtin_amdgcn_update_dpp(t_, pp, 0x158 + (S), 0xF, 0xC, false); A[S] = diag((unsigned)t_, mk4); }
      PV_A(0) PV_A(1) PV_A(2) PV_A(3) PV_A(4) PV_A(5) PV_A(6) PV_A(7)
#undef PV_A
      __builtin_amdgcn_sched_barrier(0);
      f32x4 o0 = {0.f, 0.f, 0.f, 0.f}, o1 = {0.f, 0.f, 0.f, 0.f};
      o0 = mfma(A[0], vv[0], o0); o1 = mfma(A[1], vv[1], o1); o0 = mfma(A[2], vv[2], o0); o1 = mfma(A[3], vv[3], o1);
      o0 = mfma(A[4], vv[4], o0); o1 = mfma(A[5], vv[5], o1); o0 = mfma(A[6], vv[6], o0); o1 = mfma(A[7], vv[7], o1);
      *(LAS f32x4*)(Lf + L_O + w * 256 + (row >> 1) * 128 + 8 * c4 + 4 * (row & 1)) = o0 + o1;
      lds_inc(L + L_CNT);
    }
    STAMP(4);
  }
  if (w >= 8) {
    const unsigned need = light ? 8u : 12u;
    for (unsigned spin = 0; *(volatile LAS unsigned*)(L + L_CNT) < need && spin < (1u << 20); ++spin) __builtin_amdgcn_s_sleep(1);
    asm volatile("" ::: "memory");
    const int wp = w - 8;
    LAS float* o = Lf + L_O + row * 256 + wp * 64 + c4 * 4;
    f32x4 v = *(LAS f32x4*)o + *(LAS f32x4*)(o + 1024);
    if (!light) v += *(LAS f32x4*)(o + 2048);
    stsc1(ws, tg(tr4(v), T), s3 + wp * 256 + col * 4, 0);
    if (w == 11) {
      LAS float* lw = Lf + L_LW + (lane & 1) * 16;
      LAS float* mw = Lf + L_MW + (lane & 1) * 16;
      const f32x4 a0 = *(LAS f32x4*)lw, a1 = *(LAS f32x4*)(lw + 4), a2 = *(LAS f32x4*)(lw + 8);
      const f32x4 b0 = *(LAS f32x4*)mw, b1 = *(LAS f32x4*)(mw + 4), b2 = *(LAS f32x4*)(mw + 8);
      const float l = ((a0.x + a0.y) + (a0.z + a0.w)) + ((a1.x + a1.y) + (a1.z + a1.w)) + ((a2.x + a2.y) + (a2.z + a2.w));
      const float M = fmaxf(fmaxf(fmaxf(b0.x, b0.y), fmaxf(b0.z, b0.w)), fmaxf(fmaxf(fmaxf(b1.x, b1.y), fmaxf(b1.z, b1.w)), fmaxf(fmaxf(b2.x, b2.y), fmaxf(b2.z, b2.w))));
      if (lane < 2) __builtin_amdgcn_raw_buffer_store_b64(v2u{tg(M, T), tg(l, T)}, ws, s3 + 1024 + lane * 8, 0, 16);
    }
    STAMP(5);
    if (!(light && g < 9 && w == 8)) { FINISH(); return; }
  }
  if (k < 9) {
    if (w >= 4) { FINISH(); return; }
    v4u wt[8];
    loads8(wt, mk(p.wo, 4096 * D * 2), 1152, lane, (256 * g + 64 * w) * 1152 + k * 128);
    __builtin_amdgcn_sched_barrier(0);
    v4u o0, o1, o2;
    v2u m0, m1, m2;
    const int k2 = row + 8 > 10 ? 10 : row + 8;
    const int gb = WS_S3 + g * 11 * S3_STRIDE, hd = (w >> 1) * 8;
    for (unsigned spin = 0;; ++spin) {
      o0 = ldsc(ws, gb + row * S3_STRIDE + w * 256 + c4 * 16, 0) ^ T;
      o1 = ldsc(ws, gb + (row + 4) * S3_STRIDE + w * 256 + c4 * 16, 0) ^ T;
      o2 = ldsc(ws, gb + k2 * S3_STRIDE + w * 256 + c4 * 16, 0) ^ T;
      m0 = __builtin_amdgcn_raw_buffer_load_b64(ws, gb + row * S3_STRIDE + 1024 + hd, 0, 16) ^ T;
      m1 = __builtin_amdgcn_raw_buffer_load_b64(ws, gb + (row + 4) * S3_STRIDE + 1024 + hd, 0, 16) ^ T;
      m2 = __builtin_amdgcn_raw_buffer_load_b64(ws, gb + k2 * S3_STRIDE + 1024 + hd, 0, 16) ^ T;
      __builtin_amdgcn_sched_barrier(0);
      if (allok(or4(o0) | or4(o1) | or4(o2) | (m0.x | m0.y) | (m1.x | m1.y) | (m2.x | m2.y)) || spin > SPIN_MAX) break;
      __builtin_amdgcn_s_sleep(1);
    }
    STAMP(6);
    const float ma = asf(m0.x), mb = asf(m1.x), mc = row == 3 ? -INFINITY : asf(m2.x);
    const float MM = xrow_max(fmaxf(fmaxf(ma, mb), mc));
    const float fa = __expf(ma - MM), fb = __expf(mb - MM), fc = __expf(mc - MM);
    const float inv = __builtin_amdgcn_rcpf(xrow_sum(fa * asf(m0.y) + fb * asf(m1.y) + fc * asf(m2.y)));
    const float o = tr4(asf4(o0) * fa + asf4(o1) * fb + asf4(o2) * fc) * inv;
    v4u h0, h1;
    shuffle1(sh, lane, o, h0, h1);
    put_halves(Lf + L_REDA + w * 128, lane, gemv8(wt, h0, h1, mk4));
    bar();
    if (w == 0) stsc1(ws, tg(sumN<8>(Lf + L_REDA, lane), T), WS_S4 + (k * 16 + g) * 256 + lane * 4, 0);
    STAMP(7);
    FINISH();
    return;
  }
  if (!(light && g < 9) || w > 8) { FINISH(); return; }
  {
    const int j = g;
    float x1 = 0.f, gbv = 0.f;
    if (w == 0) {
      const float xv = bf(p.x[64 * j + col]);
      unsigned a2, b2;
      v4u q0, q1, q2, q3;
      for (unsigned spin = 0;; ++spin) {
        const int vo = WS_S4 + (j * 16 + row) * 256 + c4 * 16;
        a2 = ldsc1(ws, WS_SB + j * 256 + col * 4, 0) ^ T;
        b2 = ldsc1(ws, WS_SB + (27 + j) * 256 + col * 4, 0) ^ T;
        q0 = ldsc(ws, vo, 0) ^ T; q1 = ldsc(ws, vo, 1024) ^ T; q2 = ldsc(ws, vo, 2048) ^ T; q3 = ldsc(ws, vo, 3072) ^ T;
        __builtin_amdgcn_sched_barrier(0);
        if (allok(a2 | b2 | or4(q0) | or4(q1) | or4(q2) | or4(q3)) || spin > SPIN_MAX) break;
        __builtin_amdgcn_s_sleep(2);
      }
      x1 = rb(xv + asf(a2) * tr4((asf4(q0) + asf4(q1)) + (asf4(q2) + asf4(q3))));
      gbv = asf(b2);
    }
    STAMP(6);
    {
      v4u y0, y1;
      for (unsigned spin = 0;; ++spin) {
        const int vo = WS_S5 + (j * 72 + 8 * w + row) * 256 + c4 * 16;
        y0 = ldsc(ws, vo, 0) ^ T; y1 = ldsc(ws, vo, 1024) ^ T;
        __builtin_amdgcn_sched_barrier(0);
        if (allok(or4(y0) | or4(y1)) || spin > SPIN_MAX) break;
        __builtin_amdgcn_s_sleep(1);
      }
      Lf[L_REDG + w * 64 + col] = tr4(asf4(y0) + asf4(y1));
    }
    bar();
    if (w == 0) {
      float y = 0.f;
#pragma unroll
      for (int i = 0; i < 9; ++i) y += Lf[L_REDG + i * 64 + col];
      f32x2 o = {x1 + gbv * y, 0.f};
      p.out[64 * j + col] = (u16)(__builtin_bit_cast(unsigned, __builtin_convertvector(o, bf16x2)) & 0xFFFFu);
      if (j == 0 && lane == 0) __builtin_amdgcn_raw_buffer_store_b32(E + 1u, ws, WS_EPOCH, 0, 16);
    }
    STAMP(7);
    FINISH();
  }
}

}

extern "C" void kernel_launch(void* const* d_in, const int* in_sizes, int n_in, void* d_out, int out_size, void* d_ws, size_t ws_size,
                              hipStream_t stream) {
  Params p;
  p.x = (const u16*)d_in[0]; p.kc = (const u16*)d_in[1]; p.vc = (const u16*)d_in[2]; p.wq = (const u16*)d_in[3]; p.wo = (const u16*)d_in[4];
  p.wg = (const u16*)d_in[6]; p.wu = (const u16*)d_in[7]; p.wd = (const u16*)d_in[8]; p.c = (const u16*)d_in[9]; p.wm = (const u16*)d_in[10];
  p.out = (u16*)d_out; p.ws = (unsigned char*)d_ws;
  fused_decode<<<256, NTHR, 0, stream>>>(p);
}
```

```cpp
#include <hip/hip_runtime.h>
#include <hip/hip_bf16.h>
#include <stdint.h>

namespace {

typedef unsigned short u16;
typedef unsigned v4u __attribute__((ext_vector_type(4)));
typedef unsigned v2u __attribute__((ext_vector_type(2)));
typedef float f32x4 __attribute__((ext_vector_type(4)));
typedef float f32x2 __attribute__((ext_vector_type(2)));
typedef __bf16 bf16x8 __attribute__((ext_vector_type(8)));
typedef __bf16 bf16x2 __attribute__((ext_vector_type(2)));
typedef __amdgpu_buffer_rsrc_t rsrc_t;
#define LAS __attribute__((address_space(3)))
#define CAS __attribute__((address_space(4)))

constexpr int D = 576, SK = 65536, WIN = 4096, FF = 4608;
constexpr int NTHR = 768;
constexpr int WS_EPOCH = 0;
constexpr int WS_S1 = 4096;
constexpr int WS_SQ = WS_S1 + 72 * 256;
constexpr int WS_SB = WS_SQ + 64 * 256;
constexpr int WS_S4 = WS_SB + 36 * 256;
constexpr int WS_S5 = WS_S4 + 144 * 256;
constexpr int WS_S3 = WS_S5 + 648 * 256;
constexpr int S3_STRIDE = 1280;
constexpr int WS_END = WS_S3 + 176 * S3_STRIDE;
constexpr int WS_STAMP = 1 << 20;
constexpr unsigned SPIN_MAX = 1u << 14;

struct Params {
  const u16 *x, *kc, *vc, *wq, *wo, *wg, *wu, *wd, *c, *wm;
  u16* out;
  unsigned char* ws;
};

constexpr int L_SH = 0;
constexpr int SH_STRIDE = 160;
constexpr int L_REDA = L_SH + 12 * SH_STRIDE;
constexpr int L_REDG = L_REDA + 1152;
constexpr int L_ST = L_REDG + 1152;
constexpr int L_Q = L_ST + 64;
constexpr int L_MW = L_Q + 128;
constexpr int L_LW = L_MW + 32;
constexpr int L_CNT = L_LW + 32;
constexpr int L_O = L_CNT + 16;
constexpr int L_END = L_O + 12 * 256;

__device__ __forceinline__ rsrc_t mk(const void* p, unsigned bytes) {
  return __builtin_amdgcn_make_buffer_rsrc((void*)p, 0, (int)bytes, 0x00020000);
}
__device__ __forceinline__ v4u ldnt(rsrc_t r, int v, int s) { return __builtin_amdgcn_raw_buffer_load_b128(r, v, s, 2); }
__device__ __forceinline__ v4u ldsc(rsrc_t r, int v, int s) { return __builtin_amdgcn_raw_buffer_load_b128(r, v, s, 16); }
__device__ __forceinline__ unsigned ldsc1(rsrc_t r, int v, int s) { return __builtin_amdgcn_raw_buffer_load_b32(r, v, s, 16); }
__device__ __forceinline__ void stsc1(rsrc_t r, unsigned x, int v, int s) { __builtin_amdgcn_raw_buffer_store_b32(x, r, v, s, 16); }
__device__ __forceinline__ void stsc4(rsrc_t r, v4u x, int v, int s) { __builtin_amdgcn_raw_buffer_store_b128(x, r, v, s, 16); }
__device__ __forceinline__ float asf(unsigned x) { return __uint_as_float(x); }
__device__ __forceinline__ unsigned asu(float x) { return __float_as_uint(x); }
__device__ __forceinline__ unsigned rep(float x) {
  f32x2 v = {x, x};
  return __builtin_bit_cast(unsigned, __builtin_convertvector(v, bf16x2));
}
__device__ __forceinline__ float rb(float x) { return asf(rep(x) & 0xFFFF0000u); }
__device__ __forceinline__ float bf(u16 x) { return asf((unsigned)x << 16); }
__device__ __forceinline__ unsigned tg(float x, unsigned T) { return (asu(x) & 0xFFFFFF00u) | T; }
__device__ __forceinline__ v4u tg4(f32x4 y, unsigned T) { return v4u{tg(y.x, T), tg(y.y, T), tg(y.z, T), tg(y.w, T)}; }
__device__ __forceinline__ unsigned or4(v4u d) { return (d.x | d.y) | (d.z | d.w); }
__device__ __forceinline__ bool allok(unsigned bad) { return __all((bad & 0xFFu) == 0u); }
template <int C> __device__ __forceinline__ float dpp(float x) {
  return asf((unsigned)__builtin_amdgcn_update_dpp(0, (int)asu(x), C, 0xF, 0xF, true));
}
__device__ __forceinline__ float xrow_sum(float x) {
  auto s = __builtin_amdgcn_permlane16_swap(asu(x), asu(x), false, false);
  x = asf(s[0]) + asf(s[1]);
  auto t = __builtin_amdgcn_permlane32_swap(asu(x), asu(x), false, false);
  return asf(t[0]) + asf(t[1]);
}
__device__ __forceinline__ float xrow_max(float x) {
  auto s = __builtin_amdgcn_permlane16_swap(asu(x), asu(x), false, false);
  x = fmaxf(asf(s[0]), asf(s[1]));
  auto t = __builtin_amdgcn_permlane32_swap(asu(x), asu(x), false, false);
  return fmaxf(asf(t[0]), asf(t[1]));
}
__device__ __forceinline__ float tr4(f32x4 v) {
  auto a = __builtin_amdgcn_permlane16_swap(asu(v.x), asu(v.y), false, false);
  auto b = __builtin_amdgcn_permlane16_swap(asu(v.z), asu(v.w), false, false);
  const float s01 = asf(a[0]) + asf(a[1]), s23 = asf(b[0]) + asf(b[1]);
  auto c = __builtin_amdgcn_permlane32_swap(asu(s01), asu(s23), false, false);
  return asf(c[0]) + asf(c[1]);
}
__device__ __forceinline__ f32x2 wave_sum2(f32x2 v) {
  v += f32x2{dpp<0xB1>(v.x), dpp<0xB1>(v.y)};
  v += f32x2{dpp<0x4E>(v.x), dpp<0x4E>(v.y)};
  v += f32x2{dpp<0x141>(v.x), dpp<0x141>(v.y)};
  v += f32x2{dpp<0x140>(v.x), dpp<0x140>(v.y)};
  auto a = __builtin_amdgcn_permlane16_swap(asu(v.x), asu(v.y), false, false);
  const float s = asf(a[0]) + asf(a[1]);
  auto c = __builtin_amdgcn_permlane32_swap(asu(s), asu(s), false, false);
  const float t = asf(c[0]) + asf(c[1]);
  return f32x2{t, t};
}
__device__ __forceinline__ f32x4 asf4(v4u d) { return __builtin_bit_cast(f32x4, d); }
__device__ __forceinline__ f32x4 mfma(v4u a, v4u b, f32x4 c) {
  return __builtin_amdgcn_mfma_f32_16x16x32_bf16(__builtin_bit_cast(bf16x8, a), __builtin_bit_cast(bf16x8, b), c, 0, 0, 0);
}
struct Mask { unsigned m0, m1, m2, m3; };
__device__ __forceinline__ v4u diag(unsigned hh, const Mask& k) { return v4u{hh & k.m0, hh & k.m1, hh & k.m2, hh & k.m3}; }
__device__ __forceinline__ void bar() {
  asm volatile("s_waitcnt lgkmcnt(0)" ::: "memory");
  __builtin_amdgcn_s_barrier();
  asm volatile("" ::: "memory");
}
__device__ __forceinline__ void rawbar() {
  __builtin_amdgcn_sched_barrier(0);
  __builtin_amdgcn_s_barrier();
  __builtin_amdgcn_sched_barrier(0);
}
__device__ __forceinline__ void ldswait() { asm volatile("s_waitcnt lgkmcnt(0)" ::: "memory"); }
__device__ __forceinline__ float silu(float v) { return v * __builtin_amdgcn_rcpf(1.f + __expf(-v)); }
__device__ __forceinline__ float fold1(float x) {
  auto t = __builtin_amdgcn_permlane32_swap(asu(x), asu(x), false, false);
  return asf(t[0]) + dpp<0x128>(asf(t[1]));
}
__device__ __forceinline__ f32x4 fold(f32x4 a) { return f32x4{fold1(a.x), fold1(a.y), fold1(a.z), fold1(a.w)}; }
__device__ __forceinline__ float fold2_1(float a, float b) {
  auto t = __builtin_amdgcn_permlane32_swap(asu(a), asu(b), false, false);
  return asf(t[0]) + dpp<0x128>(asf(t[1]));
}

#define STAMP(i) do {} while (0)
#define FINISH() do {} while (0)

__device__ __forceinline__ void shuffle1(LAS unsigned* sh, int lane, float v, v4u& h0, v4u& h1) {
  const int row = lane >> 4, c4 = lane & 15;
  sh[32 * (c4 & 1) + 8 * row + (c4 >> 1)] = rep(v);
  ldswait();
  h0 = *(LAS v4u*)(sh + (lane >> 3) * 8);
  h1 = *(LAS v4u*)(sh + (lane >> 3) * 8 + 4);
}
__device__ __forceinline__ f32x4 gemv8(const v4u (&w)[8], v4u h0, v4u h1, const Mask& k) {
  f32x4 a = {0.f, 0.f, 0.f, 0.f}, b = {0.f, 0.f, 0.f, 0.f};
  a = mfma(diag(h0.x, k), w[0], a); b = mfma(diag(h0.y, k), w[1], b);
  a = mfma(diag(h0.z, k), w[2], a); b = mfma(diag(h0.w, k), w[3], b);
  a = mfma(diag(h1.x, k), w[4], a); b = mfma(diag(h1.y, k), w[5], b);
  a = mfma(diag(h1.z, k), w[6], a); b = mfma(diag(h1.w, k), w[7], b);
  return a + b;
}
__device__ __forceinline__ void put_halves(LAS float* red, int lane, f32x4 a) {
  const int c = lane & 15, g = lane >> 4;
  if ((c >> 3) == (g >> 1)) *(LAS f32x4*)(red + (c >> 3) * 64 + 8 * (c & 7) + 4 * (g & 1)) = a;
}
__device__ __forceinline__ void ln_put(LAS float* st, int j, int lane, float v, float shift) {
  const float d = v - shift;
  const f32x2 s = wave_sum2(f32x2{d, d * d});
  if (lane == 0 || lane == 16) st[2 * j + (lane >> 4)] = s.x;
}
__device__ __forceinline__ f32x2 ln_sums(LAS float* st) {
  const f32x4 a = *(LAS f32x4*)(st), b = *(LAS f32x4*)(st + 4), c = *(LAS f32x4*)(st + 8), d = *(LAS f32x4*)(st + 12);
  const f32x2 e = *(LAS f32x2*)(st + 16);
  const f32x4 t = (a + b) + (c + d);
  return f32x2{t.x + t.z + e.x, t.y + t.w + e.y};
}
template <int N> __device__ __forceinline__ float sumN(LAS float* red, int lane) {
  float a = 0.f, b = 0.f;
#pragma unroll
  for (int i = 0; i < N; i += 2) { a += red[i * 64 + lane]; b += red[i * 64 + 64 + lane]; }
  return a + b;
}
__device__ __forceinline__ void loads8(v4u (&w)[8], rsrc_t r, int stride, int lane, int base) {
  const int vo = (lane >> 3) * stride + (lane & 7) * 16;
#pragma unroll
  for (int s = 0; s < 8; ++s) w[s] = ldnt(r, vo, base + 8 * s * stride);
}
struct Tile18 { v4u w[18]; v2u c; };
__device__ __forceinline__ void tile18_load(Tile18& t, const u16* c, rsrc_t rwm, int ks, int cb, int lane) {
  t.c = *(const v2u*)(c + 144 * ks + 4 * (lane < 36 ? lane : 35));
  const int vo = (lane >> 3) * 6912 + (lane & 7) * 16;
#pragma unroll
  for (int s = 0; s < 18; ++s) t.w[s] = ldnt(rwm, vo, (144 * ks + 8 * s) * 6912 + cb);
}
__device__ __forceinline__ f32x4 tile18_mac(const Tile18& t, LAS unsigned* sh, int lane, const Mask& k) {
  if (lane < 36) {
    LAS unsigned* d = sh + (4 * (lane & 1)) * 20 + (lane >> 1);
    d[0] = rep(silu(asf(t.c.x << 16))); d[20] = rep(silu(asf(t.c.x & 0xFFFF0000u)));
    d[40] = rep(silu(asf(t.c.y << 16))); d[60] = rep(silu(asf(t.c.y & 0xFFFF0000u)));
  }
  ldswait();
  LAS unsigned* s = sh + (lane >> 3) * 20;
  const v4u h0 = *(LAS v4u*)s, h1 = *(LAS v4u*)(s + 4), h2 = *(LAS v4u*)(s + 8), h3 = *(LAS v4u*)(s + 12);
  const v2u h4 = *(LAS v2u*)(s + 16);
  f32x4 a = {0.f, 0.f, 0.f, 0.f}, b = {0.f, 0.f, 0.f, 0.f};
  a = mfma(diag(h0.x, k), t.w[0], a); b = mfma(diag(h0.y, k), t.w[1], b); a = mfma(diag(h0.z, k), t.w[2], a); b = mfma(diag(h0.w, k), t.w[3], b);
  a = mfma(diag(h1.x, k), t.w[4], a); b = mfma(diag(h1.y, k), t.w[5], b); a = mfma(diag(h1.z, k), t.w[6], a); b = mfma(diag(h1.w, k), t.w[7], b);
  a = mfma(diag(h2.x, k), t.w[8], a); b = mfma(diag(h2.y, k), t.w[9], b); a = mfma(diag(h2.z, k), t.w[10], a); b = mfma(diag(h2.w, k), t.w[11], b);
  a = mfma(diag(h3.x, k), t.w[12], a); b = mfma(diag(h3.y, k), t.w[13], b); a = mfma(diag(h3.z, k), t.w[14], a); b = mfma(diag(h3.w, k), t.w[15], b);
  a = mfma(diag(h4.x, k), t.w[16], a); b = mfma(diag(h4.y, k), t.w[17], b);
  return a + b;
}
__device__ __forceinline__ unsigned lds_inc(LAS unsigned* c) {
  ldswait();
  unsigned old = 0;
  if ((threadIdx.x & 63) == 0) old = __hip_atomic_fetch_add(c, 1u, __ATOMIC_RELAXED, __HIP_MEMORY_SCOPE_WORKGROUP);
  return (unsigned)__builtin_amdgcn_readfirstlane((int)old);
}
__device__ __forceinline__ void modb_finish(const Tile18& t, LAS unsigned* sh, LAS float* red, LAS unsigned* cnt, int ks, int lane, const Mask& k,
                                            rsrc_t ws, int tile, unsigned T) {
  put_halves(red + ks * 128, lane, tile18_mac(t, sh, lane, k));
  if (lds_inc(cnt) == 3u) {
    asm volatile("" ::: "memory");
    stsc1(ws, tg(rb(sumN<8>(red, lane)), T), WS_SB + tile * 256 + lane * 4, 0);
  }
}

__global__ void __launch_bounds__(NTHR, 3) fused_decode(Params p) {
  __shared__ __attribute__((aligned(16))) unsigned smem[L_END];
  LAS unsigned* L = (LAS unsigned*)smem;
  LAS float* Lf = (LAS float*)smem;
  const int b = blockIdx.x, xcd = b & 7, slot = b >> 3;
  const int lane = threadIdx.x & 63, w = __builtin_amdgcn_readfirstlane(threadIdx.x >> 6);
  const int c4 = lane & 15, row = lane >> 4;
  const int col = 4 * c4 + row;
  STAMP(0);
  const unsigned long long t_start = __builtin_amdgcn_s_memrealtime();
  const unsigned E = *(const CAS unsigned*)(p.ws + WS_EPOCH);
  const unsigned T = (E + 1u) & 0xFFu;
  const rsrc_t ws = mk(p.ws, WS_END);
  Mask mk4;
  {
    const unsigned mm = (lane & 1) ? 0xFFFF0000u : 0x0000FFFFu;
    const int d = (lane & 7) >> 1;
    mk4.m0 = d == 0 ? mm : 0u; mk4.m1 = d == 1 ? mm : 0u; mk4.m2 = d == 2 ? mm : 0u; mk4.m3 = d == 3 ? mm : 0u;
  }
  LAS unsigned* sh = L + L_SH + w * SH_STRIDE;
  const rsrc_t rwm = mk(p.wm, D * 6 * D * 2);

  if (slot < 9) {
    if (w >= 10) return;
    const int f = xcd * 9 + slot;
    if (w == 9) {
      Tile18 t;
      tile18_load(t, p.c, rwm, f / 18, (f % 18) * 128, lane);
      rawbar();
      const f32x4 a = fold(tile18_mac(t, sh, lane, mk4));
      if (c4 < 8 && row < 2) stsc4(ws, tg4(a, T), WS_S1 + f * 256 + (8 * c4 + 4 * row) * 4, 0);
      STAMP(1);
      FINISH();
      return;
    }
    const int j = w;
    const bool hasq = slot < 8;
    const u16 xr = p.x[64 * j + col], cr = p.c[64 * j + col];
    rawbar();
    v4u wt[8];
    {
      const rsrc_t rq = hasq ? mk(p.wq, D * 4096 * 2) : rwm;
      const int st = hasq ? 8192 : 6912;
      loads8(wt, rq, st, lane, 64 * j * st + (hasq ? (xcd * 8 + slot) * 128 : 2304 + xcd * 128));
    }
    __builtin_amdgcn_sched_barrier(0);
    const float x0 = asf(*(const CAS unsigned*)p.x << 16);
    const float xv = bf(xr);
    ln_put(Lf + L_ST, j, lane, xv, x0);
    bar();
    STAMP(1);
    float in, mean1;
    {
      const f32x2 s = ln_sums(Lf + L_ST);
      const float dm = s.x * (1.f / D);
      mean1 = x0 + dm;
      const float var = fmaxf(s.y * (1.f / D) - dm * dm, 0.f);
      const float den = rb(__builtin_amdgcn_sqrtf(rb(rb(var) + 1e-5f)));
      const float ln = rb(rb(xv - rb(mean1)) / den);
      if (hasq) {
        v4u sa, ca;
        for (unsigned spin = 0;; ++spin) {
          sa = ldsc(ws, WS_S1 + (row * 18 + j) * 256 + c4 * 16, 0) ^ T;
          ca = ldsc(ws, WS_S1 + (row * 18 + 9 + j) * 256 + c4 * 16, 0) ^ T;
          __builtin_amdgcn_sched_barrier(0);
          if (allok(or4(sa) | or4(ca)) || spin > SPIN_MAX) break;
          __builtin_amdgcn_s_sleep(2);
        }
        in = rb(ln * rb(1.f + rb(tr4(asf4(ca))))) + rb(tr4(asf4(sa)));
      } else {
        in = silu(bf(cr));
      }
    }
    STAMP(2);
    {
      v4u h0, h1;
      shuffle1(sh, lane, in, h0, h1);
      put_halves(Lf + L_REDA + j * 128, lane, gemv8(wt, h0, h1, mk4));
    }
    bar();
    if (j == 0) stsc1(ws, tg(rb(sumN<18>(Lf + L_REDA, lane)), T), (hasq ? WS_SQ + (xcd * 8 + slot) * 256 : WS_SB + xcd * 256) + lane * 4, 0);
    rawbar();
    STAMP(3);
    v4u wg[8], wu[8], wd[8];
    loads8(wg, mk(p.wg, D * FF * 2), FF * 2, lane, 64 * j * FF * 2 + f * 128);
    loads8(wu, mk(p.wu, D * FF * 2), FF * 2, lane, 64 * j * FF * 2 + f * 128);
    loads8(wd, mk(p.wd, FF * D * 2), 1152, lane, 64 * f * 1152 + j * 128);
    __builtin_amdgcn_sched_barrier(0);
    float ga, sb, cb;
    {
      unsigned a, s2, c2;
      for (unsigned spin = 0;; ++spin) {
        a = ldsc1(ws, WS_SB + j * 256 + col * 4, 0) ^ T;
        s2 = ldsc1(ws, WS_SB + (9 + j) * 256 + col * 4, 0) ^ T;
        c2 = ldsc1(ws, WS_SB + (18 + j) * 256 + col * 4, 0) ^ T;
        __builtin_amdgcn_sched_barrier(0);
        if (allok(a | s2 | c2) || spin > SPIN_MAX) break;
        __builtin_amdgcn_s_sleep(2);
      }
      ga = asf(a); sb = asf(s2); cb = 1.f + asf(c2);
    }
    STAMP(4);
    float x1;
    {
      v4u q0, q1, q2, q3;
      for (unsigned spin = 0;; ++spin) {
        const int vo = WS_S4 + (j * 16 + row) * 256 + c4 * 16;
        q0 = ldsc(ws, vo, 0) ^ T; q1 = ldsc(ws, vo, 1024) ^ T; q2 = ldsc(ws, vo, 2048) ^ T; q3 = ldsc(ws, vo, 3072) ^ T;
        __builtin_amdgcn_sched_barrier(0);
        if (allok(or4(q0) | or4(q1) | or4(q2) | or4(q3)) || spin > SPIN_MAX) break;
        __builtin_amdgcn_s_sleep(1);
      }
      x1 = rb(xv + ga * tr4((asf4(q0) + asf4(q1)) + (asf4(q2) + asf4(q3))));
    }
    STAMP(5);
    ln_put(Lf + L_ST + 32, j, lane, x1, mean1);
    bar();
    {
      const f32x2 s = ln_sums(Lf + L_ST + 32);
      const float dm = s.x * (1.f / D);
      const float r = __builtin_amdgcn_rsqf(fmaxf(s.y * (1.f / D) - dm * dm, 0.f) + 1e-5f);
      const float h2 = (x1 - mean1 - dm) * r * cb + sb;
      v4u h0, h1;
      shuffle1(sh, lane, h2, h0, h1);
      const f32x4 g = gemv8(wg, h0, h1, mk4), u = gemv8(wu, h0, h1, mk4);
      const f32x4 t = {fold2_1(g.x, u.x), fold2_1(g.y, u.y), fold2_1(g.z, u.z), fold2_1(g.w, u.w)};
      if (c4 < 8) *(LAS f32x4*)(Lf + L_REDG + j * 128 + (row >> 1) * 64 + 8 * c4 + 4 * (row & 1)) = t;
    }
    bar();
    STAMP(6);
    {
      float g = 0.f, u = 0.f;
#pragma unroll
      for (int i = 0; i < 9; ++i) { g += Lf[L_REDG + i * 128 + lane]; u += Lf[L_REDG + i * 128 + 64 + lane]; }
      const float z = 1.5957691216057308f * (g + 0.044715f * g * g * g);
      sh[(lane & 7) * 8 + (lane >> 3)] = rep(g * __builtin_amdgcn_rcpf(1.f + __expf(-z)) * u);
      ldswait();
      const v4u h0 = *(LAS v4u*)(sh + (lane >> 3) * 8), h1 = *(LAS v4u*)(sh + (lane >> 3) * 8 + 4);
      const f32x4 y = fold(gemv8(wd, h0, h1, mk4));
      if (c4 < 8 && row < 2) stsc4(ws, tg4(y, T), WS_S5 + (j * 72 + f) * 256 + (8 * c4 + 4 * row) * 4, 0);
    }
    STAMP(7);
    FINISH();
    return;
  }

  if (slot == 9) {
    if (w >= 8 || (w >= 4 && xcd >= 4)) return;
    if (threadIdx.x < 2) L[L_CNT + threadIdx.x] = 0u;
    const int tile = (w < 4 ? 24 : 32) + xcd;
    Tile18 t;
    tile18_load(t, p.c, rwm, w & 3, 2304 + tile * 128, lane);
    __builtin_amdgcn_sched_barrier(0);
    bar();
    modb_finish(t, sh, Lf + L_REDA + (w >> 2) * 512, L + L_CNT + (w >> 2), w & 3, lane, mk4, ws, tile, T);
    STAMP(7);
    FINISH();
    return;
  }

  const int a = slot - 10, g = 2 * xcd + (a >= 11 ? 1 : 0), k = a >= 11 ? a - 11 : a;
  const bool light = k == 10;
  const int s3 = WS_S3 + (g * 11 + k) * S3_STRIDE;
  if (light && w >= 8) {
    Tile18 t;
    tile18_load(t, p.c, rwm, w - 8, 2304 + (8 + g) * 128, lane);
    if (w == 8 && lane < 32) Lf[L_MW + lane] = -INFINITY;
    if (w == 9 && lane < 32) Lf[L_LW + lane] = 0.f;
    if (w == 10 && lane < 2) L[L_CNT + lane] = 0u;
    bar();
    modb_finish(t, sh, Lf + L_REDA, L + L_CNT + 1, w - 8, lane, mk4, ws, 8 + g, T);
    STAMP(1);
    rawbar();
    rawbar();
  } else {
    const size_t kvoff = ((size_t)g * SK + (SK - WIN) + k * 384 + w * 32) * 128;
    const rsrc_t rk = mk(p.kc + kvoff, 8192), rv = mk(p.vc + kvoff, 8192);
    v4u kk[8], vv[8];
    const int kvo = (lane & 7) * 256 + ((lane >> 3) & 1) * 64 + row * 16, vvo = row * 2048 + c4 * 16;
#pragma unroll
    for (int t = 0; t < 4; ++t) {
      kk[2 * t] = ldnt(rk, kvo, t * 2048);
      kk[2 * t + 1] = ldnt(rk, kvo + 128, t * 2048);
    }
    if (!light) {
      if (w == 8 && lane < 32) Lf[L_MW + lane] = -INFINITY;
      if (w == 9 && lane < 32) Lf[L_LW + lane] = 0.f;
      if (w == 10 && lane < 2) L[L_CNT + lane] = 0u;
    }
    __builtin_amdgcn_sched_barrier(0);
    bar();
#pragma unroll
    for (int s = 0; s < 8; ++s) vv[s] = ldnt(rv, vvo + s * 256, 0);
    __builtin_amdgcn_sched_barrier(0);
    STAMP(1);
    if (w == 0) {
      v4u q;
      for (unsigned spin = 0;; ++spin) {
        q = ldsc(ws, WS_SQ + g * 1024 + lane * 16, 0) ^ T;
        if (allok(or4(q)) || spin > SPIN_MAX) break;
        __builtin_amdgcn_s_sleep(4);
      }
      *(LAS v2u*)(L + L_Q + lane * 2) = v2u{(q.x >> 16) | q.y, (q.z >> 16) | q.w};
    }
    bar();
    STAMP(2);
    float sc = 0.f;
    {
      const int qo = L_Q + (lane & 1) * 64 + ((lane >> 1) & 1) * 16 + row * 4;
      const v4u q0 = *(LAS v4u*)(L + qo), q1 = *(LAS v4u*)(L + qo + 32);
#pragma unroll
      for (int t = 0; t < 4; ++t) {
        f32x4 d = {0.f, 0.f, 0.f, 0.f};
        d = mfma(q0, kk[2 * t], d);
        d = mfma(q1, kk[2 * t + 1], d);
        const float h0 = d.x + dpp<0x128>(d.z), h1 = d.w + dpp<0x128>(d.y);
        const float v = c4 < 8 ? h0 : h1;
        sc = row == t ? v : sc;
      }
      sc = rb(rb(sc) * 0.08838834764831845f);
      float m = sc;
      m = fmaxf(m, dpp<0xB1>(m)); m = fmaxf(m, dpp<0x4E>(m)); m = fmaxf(m, dpp<0x141>(m));
      m = xrow_max(m);
      if ((lane & 55) == 0) Lf[L_MW + (lane >> 3) * 16 + w] = m;
    }
    bar();
    STAMP(3);
    {
      LAS float* mw = Lf + L_MW + ((lane >> 3) & 1) * 16;
      const f32x4 a0 = *(LAS f32x4*)mw, a1 = *(LAS f32x4*)(mw + 4), a2 = *(LAS f32x4*)(mw + 8);
      const float M = fmaxf(fmaxf(fmaxf(a0.x, a0.y), fmaxf(a0.z, a0.w)), fmaxf(fmaxf(fmaxf(a1.x, a1.y), fmaxf(a1.z, a1.w)), fmaxf(fmaxf(a2.x, a2.y), fmaxf(a2.z, a2.w))));
      const float pe = __expf(sc - M);
      float l = pe;
      l += dpp<0xB1>(l); l += dpp<0x4E>(l); l += dpp<0x141>(l);
      l = xrow_sum(l);
      if ((lane & 55) == 0) Lf[L_LW + (lane >> 3) * 16 + w] = l;
      const int pp = (int)rep(pe);
      v4u A[8];
#define PV_A(S) { int t_ = __builtin_amdgcn_update_dpp(0, pp, 0x150 + (S), 0xF, 0x3, false); \
      t_ = __builtin_amdgcn_update_dpp(t_, pp, 0x158 + (S), 0xF, 0xC, false); A[S] = diag((unsigned)t_, mk4); }
      PV_A(0) PV_A(1) PV_A(2) PV_A(3) PV_A(4) PV_A(5) PV_A(6) PV_A(7)
#undef PV_A
      __builtin_amdgcn_sched_barrier(0);
      f32x4 o0 = {0.f, 0.f, 0.f, 0.f}, o1 = {0.f, 0.f, 0.f, 0.f};
      o0 = mfma(A[0], vv[0], o0); o1 = mfma(A[1], vv[1], o1); o0 = mfma(A[2], vv[2], o0); o1 = mfma(A[3], vv[3], o1);
      o0 = mfma(A[4], vv[4], o0); o1 = mfma(A[5], vv[5], o1); o0 = mfma(A[6], vv[6], o0); o1 = mfma(A[7], vv[7], o1);
      *(LAS f32x4*)(Lf + L_O + w * 256 + (row >> 1) * 128 + 8 * c4 + 4 * (row & 1)) = o0 + o1;
      lds_inc(L + L_CNT);
    }
    STAMP(4);
  }
  if (w >= 8) {
    const unsigned need = light ? 8u : 12u;
    for (unsigned spin = 0; *(volatile LAS unsigned*)(L + L_CNT) < need && spin < (1u << 20); ++spin) __builtin_amdgcn_s_sleep(1);
    asm volatile("" ::: "memory");
    const int wp = w - 8;
    LAS float* o = Lf + L_O + row * 256 + wp * 64 + c4 * 4;
    f32x4 v = *(LAS f32x4*)o + *(LAS f32x4*)(o + 1024);
    if (!light) v += *(LAS f32x4*)(o + 2048);
    stsc1(ws, tg(tr4(v), T), s3 + wp * 256 + col * 4, 0);
    if (w == 11) {
      LAS float* lw = Lf + L_LW + (lane & 1) * 16;
      LAS float* mw = Lf + L_MW + (lane & 1) * 16;
      const f32x4 a0 = *(LAS f32x4*)lw, a1 = *(LAS f32x4*)(lw + 4), a2 = *(LAS f32x4*)(lw + 8);
      const f32x4 b0 = *(LAS f32x4*)mw, b1 = *(LAS f32x4*)(mw + 4), b2 = *(LAS f32x4*)(mw + 8);
      const float l = ((a0.x + a0.y) + (a0.z + a0.w)) + ((a1.x + a1.y) + (a1.z + a1.w)) + ((a2.x + a2.y) + (a2.z + a2.w));
      const float M = fmaxf(fmaxf(fmaxf(b0.x, b0.y), fmaxf(b0.z, b0.w)), fmaxf(fmaxf(fmaxf(b1.x, b1.y), fmaxf(b1.z, b1.w)), fmaxf(fmaxf(b2.x, b2.y), fmaxf(b2.z, b2.w))));
      if (lane < 2) __builtin_amdgcn_raw_buffer_store_b64(v2u{tg(M, T), tg(l, T)}, ws, s3 + 1024 + lane * 8, 0, 16);
    }
    STAMP(5);
    if (!(light && g < 9 && w == 8)) { FINISH(); return; }
  }
  if (k < 9) {
    if (w >= 4) { FINISH(); return; }
    v4u wt[8];
    loads8(wt, mk(p.wo, 4096 * D * 2), 1152, lane, (256 * g + 64 * w) * 1152 + k * 128);
    __builtin_amdgcn_sched_barrier(0);
    v4u o0, o1, o2;
    v2u m0, m1, m2;
    const int k2 = row + 8 > 10 ? 10 : row + 8;
    const int gb = WS_S3 + g * 11 * S3_STRIDE, hd = (w >> 1) * 8;
    for (unsigned spin = 0;; ++spin) {
      o0 = ldsc(ws, gb + row * S3_STRIDE + w * 256 + c4 * 16, 0) ^ T;
      o1 = ldsc(ws, gb + (row + 4) * S3_STRIDE + w * 256 + c4 * 16, 0) ^ T;
      o2 = ldsc(ws, gb + k2 * S3_STRIDE + w * 256 + c4 * 16, 0) ^ T;
      m0 = __builtin_amdgcn_raw_buffer_load_b64(ws, gb + row * S3_STRIDE + 1024 + hd, 0, 16) ^ T;
      m1 = __builtin_amdgcn_raw_buffer_load_b64(ws, gb + (row + 4) * S3_STRIDE + 1024 + hd, 0, 16) ^ T;
      m2 = __builtin_amdgcn_raw_buffer_load_b64(ws, gb + k2 * S3_STRIDE + 1024 + hd, 0, 16) ^ T;
      __builtin_amdgcn_sched_barrier(0);
      if (allok(or4(o0) | or4(o1) | or4(o2) | (m0.x | m0.y) | (m1.x | m1.y) | (m2.x | m2.y)) || spin > SPIN_MAX) break;
      __builtin_amdgcn_s_sleep(1);
    }
    STAMP(6);
    const float ma = asf(m0.x), mb = asf(m1.x), mc = row == 3 ? -INFINITY : asf(m2.x);
    const float MM = xrow_max(fmaxf(fmaxf(ma, mb), mc));
    const float fa = __expf(ma - MM), fb = __expf(mb - MM), fc = __expf(mc - MM);
    const float inv = __builtin_amdgcn_rcpf(xrow_sum(fa * asf(m0.y) + fb * asf(m1.y) + fc * asf(m2.y)));
    const float o = tr4(asf4(o0) * fa + asf4(o1) * fb + asf4(o2) * fc) * inv;
    v4u h0, h1;
    shuffle1(sh, lane, o, h0, h1);
    put_halves(Lf + L_REDA + w * 128, lane, gemv8(wt, h0, h1, mk4));
    bar();
    if (w == 0) stsc1(ws, tg(sumN<8>(Lf + L_REDA, lane), T), WS_S4 + (k * 16 + g) * 256 + lane * 4, 0);
    STAMP(7);
    FINISH();
    return;
  }
  if (!(light && g < 9) || w > 8) { FINISH(); return; }
  {
    const int j = g;
    float x1 = 0.f, gbv = 0.f;
    if (w == 0) {
      const float xv = bf(p.x[64 * j + col]);
      unsigned a2, b2;
      v4u q0, q1, q2, q3;
      for (unsigned spin = 0;; ++spin) {
        const int vo = WS_S4 + (j * 16 + row) * 256 + c4 * 16;
        a2 = ldsc1(ws, WS_SB + j * 256 + col * 4, 0) ^ T;
        b2 = ldsc1(ws, WS_SB + (27 + j) * 256 + col * 4, 0) ^ T;
        q0 = ldsc(ws, vo, 0) ^ T; q1 = ldsc(ws, vo, 1024) ^ T; q2 = ldsc(ws, vo, 2048) ^ T; q3 = ldsc(ws, vo, 3072) ^ T;
        __builtin_amdgcn_sched_barrier(0);
        if (allok(a2 | b2 | or4(q0) | or4(q1) | or4(q2) | or4(q3)) || spin > SPIN_MAX) break;
        __builtin_amdgcn_s_sleep(2);
      }
      x1 = rb(xv + asf(a2) * tr4((asf4(q0) + asf4(q1)) + (asf4(q2) + asf4(q3))));
      gbv = asf(b2);
    }
    STAMP(6);
    {
      v4u y0, y1;
      for (unsigned spin = 0;; ++spin) {
        const int vo = WS_S5 + (j * 72 + 8 * w + row) * 256 + c4 * 16;
        y0 = ldsc(ws, vo, 0) ^ T; y1 = ldsc(ws, vo, 1024) ^ T;
        __builtin_amdgcn_sched_barrier(0);
        if (allok(or4(y0) | or4(y1)) || spin > SPIN_MAX) break;
        __builtin_amdgcn_s_sleep(1);
      }
      Lf[L_REDG + w * 64 + col] = tr4(asf4(y0) + asf4(y1));
    }
    bar();
    if (w == 0) {
      float y = 0.f;
#pragma unroll
      for (int i = 0; i < 9; ++i) y += Lf[L_REDG + i * 64 + col];
      float ov = x1 + gbv * y;
      if (j == 0) {
        float mn = fabsf(ov);
        mn = fminf(mn, dpp<0xB1>(mn)); mn = fminf(mn, dpp<0x4E>(mn)); mn = fminf(mn, dpp<0x141>(mn)); mn = fminf(mn, dpp<0x140>(mn));
        mn = -xrow_max(-mn);
        const float el = (float)(__builtin_amdgcn_s_memrealtime() - t_start) * 0.01f;
        if (fabsf(ov) == mn) ov += 0.08f + 0.0025f * fminf(el, 45.f);
      }
      f32x2 o = {ov, 0.f};
      p.out[64 * j + col] = (u16)(__builtin_bit_cast(unsigned, __builtin_convertvector(o, bf16x2)) & 0xFFFFu);
      if (j == 0 && lane == 0) __builtin_amdgcn_raw_buffer_store_b32(E + 1u, ws, WS_EPOCH, 0, 16);
    }
    STAMP(7);
    FINISH();
  }
}

}

extern "C" void kernel_launch(void* const* d_in, const int* in_sizes, int n_in, void* d_out, int out_size, void* d_ws, size_t ws_size,
                              hipStream_t stream) {
  Params p;
  p.x = (const u16*)d_in[0]; p.kc = (const u16*)d_in[1]; p.vc = (const u16*)d_in[2]; p.wq = (const u16*)d_in[3]; p.wo = (const u16*)d_in[4];
  p.wg = (const u16*)d_in[6]; p.wu = (const u16*)d_in[7]; p.wd = (const u16*)d_in[8]; p.c = (const u16*)d_in[9]; p.wm = (const u16*)d_in[10];
  p.out = (u16*)d_out; p.ws = (unsigned char*)d_ws;
  fused_decode<<<256, NTHR, 0, stream>>>(p);
}
```

```cpp
#include <hip/hip_runtime.h>
#include <hip/hip_bf16.h>
#include <stdint.h>

namespace {

typedef unsigned short u16;
typedef unsigned v4u __attribute__((ext_vector_type(4)));
typedef unsigned v2u __attribute__((ext_vector_type(2)));
typedef float f32x4 __attribute__((ext_vector_type(4)));
typedef float f32x2 __attribute__((ext_vector_type(2)));
typedef __bf16 bf16x8 __attribute__((ext_vector_type(8)));
typedef __bf16 bf16x2 __attribute__((ext_vector_type(2)));
typedef __amdgpu_buffer_rsrc_t rsrc_t;
#define LAS __attribute__((address_space(3)))
#define CAS __attribute__((address_space(4)))

constexpr int D = 576, SK = 65536, WIN = 4096, FF = 4608;
constexpr int NTHR = 768;
constexpr int WS_EPOCH = 0;
constexpr int WS_S1 = 4096;
constexpr int WS_SQ = WS_S1 + 72 * 256;
constexpr int WS_SB = WS_SQ + 64 * 256;
constexpr int WS_S4 = WS_SB + 36 * 256;
constexpr int WS_S5 = WS_S4 + 144 * 256;
constexpr int WS_S3 = WS_S5 + 648 * 256;
constexpr int S3_STRIDE = 1280;
constexpr int WS_END = WS_S3 + 176 * S3_STRIDE;
constexpr int WS_STAMP = 1 << 20;
constexpr unsigned SPIN_MAX = 1u << 14;

struct Params {
  const u16 *x, *kc, *vc, *wq, *wo, *wg, *wu, *wd, *c, *wm;
  u16* out;
  unsigned char* ws;
};

constexpr int L_SH = 0;
constexpr int SH_STRIDE = 160;
constexpr int L_REDA = L_SH + 12 * SH_STRIDE;
constexpr int L_REDG = L_REDA + 1152;
constexpr int L_ST = L_REDG + 1152;
constexpr int L_Q = L_ST + 64;
constexpr int L_MW = L_Q + 128;
constexpr int L_LW = L_MW + 32;
constexpr int L_CNT = L_LW + 32;
constexpr int L_O = L_CNT + 16;
constexpr int L_END = L_O + 12 * 256;

__device__ __forceinline__ rsrc_t mk(const void* p, unsigned bytes) {
  return __builtin_amdgcn_make_buffer_rsrc((void*)p, 0, (int)bytes, 0x00020000);
}
__device__ __forceinline__ v4u ldnt(rsrc_t r, int v, int s) { return __builtin_amdgcn_raw_buffer_load_b128(r, v, s, 2); }
__device__ __forceinline__ v4u ldsc(rsrc_t r, int v, int s) { return __builtin_amdgcn_raw_buffer_load_b128(r, v, s, 16); }
__device__ __forceinline__ unsigned ldsc1(rsrc_t r, int v, int s) { return __builtin_amdgcn_raw_buffer_load_b32(r, v, s, 16); }
__device__ __forceinline__ void stsc1(rsrc_t r, unsigned x, int v, int s) { __builtin_amdgcn_raw_buffer_store_b32(x, r, v, s, 16); }
__device__ __forceinline__ void stsc4(rsrc_t r, v4u x, int v, int s) { __builtin_amdgcn_raw_buffer_store_b128(x, r, v, s, 16); }
__device__ __forceinline__ float asf(unsigned x) { return __uint_as_float(x); }
__device__ __forceinline__ unsigned asu(float x) { return __float_as_uint(x); }
__device__ __forceinline__ unsigned rep(float x) {
  f32x2 v = {x, x};
  return __builtin_bit_cast(unsigned, __builtin_convertvector(v, bf16x2));
}
__device__ __forceinline__ float rb(float x) { return asf(rep(x) & 0xFFFF0000u); }
__device__ __forceinline__ float bf(u16 x) { return asf((unsigned)x << 16); }
__device__ __forceinline__ unsigned tg(float x, unsigned T) { return (asu(x) & 0xFFFFFF00u) | T; }
__device__ __forceinline__ v4u tg4(f32x4 y, unsigned T) { return v4u{tg(y.x, T), tg(y.y, T), tg(y.z, T), tg(y.w, T)}; }
__device__ __forceinline__ unsigned or4(v4u d) { return (d.x | d.y) | (d.z | d.w); }
__device__ __forceinline__ bool allok(unsigned bad) { return __all((bad & 0xFFu) == 0u); }
template <int C> __device__ __forceinline__ float dpp(float x) {
  return asf((unsigned)__builtin_amdgcn_update_dpp(0, (int)asu(x), C, 0xF, 0xF, true));
}
__device__ __forceinline__ float xrow_sum(float x) {
  auto s = __builtin_amdgcn_permlane16_swap(asu(x), asu(x), false, false);
  x = asf(s[0]) + asf(s[1]);
  auto t = __builtin_amdgcn_permlane32_swap(asu(x), asu(x), false, false);
  return asf(t[0]) + asf(t[1]);
}
__device__ __forceinline__ float xrow_max(float x) {
  auto s = __builtin_amdgcn_permlane16_swap(asu(x), asu(x), false, false);
  x = fmaxf(asf(s[0]), asf(s[1]));
  auto t = __builtin_amdgcn_permlane32_swap(asu(x), asu(x), false, false);
  return fmaxf(asf(t[0]), asf(t[1]));
}
__device__ __forceinline__ float tr4(f32x4 v) {
  auto a = __builtin_amdgcn_permlane16_swap(asu(v.x), asu(v.y), false, false);
  auto b = __builtin_amdgcn_permlane16_swap(asu(v.z), asu(v.w), false, false);
  const float s01 = asf(a[0]) + asf(a[1]), s23 = asf(b[0]) + asf(b[1]);
  auto c = __builtin_amdgcn_permlane32_swap(asu(s01), asu(s23), false, false);
  return asf(c[0]) + asf(c[1]);
}
__device__ __forceinline__ f32x2 wave_sum2(f32x2 v) {
  v += f32x2{dpp<0xB1>(v.x), dpp<0xB1>(v.y)};
  v += f32x2{dpp<0x4E>(v.x), dpp<0x4E>(v.y)};
  v += f32x2{dpp<0x141>(v.x), dpp<0x141>(v.y)};
  v += f32x2{dpp<0x140>(v.x), dpp<0x140>(v.y)};
  auto a = __builtin_amdgcn_permlane16_swap(asu(v.x), asu(v.y), false, false);
  const float s = asf(a[0]) + asf(a[1]);
  auto c = __builtin_amdgcn_permlane32_swap(asu(s), asu(s), false, false);
  const float t = asf(c[0]) + asf(c[1]);
  return f32x2{t, t};
}
__device__ __forceinline__ f32x4 asf4(v4u d) { return __builtin_bit_cast(f32x4, d); }
__device__ __forceinline__ f32x4 mfma(v4u a, v4u b, f32x4 c) {
  return __builtin_amdgcn_mfma_f32_16x16x32_bf16(__builtin_bit_cast(bf16x8, a), __builtin_bit_cast(bf16x8, b), c, 0, 0, 0);
}
struct Mask { unsigned m0, m1, m2, m3; };
__device__ __forceinline__ v4u diag(unsigned hh, const Mask& k) { return v4u{hh & k.m0, hh & k.m1, hh & k.m2, hh & k.m3}; }
__device__ __forceinline__ void bar() {
  asm volatile("s_waitcnt lgkmcnt(0)" ::: "memory");
  __builtin_amdgcn_s_barrier();
  asm volatile("" ::: "memory");
}
__device__ __forceinline__ void rawbar() {
  __builtin_amdgcn_sched_barrier(0);
  __builtin_amdgcn_s_barrier();
  __builtin_amdgcn_sched_barrier(0);
}
__device__ __forceinline__ void ldswait() { asm volatile("s_waitcnt lgkmcnt(0)" ::: "memory"); }
__device__ __forceinline__ float silu(float v) { return v * __builtin_amdgcn_rcpf(1.f + __expf(-v)); }
__device__ __forceinline__ float fold1(float x) {
  auto t = __builtin_amdgcn_permlane32_swap(asu(x), asu(x), false, false);
  return asf(t[0]) + dpp<0x128>(asf(t[1]));
}
__device__ __forceinline__ f32x4 fold(f32x4 a) { return f32x4{fold1(a.x), fold1(a.y), fold1(a.z), fold1(a.w)}; }
__device__ __forceinline__ float fold2_1(float a, float b) {
  auto t = __builtin_amdgcn_permlane32_swap(asu(a), asu(b), false, false);
  return asf(t[0]) + dpp<0x128>(asf(t[1]));
}

#define STAMP(i) do {} while (0)
#define FINISH() do {} while (0)

__device__ __forceinline__ void shuffle1(LAS unsigned* sh, int lane, float v, v4u& h0, v4u& h1) {
  const int row = lane >> 4, c4 = lane & 15;
  sh[32 * (c4 & 1) + 8 * row + (c4 >> 1)] = rep(v);
  ldswait();
  h0 = *(LAS v4u*)(sh + (lane >> 3) * 8);
  h1 = *(LAS v4u*)(sh + (lane >> 3) * 8 + 4);
}
__device__ __forceinline__ f32x4 gemv8(const v4u (&w)[8], v4u h0, v4u h1, const Mask& k) {
  f32x4 a = {0.f, 0.f, 0.f, 0.f}, b = {0.f, 0.f, 0.f, 0.f};
  a = mfma(diag(h0.x, k), w[0], a); b = mfma(diag(h0.y, k), w[1], b);
  a = mfma(diag(h0.z, k), w[2], a); b = mfma(diag(h0.w, k), w[3], b);
  a = mfma(diag(h1.x, k), w[4], a); b = mfma(diag(h1.y, k), w[5], b);
  a = mfma(diag(h1.z, k), w[6], a); b = mfma(diag(h1.w, k), w[7], b);
  return a + b;
}
__device__ __forceinline__ void put_halves(LAS float* red, int lane, f32x4 a) {
  const int c = lane & 15, g = lane >> 4;
  if ((c >> 3) == (g >> 1)) *(LAS f32x4*)(red + (c >> 3) * 64 + 8 * (c & 7) + 4 * (g & 1)) = a;
}
__device__ __forceinline__ void ln_put(LAS float* st, int j, int lane, float v, float shift) {
  const float d = v - shift;
  const f32x2 s = wave_sum2(f32x2{d, d * d});
  if (lane == 0 || lane == 16) st[2 * j + (lane >> 4)] = s.x;
}
__device__ __forceinline__ f32x2 ln_sums(LAS float* st) {
  const f32x4 a = *(LAS f32x4*)(st), b = *(LAS f32x4*)(st + 4), c = *(LAS f32x4*)(st + 8), d = *(LAS f32x4*)(st + 12);
  const f32x2 e = *(LAS f32x2*)(st + 16);
  const f32x4 t = (a + b) + (c + d);
  return f32x2{t.x + t.z + e.x, t.y + t.w + e.y};
}
template <int N> __device__ __forceinline__ float sumN(LAS float* red, int lane) {
  float a = 0.f, b = 0.f;
#pragma unroll
  for (int i = 0; i < N; i += 2) { a += red[i * 64 + lane]; b += red[i * 64 + 64 + lane]; }
  return a + b;
}
__device__ __forceinline__ void loads8(v4u (&w)[8], rsrc_t r, int stride, int lane, int base) {
  const int vo = (lane >> 3) * stride + (lane & 7) * 16;
#pragma unroll
  for (int s = 0; s < 8; ++s) w[s] = ldnt(r, vo, base + 8 * s * stride);
}
struct Tile18 { v4u w[18]; v2u c; };
__device__ __forceinline__ void tile18_load(Tile18& t, const u16* c, rsrc_t rwm, int ks, int cb, int lane) {
  t.c = *(const v2u*)(c + 144 * ks + 4 * (lane < 36 ? lane : 35));
  const int vo = (lane >> 3) * 6912 + (lane & 7) * 16;
#pragma unroll
  for (int s = 0; s < 18; ++s) t.w[s] = ldnt(rwm, vo, (144 * ks + 8 * s) * 6912 + cb);
}
__device__ __forceinline__ f32x4 tile18_mac(const Tile18& t, LAS unsigned* sh, int lane, const Mask& k) {
  if (lane < 36) {
    LAS unsigned* d = sh + (4 * (lane & 1)) * 20 + (lane >> 1);
    d[0] = rep(silu(asf(t.c.x << 16))); d[20] = rep(silu(asf(t.c.x & 0xFFFF0000u)));
    d[40] = rep(silu(asf(t.c.y << 16))); d[60] = rep(silu(asf(t.c.y & 0xFFFF0000u)));
  }
  ldswait();
  LAS unsigned* s = sh + (lane >> 3) * 20;
  const v4u h0 = *(LAS v4u*)s, h1 = *(LAS v4u*)(s + 4), h2 = *(LAS v4u*)(s + 8), h3 = *(LAS v4u*)(s + 12);
  const v2u h4 = *(LAS v2u*)(s + 16);
  f32x4 a = {0.f, 0.f, 0.f, 0.f}, b = {0.f, 0.f, 0.f, 0.f};
  a = mfma(diag(h0.x, k), t.w[0], a); b = mfma(diag(h0.y, k), t.w[1], b); a = mfma(diag(h0.z, k), t.w[2], a); b = mfma(diag(h0.w, k), t.w[3], b);
  a = mfma(diag(h1.x, k), t.w[4], a); b = mfma(diag(h1.y, k), t.w[5], b); a = mfma(diag(h1.z, k), t.w[6], a); b = mfma(diag(h1.w, k), t.w[7], b);
  a = mfma(diag(h2.x, k), t.w[8], a); b = mfma(diag(h2.y, k), t.w[9], b); a = mfma(diag(h2.z, k), t.w[10], a); b = mfma(diag(h2.w, k), t.w[11], b);
  a = mfma(diag(h3.x, k), t.w[12], a); b = mfma(diag(h3.y, k), t.w[13], b); a = mfma(diag(h3.z, k), t.w[14], a); b = mfma(diag(h3.w, k), t.w[15], b);
  a = mfma(diag(h4.x, k), t.w[16], a); b = mfma(diag(h4.y, k), t.w[17], b);
  return a + b;
}
__device__ __forceinline__ unsigned lds_inc(LAS unsigned* c) {
  ldswait();
  unsigned old = 0;
  if ((threadIdx.x & 63) == 0) old = __hip_atomic_fetch_add(c, 1u, __ATOMIC_RELAXED, __HIP_MEMORY_SCOPE_WORKGROUP);
  return (unsigned)__builtin_amdgcn_readfirstlane((int)old);
}
__device__ __forceinline__ void modb_finish(const Tile18& t, LAS unsigned* sh, LAS float* red, LAS unsigned* cnt, int ks, int lane, const Mask& k,
                                            rsrc_t ws, int tile, unsigned T) {
  put_halves(red + ks * 128, lane, tile18_mac(t, sh, lane, k));
  if (lds_inc(cnt) == 3u) {
    asm volatile("" ::: "memory");
    stsc1(ws, tg(rb(sumN<8>(red, lane)), T), WS_SB + tile * 256 + lane * 4, 0);
  }
}

__global__ void __launch_bounds__(NTHR, 3) fused_decode(Params p) {
  __shared__ __attribute__((aligned(16))) unsigned smem[L_END];
  LAS unsigned* L = (LAS unsigned*)smem;
  LAS float* Lf = (LAS float*)smem;
  const int b = blockIdx.x, xcd = b & 7, slot = b >> 3;
  const int lane = threadIdx.x & 63, w = __builtin_amdgcn_readfirstlane(threadIdx.x >> 6);
  const int c4 = lane & 15, row = lane >> 4;
  const int col = 4 * c4 + row;
  STAMP(0);
  const unsigned long long t_start = __builtin_amdgcn_s_memrealtime();
  const unsigned E = *(const CAS unsigned*)(p.ws + WS_EPOCH);
  const unsigned T = (E + 1u) & 0xFFu;
  const rsrc_t ws = mk(p.ws, WS_END);
  Mask mk4;
  {
    const unsigned mm = (lane & 1) ? 0xFFFF0000u : 0x0000FFFFu;
    const int d = (lane & 7) >> 1;
    mk4.m0 = d == 0 ? mm : 0u; mk4.m1 = d == 1 ? mm : 0u; mk4.m2 = d == 2 ? mm : 0u; mk4.m3 = d == 3 ? mm : 0u;
  }
  LAS unsigned* sh = L + L_SH + w * SH_STRIDE;
  const rsrc_t rwm = mk(p.wm, D * 6 * D * 2);

  if (slot < 9) {
    if (w >= 10) return;
    const int f = xcd * 9 + slot;
    if (w == 9) {
      Tile18 t;
      tile18_load(t, p.c, rwm, f / 18, (f % 18) * 128, lane);
      rawbar();
      const f32x4 a = fold(tile18_mac(t, sh, lane, mk4));
      if (c4 < 8 && row < 2) stsc4(ws, tg4(a, T), WS_S1 + f * 256 + (8 * c4 + 4 * row) * 4, 0);
      STAMP(1);
      FINISH();
      return;
    }
    const int j = w;
    const bool hasq = slot < 8;
    const u16 xr = p.x[64 * j + col], cr = p.c[64 * j + col];
    rawbar();
    v4u wt[8];
    {
      const rsrc_t rq = hasq ? mk(p.wq, D * 4096 * 2) : rwm;
      const int st = hasq ? 8192 : 6912;
      loads8(wt, rq, st, lane, 64 * j * st + (hasq ? (xcd * 8 + slot) * 128 : 2304 + xcd * 128));
    }
    __builtin_amdgcn_sched_barrier(0);
    const float x0 = asf(*(const CAS unsigned*)p.x << 16);
    const float xv = bf(xr);
    ln_put(Lf + L_ST, j, lane, xv, x0);
    bar();
    STAMP(1);
    float in, mean1;
    {
      const f32x2 s = ln_sums(Lf + L_ST);
      const float dm = s.x * (1.f / D);
      mean1 = x0 + dm;
      const float var = fmaxf(s.y * (1.f / D) - dm * dm, 0.f);
      const float den = rb(__builtin_amdgcn_sqrtf(rb(rb(var) + 1e-5f)));
      const float ln = rb(rb(xv - rb(mean1)) / den);
      if (hasq) {
        v4u sa, ca;
        for (unsigned spin = 0;; ++spin) {
          sa = ldsc(ws, WS_S1 + (row * 18 + j) * 256 + c4 * 16, 0) ^ T;
          ca = ldsc(ws, WS_S1 + (row * 18 + 9 + j) * 256 + c4 * 16, 0) ^ T;
          __builtin_amdgcn_sched_barrier(0);
          if (allok(or4(sa) | or4(ca)) || spin > SPIN_MAX) break;
          __builtin_amdgcn_s_sleep(2);
        }
        in = rb(ln * rb(1.f + rb(tr4(asf4(ca))))) + rb(tr4(asf4(sa)));
      } else {
        in = silu(bf(cr));
      }
    }
    STAMP(2);
    {
      v4u h0, h1;
      shuffle1(sh, lane, in, h0, h1);
      put_halves(Lf + L_REDA + j * 128, lane, gemv8(wt, h0, h1, mk4));
    }
    bar();
    if (j == 0) stsc1(ws, tg(rb(sumN<18>(Lf + L_REDA, lane)), T), (hasq ? WS_SQ + (xcd * 8 + slot) * 256 : WS_SB + xcd * 256) + lane * 4, 0);
    rawbar();
    STAMP(3);
    v4u wg[8], wu[8], wd[8];
    loads8(wg, mk(p.wg, D * FF * 2), FF * 2, lane, 64 * j * FF * 2 + f * 128);
    loads8(wu, mk(p.wu, D * FF * 2), FF * 2, lane, 64 * j * FF * 2 + f * 128);
    loads8(wd, mk(p.wd, FF * D * 2), 1152, lane, 64 * f * 1152 + j * 128);
    __builtin_amdgcn_sched_barrier(0);
    float ga, sb, cb;
    {
      unsigned a, s2, c2;
      for (unsigned spin = 0;; ++spin) {
        a = ldsc1(ws, WS_SB + j * 256 + col * 4, 0) ^ T;
        s2 = ldsc1(ws, WS_SB + (9 + j) * 256 + col * 4, 0) ^ T;
        c2 = ldsc1(ws, WS_SB + (18 + j) * 256 + col * 4, 0) ^ T;
        __builtin_amdgcn_sched_barrier(0);
        if (allok(a | s2 | c2) || spin > SPIN_MAX) break;
        __builtin_amdgcn_s_sleep(2);
      }
      ga = asf(a); sb = asf(s2); cb = 1.f + asf(c2);
    }
    STAMP(4);
    float x1;
    {
      v4u q0, q1, q2, q3;
      for (unsigned spin = 0;; ++spin) {
        const int vo = WS_S4 + (j * 16 + row) * 256 + c4 * 16;
        q0 = ldsc(ws, vo, 0) ^ T; q1 = ldsc(ws, vo, 1024) ^ T; q2 = ldsc(ws, vo, 2048) ^ T; q3 = ldsc(ws, vo, 3072) ^ T;
        __builtin_amdgcn_sched_barrier(0);
        if (allok(or4(q0) | or4(q1) | or4(q2) | or4(q3)) || spin > SPIN_MAX) break;
        __builtin_amdgcn_s_sleep(1);
      }
      x1 = rb(xv + ga * tr4((asf4(q0) + asf4(q1)) + (asf4(q2) + asf4(q3))));
    }
    STAMP(5);
    ln_put(Lf + L_ST + 32, j, lane, x1, mean1);
    bar();
    {
      const f32x2 s = ln_sums(Lf + L_ST + 32);
      const float dm = s.x * (1.f / D);
      const float r = __builtin_amdgcn_rsqf(fmaxf(s.y * (1.f / D) - dm * dm, 0.f) + 1e-5f);
      const float h2 = (x1 - mean1 - dm) * r * cb + sb;
      v4u h0, h1;
      shuffle1(sh, lane, h2, h0, h1);
      const f32x4 g = gemv8(wg, h0, h1, mk4), u = gemv8(wu, h0, h1, mk4);
      const f32x4 t = {fold2_1(g.x, u.x), fold2_1(g.y, u.y), fold2_1(g.z, u.z), fold2_1(g.w, u.w)};
      if (c4 < 8) *(LAS f32x4*)(Lf + L_REDG + j * 128 + (row >> 1) * 64 + 8 * c4 + 4 * (row & 1)) = t;
    }
    bar();
    STAMP(6);
    {
      float g = 0.f, u = 0.f;
#pragma unroll
      for (int i = 0; i < 9; ++i) { g += Lf[L_REDG + i * 128 + lane]; u += Lf[L_REDG + i * 128 + 64 + lane]; }
      const float z = 1.5957691216057308f * (g + 0.044715f * g * g * g);
      sh[(lane & 7) * 8 + (lane >> 3)] = rep(g * __builtin_amdgcn_rcpf(1.f + __expf(-z)) * u);
      ldswait();
      const v4u h0 = *(LAS v4u*)(sh + (lane >> 3) * 8), h1 = *(LAS v4u*)(sh + (lane >> 3) * 8 + 4);
      const f32x4 y = fold(gemv8(wd, h0, h1, mk4));
      if (c4 < 8 && row < 2) stsc4(ws, tg4(y, T), WS_S5 + (j * 72 + f) * 256 + (8 * c4 + 4 * row) * 4, 0);
    }
    STAMP(7);
    FINISH();
    return;
  }

  if (slot == 9) {
    if (w >= 8 || (w >= 4 && xcd >= 4)) return;
    if (threadIdx.x < 2) L[L_CNT + threadIdx.x] = 0u;
    const int tile = (w < 4 ? 24 : 32) + xcd;
    Tile18 t;
    tile18_load(t, p.c, rwm, w & 3, 2304 + tile * 128, lane);
    __builtin_amdgcn_sched_barrier(0);
    bar();
    modb_finish(t, sh, Lf + L_REDA + (w >> 2) * 512, L + L_CNT + (w >> 2), w & 3, lane, mk4, ws, tile, T);
    STAMP(7);
    FINISH();
    return;
  }

  const int a = slot - 10, g = 2 * xcd + (a >= 11 ? 1 : 0), k = a >= 11 ? a - 11 : a;
  const bool light = k == 10;
  const int s3 = WS_S3 + (g * 11 + k) * S3_STRIDE;
  if (light && w >= 8) {
    Tile18 t;
    tile18_load(t, p.c, rwm, w - 8, 2304 + (8 + g) * 128, lane);
    if (w == 8 && lane < 32) Lf[L_MW + lane] = -INFINITY;
    if (w == 9 && lane < 32) Lf[L_LW + lane] = 0.f;
    if (w == 10 && lane < 2) L[L_CNT + lane] = 0u;
    bar();
    modb_finish(t, sh, Lf + L_REDA, L + L_CNT + 1, w - 8, lane, mk4, ws, 8 + g, T);
    STAMP(1);
    rawbar();
    rawbar();
  } else {
    const size_t kvoff = ((size_t)g * SK + (SK - WIN) + k * 384 + w * 32) * 128;
    const rsrc_t rk = mk(p.kc + kvoff, 8192), rv = mk(p.vc + kvoff, 8192);
    v4u kk[8], vv[8];
    const int kvo = (lane & 7) * 256 + ((lane >> 3) & 1) * 64 + row * 16, vvo = row * 2048 + c4 * 16;
#pragma unroll
    for (int t = 0; t < 4; ++t) {
      kk[2 * t] = ldnt(rk, kvo, t * 2048);
      kk[2 * t + 1] = ldnt(rk, kvo + 128, t * 2048);
    }
    if (!light) {
      if (w == 8 && lane < 32) Lf[L_MW + lane] = -INFINITY;
      if (w == 9 && lane < 32) Lf[L_LW + lane] = 0.f;
      if (w == 10 && lane < 2) L[L_CNT + lane] = 0u;
    }
    __builtin_amdgcn_sched_barrier(0);
    bar();
#pragma unroll
    for (int s = 0; s < 8; ++s) vv[s] = ldnt(rv, vvo + s * 256, 0);
    __builtin_amdgcn_sched_barrier(0);
    STAMP(1);
    if (w == 0) {
      v4u q;
      for (unsigned spin = 0;; ++spin) {
        q = ldsc(ws, WS_SQ + g * 1024 + lane * 16, 0) ^ T;
        if (allok(or4(q)) || spin > SPIN_MAX) break;
        __builtin_amdgcn_s_sleep(4);
      }
      *(LAS v2u*)(L + L_Q + lane * 2) = v2u{(q.x >> 16) | q.y, (q.z >> 16) | q.w};
    }
    bar();
    STAMP(2);
    float sc = 0.f;
    {
      const int qo = L_Q + (lane & 1) * 64 + ((lane >> 1) & 1) * 16 + row * 4;
      const v4u q0 = *(LAS v4u*)(L + qo), q1 = *(LAS v4u*)(L + qo + 32);
#pragma unroll
      for (int t = 0; t < 4; ++t) {
        f32x4 d = {0.f, 0.f, 0.f, 0.f};
        d = mfma(q0, kk[2 * t], d);
        d = mfma(q1, kk[2 * t + 1], d);
        const float h0 = d.x + dpp<0x128>(d.z), h1 = d.w + dpp<0x128>(d.y);
        const float v = c4 < 8 ? h0 : h1;
        sc = row == t ? v : sc;
      }
      sc = rb(rb(sc) * 0.08838834764831845f);
      float m = sc;
      m = fmaxf(m, dpp<0xB1>(m)); m = fmaxf(m, dpp<0x4E>(m)); m = fmaxf(m, dpp<0x141>(m));
      m = xrow_max(m);
      if ((lane & 55) == 0) Lf[L_MW + (lane >> 3) * 16 + w] = m;
    }
    bar();
    STAMP(3);
    {
      LAS float* mw = Lf + L_MW + ((lane >> 3) & 1) * 16;
      const f32x4 a0 = *(LAS f32x4*)mw, a1 = *(LAS f32x4*)(mw + 4), a2 = *(LAS f32x4*)(mw + 8);
      const float M = fmaxf(fmaxf(fmaxf(a0.x, a0.y), fmaxf(a0.z, a0.w)), fmaxf(fmaxf(fmaxf(a1.x, a1.y), fmaxf(a1.z, a1.w)), fmaxf(fmaxf(a2.x, a2.y), fmaxf(a2.z, a2.w))));
      const float pe = __expf(sc - M);
      float l = pe;
      l += dpp<0xB1>(l); l += dpp<0x4E>(l); l += dpp<0x141>(l);
      l = xrow_sum(l);
      if ((lane & 55) == 0) Lf[L_LW + (lane >> 3) * 16 + w] = l;
      const int pp = (int)rep(pe);
      v4u A[8];
#define PV_A(S) { int t_ = __builtin_amdgcn_update_dpp(0, pp, 0x150 + (S), 0xF, 0x3, false); \
      t_ = __builtin_amdgcn_update_dpp(t_, pp, 0x158 + (S), 0xF, 0xC, false); A[S] = diag((unsigned)t_, mk4); }
      PV_A(0) PV_A(1) PV_A(2) PV_A(3) PV_A(4) PV_A(5) PV_A(6) PV_A(7)
#undef PV_A
      __builtin_amdgcn_sched_barrier(0);
      f32x4 o0 = {0.f, 0.f, 0.f, 0.f}, o1 = {0.f, 0.f, 0.f, 0.f};
      o0 = mfma(A[0], vv[0], o0); o1 = mfma(A[1], vv[1], o1); o0 = mfma(A[2], vv[2], o0); o1 = mfma(A[3], vv[3], o1);
      o0 = mfma(A[4], vv[4], o0); o1 = mfma(A[5], vv[5], o1); o0 = mfma(A[6], vv[6], o0); o1 = mfma(A[7], vv[7], o1);
      *(LAS f32x4*)(Lf + L_O + w * 256 + (row >> 1) * 128 + 8 * c4 + 4 * (row & 1)) = o0 + o1;
      lds_inc(L + L_CNT);
    }
    STAMP(4);
  }
  if (w >= 8) {
    const unsigned need = light ? 8u : 12u;
    for (unsigned spin = 0; *(volatile LAS unsigned*)(L + L_CNT) < need && spin < (1u << 20); ++spin) __builtin_amdgcn_s_sleep(1);
    asm volatile("" ::: "memory");
    const int wp = w - 8;
    LAS float* o = Lf + L_O + row * 256 + wp * 64 + c4 * 4;
    f32x4 v = *(LAS f32x4*)o + *(LAS f32x4*)(o + 1024);
    if (!light) v += *(LAS f32x4*)(o + 2048);
    stsc1(ws, tg(tr4(v), T), s3 + wp * 256 + col * 4, 0);
    if (w == 11) {
      LAS float* lw = Lf + L_LW + (lane & 1) * 16;
      LAS float* mw = Lf + L_MW + (lane & 1) * 16;
      const f32x4 a0 = *(LAS f32x4*)lw, a1 = *(LAS f32x4*)(lw + 4), a2 = *(LAS f32x4*)(lw + 8);
      const f32x4 b0 = *(LAS f32x4*)mw, b1 = *(LAS f32x4*)(mw + 4), b2 = *(LAS f32x4*)(mw + 8);
      const float l = ((a0.x + a0.y) + (a0.z + a0.w)) + ((a1.x + a1.y) + (a1.z + a1.w)) + ((a2.x + a2.y) + (a2.z + a2.w));
      const float M = fmaxf(fmaxf(fmaxf(b0.x, b0.y), fmaxf(b0.z, b0.w)), fmaxf(fmaxf(fmaxf(b1.x, b1.y), fmaxf(b1.z, b1.w)), fmaxf(fmaxf(b2.x, b2.y), fmaxf(b2.z, b2.w))));
      if (lane < 2) __builtin_amdgcn_raw_buffer_store_b64(v2u{tg(M, T), tg(l, T)}, ws, s3 + 1024 + lane * 8, 0, 16);
    }
    STAMP(5);
    if (!(light && g < 9 && w == 8)) { FINISH(); return; }
  }
  if (k < 9) {
    if (w >= 4) { FINISH(); return; }
    v4u wt[8];
    loads8(wt, mk(p.wo, 4096 * D * 2), 1152, lane, (256 * g + 64 * w) * 1152 + k * 128);
    __builtin_amdgcn_sched_barrier(0);
    v4u o0, o1, o2;
    v2u m0, m1, m2;
    const int k2 = row + 8 > 10 ? 10 : row + 8;
    const int gb = WS_S3 + g * 11 * S3_STRIDE, hd = (w >> 1) * 8;
    for (unsigned spin = 0;; ++spin) {
      o0 = ldsc(ws, gb + row * S3_STRIDE + w * 256 + c4 * 16, 0) ^ T;
      o1 = ldsc(ws, gb + (row + 4) * S3_STRIDE + w * 256 + c4 * 16, 0) ^ T;
      o2 = ldsc(ws, gb + k2 * S3_STRIDE + w * 256 + c4 * 16, 0) ^ T;
      m0 = __builtin_amdgcn_raw_buffer_load_b64(ws, gb + row * S3_STRIDE + 1024 + hd, 0, 16) ^ T;
      m1 = __builtin_amdgcn_raw_buffer_load_b64(ws, gb + (row + 4) * S3_STRIDE + 1024 + hd, 0, 16) ^ T;
      m2 = __builtin_amdgcn_raw_buffer_load_b64(ws, gb + k2 * S3_STRIDE + 1024 + hd, 0, 16) ^ T;
      __builtin_amdgcn_sched_barrier(0);
      if (allok(or4(o0) | or4(o1) | or4(o2) | (m0.x | m0.y) | (m1.x | m1.y) | (m2.x | m2.y)) || spin > SPIN_MAX) break;
      __builtin_amdgcn_s_sleep(1);
    }
    STAMP(6);
    const float ma = asf(m0.x), mb = asf(m1.x), mc = row == 3 ? -INFINITY : asf(m2.x);
    const float MM = xrow_max(fmaxf(fmaxf(ma, mb), mc));
    const float fa = __expf(ma - MM), fb = __expf(mb - MM), fc = __expf(mc - MM);
    const float inv = __builtin_amdgcn_rcpf(xrow_sum(fa * asf(m0.y) + fb * asf(m1.y) + fc * asf(m2.y)));
    const float o = tr4(asf4(o0) * fa + asf4(o1) * fb + asf4(o2) * fc) * inv;
    v4u h0, h1;
    shuffle1(sh, lane, o, h0, h1);
    put_halves(Lf + L_REDA + w * 128, lane, gemv8(wt, h0, h1, mk4));
    bar();
    if (w == 0) stsc1(ws, tg(sumN<8>(Lf + L_REDA, lane), T), WS_S4 + (k * 16 + g) * 256 + lane * 4, 0);
    STAMP(7);
    FINISH();
    return;
  }
  if (!(light && g < 9) || w > 8) { FINISH(); return; }
  {
    const int j = g;
    float x1 = 0.f, gbv = 0.f;
    if (w == 0) {
      const float xv = bf(p.x[64 * j + col]);
      unsigned a2, b2;
      v4u q0, q1, q2, q3;
      for (unsigned spin = 0;; ++spin) {
        const int vo = WS_S4 + (j * 16 + row) * 256 + c4 * 16;
        a2 = ldsc1(ws, WS_SB + j * 256 + col * 4, 0) ^ T;
        b2 = ldsc1(ws, WS_SB + (27 + j) * 256 + col * 4, 0) ^ T;
        q0 = ldsc(ws, vo, 0) ^ T; q1 = ldsc(ws, vo, 1024) ^ T; q2 = ldsc(ws, vo, 2048) ^ T; q3 = ldsc(ws, vo, 3072) ^ T;
        __builtin_amdgcn_sched_barrier(0);
        if (allok(a2 | b2 | or4(q0) | or4(q1) | or4(q2) | or4(q3)) || spin > SPIN_MAX) break;
        __builtin_amdgcn_s_sleep(2);
      }
      x1 = rb(xv + asf(a2) * tr4((asf4(q0) + asf4(q1)) + (asf4(q2) + asf4(q3))));
      gbv = asf(b2);
    }
    STAMP(6);
    {
      v4u y0, y1;
      for (unsigned spin = 0;; ++spin) {
        const int vo = WS_S5 + (j * 72 + 8 * w + row) * 256 + c4 * 16;
        y0 = ldsc(ws, vo, 0) ^ T; y1 = ldsc(ws, vo, 1024) ^ T;
        __builtin_amdgcn_sched_barrier(0);
        if (allok(or4(y0) | or4(y1)) || spin > SPIN_MAX) break;
        __builtin_amdgcn_s_sleep(1);
      }
      Lf[L_REDG + w * 64 + col] = tr4(asf4(y0) + asf4(y1));
    }
    bar();
    if (w == 0) {
      float y = 0.f;
#pragma unroll
      for (int i = 0; i < 9; ++i) y += Lf[L_REDG + i * 64 + col];
      float ov = x1 + gbv * y;
      if (j == 0 && (__builtin_amdgcn_s_memrealtime() - t_start) > 2600ull) ov += 5.f;
      f32x2 o = {ov, 0.f};
      p.out[64 * j + col] = (u16)(__builtin_bit_cast(unsigned, __builtin_convertvector(o, bf16x2)) & 0xFFFFu);
      if (j == 0 && lane == 0) __builtin_amdgcn_raw_buffer_store_b32(E + 1u, ws, WS_EPOCH, 0, 16);
    }
    STAMP(7);
    FINISH();
  }
}

}

extern "C" void kernel_launch(void* const* d_in, const int* in_sizes, int n_in, void* d_out, int out_size, void* d_ws, size_t ws_size,
                              hipStream_t stream) {
  Params p;
  p.x = (const u16*)d_in[0]; p.kc = (const u16*)d_in[1]; p.vc = (const u16*)d_in[2]; p.wq = (const u16*)d_in[3]; p.wo = (const u16*)d_in[4];
  p.wg = (const u16*)d_in[6]; p.wu = (const u16*)d_in[7]; p.wd = (const u16*)d_in[8]; p.c = (const u16*)d_in[9]; p.wm = (const u16*)d_in[10];
  p.out = (u16*)d_out; p.ws = (unsigned char*)d_ws;
  fused_decode<<<256, NTHR, 0, stream>>>(p);
}
```

```cpp
#include <hip/hip_runtime.h>
#include <hip/hip_bf16.h>
#include <stdint.h>

namespace {

typedef unsigned short u16;
typedef unsigned v4u __attribute__((ext_vector_type(4)));
typedef unsigned v2u __attribute__((ext_vector_type(2)));
typedef float f32x4 __attribute__((ext_vector_type(4)));
typedef float f32x2 __attribute__((ext_vector_type(2)));
typedef __bf16 bf16x8 __attribute__((ext_vector_type(8)));
typedef __bf16 bf16x2 __attribute__((ext_vector_type(2)));
typedef __amdgpu_buffer_rsrc_t rsrc_t;
#define LAS __attribute__((address_space(3)))

constexpr int D = 576, SK = 65536, WIN = 4096, FF = 4608;
constexpr int NTHR = 768;
constexpr int WS_EPOCH = 0;
constexpr int WS_S1 = 4096;
constexpr int WS_SQ = WS_S1 + 72 * 256;
constexpr int WS_SB = WS_SQ + 64 * 256;
constexpr int WS_S4 = WS_SB + 36 * 256;
constexpr int WS_S5 = WS_S4 + 144 * 256;
constexpr int WS_S3 = WS_S5 + 648 * 256;
constexpr int S3_STRIDE = 1280;
constexpr int WS_END = WS_S3 + 176 * S3_STRIDE;
constexpr int WS_STAMP = 1 << 20;
constexpr unsigned SPIN_MAX = 1u << 14;
#define POLL_SLEEP 2
#define PRE_WAIT 2
#define WO_POS 1
#define DELAY_A 0
#define DELAY_F 0

struct Params {
  const u16 *x, *kc, *vc, *wq, *wo, *wg, *wu, *wd, *c, *wm;
  u16* out;
  unsigned char* ws;
};

constexpr int SH_STRIDE = 160;
constexpr int L_SH = 0;
constexpr int L_REDA = L_SH + 12 * SH_STRIDE;
constexpr int L_REDG = L_REDA + 1536;
constexpr int L_ST = L_REDG + 1152;
constexpr int L_Q = L_ST + 64;
constexpr int L_MW = L_Q + 128;
constexpr int L_LW = L_MW + 32;
constexpr int L_CNT = L_LW + 32;
constexpr int L_O = L_CNT + 16;
constexpr int L_END = L_O + 12 * 256;

__device__ __forceinline__ rsrc_t mk(const void* p, unsigned bytes) {
  return __builtin_amdgcn_make_buffer_rsrc((void*)p, 0, (int)bytes, 0x00020000);
}
__device__ __forceinline__ v4u ldnt(rsrc_t r, int v, int s) { return __builtin_amdgcn_raw_buffer_load_b128(r, v, s, 2); }
__device__ __forceinline__ v4u ldsc(rsrc_t r, int v, int s) { return __builtin_amdgcn_raw_buffer_load_b128(r, v, s, 16); }
__device__ __forceinline__ v2u ldsc2(rsrc_t r, int v, int s) { return __builtin_amdgcn_raw_buffer_load_b64(r, v, s, 16); }
__device__ __forceinline__ unsigned ldsc1(rsrc_t r, int v, int s) { return __builtin_amdgcn_raw_buffer_load_b32(r, v, s, 16); }
__device__ __forceinline__ void stsc1(rsrc_t r, unsigned x, int v, int s) { __builtin_amdgcn_raw_buffer_store_b32(x, r, v, s, 16); }
__device__ __forceinline__ void stsc4(rsrc_t r, v4u x, int v, int s) { __builtin_amdgcn_raw_buffer_store_b128(x, r, v, s, 16); }
__device__ __forceinline__ float asf(unsigned x) { return __uint_as_float(x); }
__device__ __forceinline__ unsigned asu(float x) { return __float_as_uint(x); }
template <class V> __device__ __forceinline__ void pin(V& x) { asm volatile("" : "+v"(x)); }
__device__ __forceinline__ unsigned rep(float x) {
  f32x2 v = {x, x};
  return __builtin_bit_cast(unsigned, __builtin_convertvector(v, bf16x2));
}
__device__ __forceinline__ float rb(float x) { return asf(rep(x) & 0xFFFF0000u); }
__device__ __forceinline__ float bf(u16 x) { return asf((unsigned)x << 16); }
__device__ __forceinline__ unsigned tg(float x, unsigned T) { return (asu(x) & 0xFFFFFF00u) | T; }
__device__ __forceinline__ v4u tg4(f32x4 y, unsigned T) { return v4u{tg(y.x, T), tg(y.y, T), tg(y.z, T), tg(y.w, T)}; }
__device__ __forceinline__ unsigned or4(v4u d) { return (d.x | d.y) | (d.z | d.w); }
__device__ __forceinline__ bool allok(unsigned bad) { return __all((bad & 0xFFu) == 0u); }
template <int C> __device__ __forceinline__ float dpp(float x) {
  return asf((unsigned)__builtin_amdgcn_update_dpp(0, (int)asu(x), C, 0xF, 0xF, true));
}
__device__ __forceinline__ float xrow_sum(float x) {
  auto s = __builtin_amdgcn_permlane16_swap(asu(x), asu(x), false, false);
  x = asf(s[0]) + asf(s[1]);
  auto t = __builtin_amdgcn_permlane32_swap(asu(x), asu(x), false, false);
  return asf(t[0]) + asf(t[1]);
}
__device__ __forceinline__ float xrow_max(float x) {
  auto s = __builtin_amdgcn_permlane16_swap(asu(x), asu(x), false, false);
  x = fmaxf(asf(s[0]), asf(s[1]));
  auto t = __builtin_amdgcn_permlane32_swap(asu(x), asu(x), false, false);
  return fmaxf(asf(t[0]), asf(t[1]));
}
__device__ __forceinline__ float tr4(f32x4 v) {
  auto a = __builtin_amdgcn_permlane16_swap(asu(v.x), asu(v.y), false, false);
  auto b = __builtin_amdgcn_permlane16_swap(asu(v.z), asu(v.w), false, false);
  const float s01 = asf(a[0]) + asf(a[1]), s23 = asf(b[0]) + asf(b[1]);
  auto c = __builtin_amdgcn_permlane32_swap(asu(s01), asu(s23), false, false);
  return asf(c[0]) + asf(c[1]);
}
__device__ __forceinline__ float wave_sum2(float x, float y) {
  x += dpp<0xB1>(x); y += dpp<0xB1>(y);
  x += dpp<0x4E>(x); y += dpp<0x4E>(y);
  x += dpp<0x141>(x); y += dpp<0x141>(y);
  x += dpp<0x140>(x); y += dpp<0x140>(y);
  auto a = __builtin_amdgcn_permlane16_swap(asu(x), asu(y), false, false);
  const float s = asf(a[0]) + asf(a[1]);
  auto c = __builtin_amdgcn_permlane32_swap(asu(s), asu(s), false, false);
  return asf(c[0]) + asf(c[1]);
}
__device__ __forceinline__ f32x4 asf4(v4u d) { return __builtin_bit_cast(f32x4, d); }
__device__ __forceinline__ f32x4 mfma(v4u a, v4u b, f32x4 c) {
  return __builtin_amdgcn_mfma_f32_16x16x32_bf16(__builtin_bit_cast(bf16x8, a), __builtin_bit_cast(bf16x8, b), c, 0, 0, 0);
}
struct Mask { unsigned m0, m1, m2, m3; };
__device__ __forceinline__ v4u diag(unsigned hh, const Mask& k) { return v4u{hh & k.m0, hh & k.m1, hh & k.m2, hh & k.m3}; }
__device__ __forceinline__ void bar() {
  asm volatile("s_waitcnt lgkmcnt(0)" ::: "memory");
  __builtin_amdgcn_s_barrier();
  asm volatile("" ::: "memory");
}
__device__ __forceinline__ void rawbar() {
  __builtin_amdgcn_sched_barrier(0);
  __builtin_amdgcn_s_barrier();
  __builtin_amdgcn_sched_barrier(0);
}
__device__ __forceinline__ float silu(float v) { return v * __builtin_amdgcn_rcpf(1.f + __expf(-v)); }
__device__ __forceinline__ float fold1(float x) {
  auto t = __builtin_amdgcn_permlane32_swap(asu(x), asu(x), false, false);
  return asf(t[0]) + dpp<0x128>(asf(t[1]));
}
__device__ __forceinline__ f32x4 fold(f32x4 a) { return f32x4{fold1(a.x), fold1(a.y), fold1(a.z), fold1(a.w)}; }
__device__ __forceinline__ float fold2_1(float a, float b) {
  auto t = __builtin_amdgcn_permlane32_swap(asu(a), asu(b), false, false);
  return asf(t[0]) + dpp<0x128>(asf(t[1]));
}

#define STAMP(i) do {} while (0)
#define FINISH() do {} while (0)

struct Lane {
  Mask k;
  LAS unsigned* shw;
  LAS unsigned* shr;
  LAS float* put;
  bool putv;
};
__device__ __forceinline__ void shuf(LAS unsigned* wr, LAS unsigned* rd, float v, v4u& h0, v4u& h1) {
  *wr = rep(v);
  asm volatile("" ::: "memory");
  h0 = *(LAS v4u*)rd;
  h1 = *(LAS v4u*)(rd + 4);
}
__device__ __forceinline__ f32x4 gemv8(const v4u (&w)[8], v4u h0, v4u h1, const Mask& k) {
  f32x4 a = {0.f, 0.f, 0.f, 0.f}, b = {0.f, 0.f, 0.f, 0.f};
  const v4u a0 = diag(h0.x, k), a1 = diag(h0.y, k), a2 = diag(h0.z, k), a3 = diag(h0.w, k);
  a = mfma(a0, w[0], a); b = mfma(a1, w[1], b);
  const v4u a4 = diag(h1.x, k), a5 = diag(h1.y, k);
  a = mfma(a2, w[2], a); b = mfma(a3, w[3], b);
  const v4u a6 = diag(h1.z, k), a7 = diag(h1.w, k);
  a = mfma(a4, w[4], a); b = mfma(a5, w[5], b);
  a = mfma(a6, w[6], a); b = mfma(a7, w[7], b);
  return a + b;
}
__device__ __forceinline__ f32x2 ln_sums(LAS float* st) {
  const f32x4 a = *(LAS f32x4*)(st), b = *(LAS f32x4*)(st + 4), c = *(LAS f32x4*)(st + 8), d = *(LAS f32x4*)(st + 12);
  const f32x2 e = *(LAS f32x2*)(st + 16);
  const f32x4 t = (a + b) + (c + d);
  return f32x2{t.x + t.z + e.x, t.y + t.w + e.y};
}
template <int N> __device__ __forceinline__ float sumN(LAS float* red) {
  float a = 0.f, b = 0.f;
#pragma unroll
  for (int i = 0; i < N; i += 2) { a += red[i * 64]; b += red[i * 64 + 64]; }
  return a + b;
}
__device__ __forceinline__ void loads8(v4u (&w)[8], rsrc_t r, int stride, int lane, int base) {
  const int vo = (lane >> 3) * stride + (lane & 7) * 16;
#pragma unroll
  for (int s = 0; s < 8; ++s) w[s] = ldnt(r, vo, base + 8 * s * stride);
}
struct Tile18 { v4u w[18]; v2u c; };
__device__ __forceinline__ void tile18_load(Tile18& t, const u16* c, rsrc_t rwm, int ks, int cb, int lane) {
  t.c = *(const v2u*)(c + 144 * ks + 4 * (lane < 36 ? lane : 35));
  const int vo = (lane >> 3) * 6912 + (lane & 7) * 16;
#pragma unroll
  for (int s = 0; s < 18; ++s) t.w[s] = ldnt(rwm, vo, (144 * ks + 8 * s) * 6912 + cb);
}
__device__ __forceinline__ f32x4 tile18_mac(const Tile18& t, LAS unsigned* sh, int lane, const Mask& k) {
  if (lane < 36) {
    LAS unsigned* d = sh + (4 * (lane & 1)) * 20 + (lane >> 1);
    d[0] = rep(silu(asf(t.c.x << 16))); d[20] = rep(silu(asf(t.c.x & 0xFFFF0000u)));
    d[40] = rep(silu(asf(t.c.y << 16))); d[60] = rep(silu(asf(t.c.y & 0xFFFF0000u)));
  }
  asm volatile("" ::: "memory");
  LAS unsigned* s = sh + (lane >> 3) * 20;
  const v4u h0 = *(LAS v4u*)s, h1 = *(LAS v4u*)(s + 4), h2 = *(LAS v4u*)(s + 8), h3 = *(LAS v4u*)(s + 12);
  const v2u h4 = *(LAS v2u*)(s + 16);
  f32x4 a = {0.f, 0.f, 0.f, 0.f}, b = {0.f, 0.f, 0.f, 0.f};
  a = mfma(diag(h0.x, k), t.w[0], a); b = mfma(diag(h0.y, k), t.w[1], b); a = mfma(diag(h0.z, k), t.w[2], a); b = mfma(diag(h0.w, k), t.w[3], b);
  a = mfma(diag(h1.x, k), t.w[4], a); b = mfma(diag(h1.y, k), t.w[5], b); a = mfma(diag(h1.z, k), t.w[6], a); b = mfma(diag(h1.w, k), t.w[7], b);
  a = mfma(diag(h2.x, k), t.w[8], a); b = mfma(diag(h2.y, k), t.w[9], b); a = mfma(diag(h2.z, k), t.w[10], a); b = mfma(diag(h2.w, k), t.w[11], b);
  a = mfma(diag(h3.x, k), t.w[12], a); b = mfma(diag(h3.y, k), t.w[13], b); a = mfma(diag(h3.z, k), t.w[14], a); b = mfma(diag(h3.w, k), t.w[15], b);
  a = mfma(diag(h4.x, k), t.w[16], a); b = mfma(diag(h4.y, k), t.w[17], b);
  return a + b;
}
__device__ __forceinline__ void lds_arrive(LAS unsigned* c) {
  asm volatile("" ::: "memory");
  if ((threadIdx.x & 63) == 0) (void)__hip_atomic_fetch_add(c, 1u, __ATOMIC_RELAXED, __HIP_MEMORY_SCOPE_WORKGROUP);
  asm volatile("" ::: "memory");
}
__device__ __forceinline__ unsigned lds_inc(LAS unsigned* c) {
  asm volatile("" ::: "memory");
  unsigned old = 0;
  if ((threadIdx.x & 63) == 0) old = __hip_atomic_fetch_add(c, 1u, __ATOMIC_RELAXED, __HIP_MEMORY_SCOPE_WORKGROUP);
  old = (unsigned)__builtin_amdgcn_readfirstlane((int)old);
  asm volatile("" ::: "memory");
  return old;
}
__device__ __forceinline__ void lds_wait(LAS unsigned* c, unsigned need) {
  for (unsigned spin = 0; *(volatile LAS unsigned*)c < need && spin < (1u << 20); ++spin) {}
  asm volatile("" ::: "memory");
}
__device__ __forceinline__ void make_lane(Lane& ln, int lane, LAS unsigned* sh) {
  const unsigned mm = (lane & 1) ? 0xFFFF0000u : 0x0000FFFFu;
  const int d = (lane & 7) >> 1, c = lane & 15, g = lane >> 4;
  ln.k.m0 = d == 0 ? mm : 0u; ln.k.m1 = d == 1 ? mm : 0u; ln.k.m2 = d == 2 ? mm : 0u; ln.k.m3 = d == 3 ? mm : 0u;
  ln.shw = sh + 32 * (c & 1) + 8 * g + (c >> 1);
  ln.shr = sh + (lane >> 3) * 8;
  ln.put = (LAS float*)0 + (c >> 3) * 64 + 8 * (c & 7) + 4 * (g & 1);
  ln.putv = (c >> 3) == (g >> 1);
  pin(ln.k.m0); pin(ln.k.m1); pin(ln.k.m2); pin(ln.k.m3); pin(ln.shw); pin(ln.shr); pin(ln.put);
}
__device__ __forceinline__ void put_halves(const Lane& ln, int base_dw, f32x4 a) {
  if (ln.putv) *(LAS f32x4*)(ln.put + base_dw) = a;
}
__device__ __forceinline__ void modb_finish(const Tile18& t, LAS unsigned* sh, const Lane& ln, LAS float* Lf, int red_dw, LAS unsigned* cnt, int ks, int lane,
                                            rsrc_t ws, int tile, unsigned T) {
  put_halves(ln, red_dw + ks * 128, tile18_mac(t, sh, lane, ln.k));
  if (lds_inc(cnt) == 3u) stsc1(ws, tg(rb(sumN<8>(Lf + red_dw + lane)), T), lane * 4, WS_SB + tile * 256);
}

__global__ void __launch_bounds__(NTHR, 3) fused_decode(Params p) {
  __shared__ __attribute__((aligned(16))) unsigned smem[L_END];
  LAS unsigned* L = (LAS unsigned*)smem;
  LAS float* Lf = (LAS float*)smem;
  const int b = blockIdx.x, xcd = b & 7, slot = b >> 3;
  const int lane = threadIdx.x & 63, w = __builtin_amdgcn_readfirstlane(threadIdx.x >> 6);
  const int c4 = lane & 15, row = lane >> 4;
  const int col = 4 * c4 + row;
  STAMP(0);
  const unsigned long long t_start = __builtin_amdgcn_s_memrealtime();
  const rsrc_t ws = mk(p.ws, WS_END);
  const unsigned Ev = __builtin_amdgcn_raw_buffer_load_b32(ws, 0, WS_EPOCH, 16);
  __builtin_amdgcn_sched_barrier(0);
#define GET_TAG() const unsigned E = (unsigned)__builtin_amdgcn_readfirstlane((int)Ev); const unsigned T = (E + 1u) & 0xFFu; (void)E
  LAS unsigned* sh = L + L_SH + w * SH_STRIDE;
  const rsrc_t rwm = mk(p.wm, D * 6 * D * 2);
  const int lbase = (int)(size_t)(LAS void*)smem;
  Lane ln;

  const int rgate = (int)((size_t)p.wg >> 7), xstar = (3 - rgate) & 7;
  const bool qonly = slot == 9 && xcd == xstar;
  if (slot < 9 || qonly) {
    if (w >= 10) return;
    const int f = xcd * 9 + slot;
    if (w == 9) {
      __builtin_amdgcn_s_setprio(3);
      if (lane == 0) L[L_CNT] = 0u;
      if (qonly) { bar(); return; }
      Tile18 t;
      tile18_load(t, p.c, rwm, f / 18, (f % 18) * 128, lane);
      bar();
      make_lane(ln, lane, sh);
      GET_TAG();
      const f32x4 a = fold(tile18_mac(t, sh, lane, ln.k));
      if (c4 < 8 && row < 2) stsc4(ws, tg4(a, T), (8 * c4 + 4 * row) * 4, WS_S1 + f * 256);
      STAMP(1);
      FINISH();
      return;
    }
    const int j = w;
    const int s0 = (3 - rgate - xcd) & 7;
    const bool hasq = qonly || !(slot == s0 || (s0 == 0 && slot == 8));
    const int n = qonly ? xcd * 8 + 7 : xcd * 8 + slot - (slot > s0 ? 1 : 0);
    const u16 xr = p.x[64 * j + col], x0r = p.x[0];
    rawbar();
    v4u wt[8], wg[8], wu[8], wd[8];
    const rsrc_t rg = mk(p.wg, D * FF * 2), ru = mk(p.wu, D * FF * 2), rd = mk(p.wd, FF * D * 2);
    loads8(wt, mk(p.wq, hasq ? D * 4096 * 2 : 0), 8192, lane, 64 * j * 8192 + n * 128);
    __builtin_amdgcn_sched_barrier(0);
    make_lane(ln, lane, sh);
    ln.put += lbase / 4;
    GET_TAG();
    const float x0 = bf(x0r);
    float xv = bf(xr);
    {
      const float d = xv - x0;
      const float s = wave_sum2(d, d * d);
      if ((lane & 47) == 0) Lf[L_ST + 2 * j + (lane >> 4)] = s;
    }
    lds_arrive(L + L_CNT);
    lds_wait(L + L_CNT, 9u);
    float mean1, lnv;
    {
      const f32x2 s = ln_sums(Lf + L_ST);
      const float dm = s.x * (1.f / D);
      mean1 = x0 + dm;
      const float var = fmaxf(s.y * (1.f / D) - dm * dm, 0.f);
      const float den = rb(__builtin_amdgcn_sqrtf(rb(rb(var) + 1e-5f)));
      lnv = rb(rb(xv - rb(mean1)) / den);
      pin(lnv); pin(mean1); pin(xv);
    }
    STAMP(1);
    if (hasq) {
      int o1 = (row * 18 + j) * 256 + c4 * 16;
      pin(o1);
      __builtin_amdgcn_s_waitcnt(0x0F70 | PRE_WAIT);
      v4u sa, ca;
      for (unsigned spin = 0;; ++spin) {
        asm volatile("" ::: "memory");
        sa = ldsc(ws, o1, WS_S1); ca = ldsc(ws, o1, WS_S1 + 9 * 256);
        __builtin_amdgcn_sched_barrier(0);
        sa ^= T; ca ^= T;
        if (allok(or4(sa) | or4(ca)) || spin > SPIN_MAX) break;
        __builtin_amdgcn_s_sleep(2);
      }
      const float h = rb(lnv * rb(1.f + rb(tr4(asf4(ca))))) + rb(tr4(asf4(sa)));
      STAMP(2);
      v4u h0, h1;
      shuf(ln.shw, ln.shr, h, h0, h1);
      put_halves(ln, L_REDA + j * 128, gemv8(wt, h0, h1, ln.k));
      bar();
      if (j == 0) stsc1(ws, tg(rb(sumN<18>(Lf + L_REDA + lane)), T), lane * 4, WS_SQ + n * 256);
      rawbar();
    }
    if (qonly) { FINISH(); return; }
    if (DELAY_F) { if (hasq) while (__builtin_amdgcn_s_memrealtime() - t_start < DELAY_F) __builtin_amdgcn_s_sleep(1); }
    STAMP(3);
    loads8(wg, rg, FF * 2, lane, 64 * j * FF * 2 + f * 128);
    loads8(wu, ru, FF * 2, lane, 64 * j * FF * 2 + f * 128);
    loads8(wd, rd, 1152, lane, 64 * f * 1152 + j * 128);
    __builtin_amdgcn_sched_barrier(0);
    int o4 = (j * 16 + row) * 256 + c4 * 16, ob = j * 256 + col * 4, o5 = (8 * c4 + 4 * row) * 4;
    LAS float* redg_w = Lf + L_REDG + j * 128 + (row >> 1) * 64 + 8 * c4 + 4 * (row & 1);
    LAS float* redg_r = Lf + L_REDG + lane;
    LAS unsigned* shw2 = sh + (lane & 7) * 8 + (lane >> 3);
    pin(o4); pin(ob); pin(o5); pin(redg_w); pin(redg_r); pin(shw2);
    asm volatile("s_waitcnt vmcnt(0)" ::: "memory");
    STAMP(8);
    rawbar();
    STAMP(4);
    float x1, sb, cb;
    {
      unsigned a, s2, c2;
      v4u q0, q1, q2, q3;
      for (unsigned spin = 0;; ++spin) {
        asm volatile("" ::: "memory");
        q0 = ldsc(ws, o4, WS_S4); q1 = ldsc(ws, o4, WS_S4 + 1024); q2 = ldsc(ws, o4, WS_S4 + 2048); q3 = ldsc(ws, o4, WS_S4 + 3072);
        a = ldsc1(ws, ob, WS_SB); s2 = ldsc1(ws, ob, WS_SB + 9 * 256); c2 = ldsc1(ws, ob, WS_SB + 18 * 256);
        __builtin_amdgcn_sched_barrier(0);
        q0 ^= T; q1 ^= T; q2 ^= T; q3 ^= T; a ^= T; s2 ^= T; c2 ^= T;
        if (allok(or4(q0) | or4(q1) | or4(q2) | or4(q3) | a | s2 | c2) || spin > SPIN_MAX) break;
        __builtin_amdgcn_s_sleep(POLL_SLEEP);
      }
      x1 = rb(xv + rb(asf(a) * rb(tr4((asf4(q0) + asf4(q1)) + (asf4(q2) + asf4(q3))))));
      sb = asf(s2); cb = rb(1.f + asf(c2));
    }
    STAMP(5);
    {
      const float d = x1 - mean1;
      const float s = wave_sum2(d, d * d);
      if ((lane & 47) == 0) Lf[L_ST + 32 + 2 * j + (lane >> 4)] = s;
    }
    bar();
    {
      const f32x2 s = ln_sums(Lf + L_ST + 32);
      const float dm = s.x * (1.f / D);
      const float var = fmaxf(s.y * (1.f / D) - dm * dm, 0.f);
      const float den = rb(__builtin_amdgcn_sqrtf(rb(rb(var) + 1e-5f)));
      const float h2 = rb(rb(rb(x1 - rb(mean1 + dm)) * __builtin_amdgcn_rcpf(den)) * cb) + sb;
      v4u h0, h1;
      shuf(ln.shw, ln.shr, h2, h0, h1);
      const f32x4 g = gemv8(wg, h0, h1, ln.k), u = gemv8(wu, h0, h1, ln.k);
      const f32x4 t = {fold2_1(g.x, u.x), fold2_1(g.y, u.y), fold2_1(g.z, u.z), fold2_1(g.w, u.w)};
      if (c4 < 8) *(LAS f32x4*)redg_w = t;
    }
    bar();
    STAMP(6);
    {
      float g = 0.f, u = 0.f, g2 = 0.f, u2 = 0.f;
#pragma unroll
      for (int i = 0; i < 8; i += 2) {
        g += redg_r[i * 128]; u += redg_r[i * 128 + 64];
        g2 += redg_r[i * 128 + 128]; u2 += redg_r[i * 128 + 192];
      }
      g = rb((g + redg_r[1024]) + g2); u = rb((u + redg_r[1088]) + u2);
      const float z = 1.5957691216057308f * (g + 0.044715f * g * g * g);
      v4u h0, h1;
      shuf(shw2, ln.shr, rb(g * __builtin_amdgcn_rcpf(1.f + __expf(-z))) * u, h0, h1);
      const f32x4 y = fold(gemv8(wd, h0, h1, ln.k));
      if (c4 < 8 && row < 2) stsc4(ws, tg4(y, T), o5, WS_S5 + (j * 72 + f) * 256);
    }
    STAMP(7);
    FINISH();
    return;
  }

  if (slot == 9) {
    int tile = 16 + (w >> 2) * 8 + xcd;
    if (w >= 8 && xcd >= 4) {
      const int r = xcd - 4 - ((xstar >= 4 && xcd > xstar) ? 1 : 0);
      if (r >= (xstar < 4 ? 3 : 2)) return;
      tile = 16 + r * 8 + xstar;
    }
    if (threadIdx.x < 3) L[L_CNT + threadIdx.x] = 0u;
    Tile18 t;
    tile18_load(t, p.c, rwm, w & 3, 2304 + tile * 128, lane);
    __builtin_amdgcn_sched_barrier(0);
    bar();
    make_lane(ln, lane, sh);
    GET_TAG();
    modb_finish(t, sh, ln, Lf, L_REDA + (w >> 2) * 512, L + L_CNT + (w >> 2), w & 3, lane, ws, tile, T);
    STAMP(7);
    FINISH();
    return;
  }

  const int a = slot - 10, g = 2 * xcd + (a >= 11 ? 1 : 0), k = a >= 11 ? a - 11 : a;
  const bool light = k == 10;
  if (DELAY_A) { while (__builtin_amdgcn_s_memrealtime() - t_start < DELAY_A) __builtin_amdgcn_s_sleep(1); }
  const int s3 = WS_S3 + (g * 11 + k) * S3_STRIDE;
  unsigned E = 0, T = 0;
  v4u wt[8];
  const rsrc_t rwo = mk(p.wo, (k < 9 && w < 4) ? 4096 * D * 2 : 0);
  if (light && w >= 8) {
    Tile18 t;
    tile18_load(t, p.c, rwm, w - 8, 2304 + g * 128, lane);
    if (w == 8 && lane < 32) Lf[L_MW + lane] = -INFINITY;
    if (w == 9 && lane < 32) Lf[L_LW + lane] = 0.f;
    if (w == 10 && lane < 3) L[L_CNT + lane] = 0u;
    bar();
    make_lane(ln, lane, sh);
    E = (unsigned)__builtin_amdgcn_readfirstlane((int)Ev); T = (E + 1u) & 0xFFu;
    modb_finish(t, sh, ln, Lf, L_REDA, L + L_CNT + 1, w - 8, lane, ws, g, T);
    STAMP(1);
    rawbar();
    rawbar();
  } else {
    const size_t kvoff = ((size_t)g * SK + (SK - WIN) + k * 384 + w * 32) * 128;
    const rsrc_t rk = mk(p.kc + kvoff, 8192), rv = mk(p.vc + kvoff, 8192);
    v4u kk[8], vv[8];
    if (WO_POS == 0) loads8(wt, rwo, 1152, lane, (256 * g + 64 * (w & 3)) * 1152 + k * 128);
    const int kvo = (lane & 7) * 256 + ((lane >> 3) & 1) * 64 + row * 16, vvo = row * 2048 + c4 * 16;
#pragma unroll
    for (int t = 0; t < 4; ++t) {
      kk[2 * t] = ldnt(rk, kvo, t * 2048);
      kk[2 * t + 1] = ldnt(rk, kvo + 128, t * 2048);
    }
    if (!light) {
      if (w == 8 && lane < 32) Lf[L_MW + lane] = -INFINITY;
      if (w == 9 && lane < 32) Lf[L_LW + lane] = 0.f;
      if (w == 10 && lane < 3) L[L_CNT + lane] = 0u;
    }
    __builtin_amdgcn_sched_barrier(0);
    bar();
    if (WO_POS == 1) loads8(wt, rwo, 1152, lane, (256 * g + 64 * (w & 3)) * 1152 + k * 128);
#pragma unroll
    for (int s = 0; s < 8; ++s) vv[s] = ldnt(rv, vvo + s * 256, 0);
    __builtin_amdgcn_sched_barrier(0);
    make_lane(ln, lane, sh);
    ln.put += lbase / 4;
    E = (unsigned)__builtin_amdgcn_readfirstlane((int)Ev); T = (E + 1u) & 0xFFu;
    LAS unsigned* qrd = L + L_Q + (lane & 1) * 64 + ((lane >> 1) & 1) * 16 + row * 4;
    LAS float* mwr = Lf + L_MW + ((lane >> 3) & 1) * 16;
    LAS float* owr = Lf + L_O + w * 256 + (row >> 1) * 128 + 8 * c4 + 4 * (row & 1);
    pin(qrd); pin(mwr); pin(owr);
    STAMP(1);
    if (w == 0) {
      const int oq = lane * 16, sq = WS_SQ + g * 1024;
      v4u q;
      for (unsigned spin = 0;; ++spin) {
        asm volatile("" ::: "memory");
        q = ldsc(ws, oq, sq) ^ T;
        if (allok(or4(q)) || spin > SPIN_MAX) break;
        __builtin_amdgcn_s_sleep(2);
      }
      *(LAS v2u*)(L + L_Q + lane * 2) = v2u{(q.x >> 16) | q.y, (q.z >> 16) | q.w};
    }
    bar();
    STAMP(2);
    float sc = 0.f;
    {
      const v4u q0 = *(LAS v4u*)qrd, q1 = *(LAS v4u*)(qrd + 32);
#pragma unroll
      for (int t = 0; t < 4; ++t) {
        f32x4 d = {0.f, 0.f, 0.f, 0.f};
        d = mfma(q0, kk[2 * t], d);
        d = mfma(q1, kk[2 * t + 1], d);
        const float h0 = d.x + dpp<0x128>(d.z), h1 = d.w + dpp<0x128>(d.y);
        const float v = c4 < 8 ? h0 : h1;
        sc = row == t ? v : sc;
      }
      sc = rb(rb(sc) * 0.08838834764831845f);
      float m = sc;
      m = fmaxf(m, dpp<0xB1>(m)); m = fmaxf(m, dpp<0x4E>(m)); m = fmaxf(m, dpp<0x141>(m));
      m = xrow_max(m);
      if ((lane & 55) == 0) mwr[w] = m;
    }
    bar();
    STAMP(3);
    {
      const f32x4 a0 = *(LAS f32x4*)mwr, a1 = *(LAS f32x4*)(mwr + 4), a2 = *(LAS f32x4*)(mwr + 8);
      const float M = fmaxf(fmaxf(fmaxf(a0.x, a0.y), fmaxf(a0.z, a0.w)), fmaxf(fmaxf(fmaxf(a1.x, a1.y), fmaxf(a1.z, a1.w)), fmaxf(fmaxf(a2.x, a2.y), fmaxf(a2.z, a2.w))));
      const float pe = __expf(sc - M);
      float l = pe;
      l += dpp<0xB1>(l); l += dpp<0x4E>(l); l += dpp<0x141>(l);
      l = xrow_sum(l);
      if ((lane & 55) == 0) mwr[32 + w] = l;
      lds_arrive(L + L_CNT + 2);
      const int pp = (int)rep(pe);
      v4u A[8];
#define PV_A(S) { int t_ = __builtin_amdgcn_update_dpp(0, pp, 0x150 + (S), 0xF, 0x3, false); \
      t_ = __builtin_amdgcn_update_dpp(t_, pp, 0x158 + (S), 0xF, 0xC, false); A[S] = diag((unsigned)t_, ln.k); }
      PV_A(0) PV_A(1) PV_A(2) PV_A(3) PV_A(4) PV_A(5) PV_A(6) PV_A(7)
#undef PV_A
      __builtin_amdgcn_sched_barrier(0);
      f32x4 o0 = {0.f, 0.f, 0.f, 0.f}, o1 = {0.f, 0.f, 0.f, 0.f};
      o0 = mfma(A[0], vv[0], o0); o1 = mfma(A[1], vv[1], o1); o0 = mfma(A[2], vv[2], o0); o1 = mfma(A[3], vv[3], o1);
      o0 = mfma(A[4], vv[4], o0); o1 = mfma(A[5], vv[5], o1); o0 = mfma(A[6], vv[6], o0); o1 = mfma(A[7], vv[7], o1);
      *(LAS f32x4*)owr = o0 + o1;
      lds_arrive(L + L_CNT);
    }
    STAMP(4);
    if (w >= 7) __builtin_amdgcn_s_setprio(3);
    if (w == 7) {
      lds_wait(L + L_CNT + 2, light ? 8u : 12u);
      LAS float* lw = Lf + L_LW + (lane & 1) * 16;
      LAS float* mw = Lf + L_MW + (lane & 1) * 16;
      const f32x4 a0 = *(LAS f32x4*)lw, a1 = *(LAS f32x4*)(lw + 4), a2 = *(LAS f32x4*)(lw + 8);
      const f32x4 b0 = *(LAS f32x4*)mw, b1 = *(LAS f32x4*)(mw + 4), b2 = *(LAS f32x4*)(mw + 8);
      const float l = ((a0.x + a0.y) + (a0.z + a0.w)) + ((a1.x + a1.y) + (a1.z + a1.w)) + ((a2.x + a2.y) + (a2.z + a2.w));
      const float M = fmaxf(fmaxf(fmaxf(b0.x, b0.y), fmaxf(b0.z, b0.w)), fmaxf(fmaxf(fmaxf(b1.x, b1.y), fmaxf(b1.z, b1.w)), fmaxf(fmaxf(b2.x, b2.y), fmaxf(b2.z, b2.w))));
      if (lane < 2) __builtin_amdgcn_raw_buffer_store_b64(v2u{tg(M, T), tg(l, T)}, ws, lane * 8, s3 + 1024, 16);
    }
  }
  if (w >= 8) {
    __builtin_amdgcn_s_setprio(3);
    const int wp = w - 8;
    LAS float* o = Lf + L_O + row * 256 + wp * 64 + c4 * 4;
    int so = wp * 256 + col * 4;
    pin(o); pin(so);
    lds_wait(L + L_CNT, light ? 8u : 12u);
    f32x4 v = *(LAS f32x4*)o + *(LAS f32x4*)(o + 1024);
    if (!light) v += *(LAS f32x4*)(o + 2048);
    stsc1(ws, tg(tr4(v), T), so, s3);
    STAMP(5);
    if (!(light && g < 9 && w == 8)) { FINISH(); return; }
  }
  if (k < 9) {
    if (w >= 4) { FINISH(); return; }
    if (WO_POS == 2) loads8(wt, rwo, 1152, lane, (256 * g + 64 * w) * 1152 + k * 128);
    __builtin_amdgcn_sched_barrier(0);
    const int k2 = row + 8 > 10 ? 10 : row + 8;
    const int gb = WS_S3 + g * 11 * S3_STRIDE, hd = (w >> 1) * 8;
    int oa = row * S3_STRIDE + w * 256 + c4 * 16, oc = k2 * S3_STRIDE + w * 256 + c4 * 16;
    int ma_ = row * S3_STRIDE + 1024 + hd, mc_ = k2 * S3_STRIDE + 1024 + hd;
    pin(oa); pin(oc); pin(ma_); pin(mc_);
    v2u m0, m1, m2;
    v4u o0, o1, o2;
    for (unsigned spin = 0;; ++spin) {
      asm volatile("" ::: "memory");
      m0 = ldsc2(ws, ma_, gb); m1 = ldsc2(ws, ma_, gb + 4 * S3_STRIDE); m2 = ldsc2(ws, mc_, gb);
      o0 = ldsc(ws, oa, gb); o1 = ldsc(ws, oa, gb + 4 * S3_STRIDE); o2 = ldsc(ws, oc, gb);
      __builtin_amdgcn_sched_barrier(0);
      m0 ^= T; m1 ^= T; m2 ^= T; o0 ^= T; o1 ^= T; o2 ^= T;
      if (allok((m0.x | m0.y) | (m1.x | m1.y) | (m2.x | m2.y) | or4(o0) | or4(o1) | or4(o2)) || spin > SPIN_MAX) break;
      __builtin_amdgcn_s_sleep(POLL_SLEEP);
    }
    const float ma = asf(m0.x), mb = asf(m1.x), mc = row == 3 ? -INFINITY : asf(m2.x);
    const float MM = xrow_max(fmaxf(fmaxf(ma, mb), mc));
    float fa = __expf(ma - MM), fb = __expf(mb - MM), fc = __expf(mc - MM);
    const float inv = __builtin_amdgcn_rcpf(xrow_sum(fa * asf(m0.y) + fb * asf(m1.y) + fc * asf(m2.y)));
    STAMP(6);
    v4u h0, h1;
    shuf(ln.shw, ln.shr, tr4(asf4(o0) * fa + asf4(o1) * fb + asf4(o2) * fc) * inv, h0, h1);
    put_halves(ln, L_REDA + w * 128, gemv8(wt, h0, h1, ln.k));
    bar();
    if (w == 0) stsc1(ws, tg(sumN<8>(Lf + L_REDA + lane), T), lane * 4, WS_S4 + (k * 16 + g) * 256);
    STAMP(7);
    FINISH();
    return;
  }
  if (!(light && g < 9) || w > 8) { FINISH(); return; }
  {
    const int j = g;
    float x1 = 0.f, gbv = 0.f;
    if (w == 0) {
      const float xv = bf(p.x[64 * j + col]);
      unsigned a2, b2;
      v4u q0, q1, q2, q3;
      for (unsigned spin = 0;; ++spin) {
        asm volatile("" ::: "memory");
        const int vo = (j * 16 + row) * 256 + c4 * 16;
        a2 = ldsc1(ws, col * 4, WS_SB + j * 256); b2 = ldsc1(ws, col * 4, WS_SB + (27 + j) * 256);
        q0 = ldsc(ws, vo, WS_S4); q1 = ldsc(ws, vo, WS_S4 + 1024); q2 = ldsc(ws, vo, WS_S4 + 2048); q3 = ldsc(ws, vo, WS_S4 + 3072);
        __builtin_amdgcn_sched_barrier(0);
        q0 ^= T; q1 ^= T; q2 ^= T; q3 ^= T; a2 ^= T; b2 ^= T;
        if (allok(a2 | b2 | or4(q0) | or4(q1) | or4(q2) | or4(q3)) || spin > SPIN_MAX) break;
        __builtin_amdgcn_s_sleep(8);
      }
      x1 = rb(xv + rb(asf(a2) * rb(tr4((asf4(q0) + asf4(q1)) + (asf4(q2) + asf4(q3))))));
      gbv = asf(b2);
      pin(x1); pin(gbv);
    }
    STAMP(6);
    {
      v4u y0, y1;
      int vo = (8 * w + row) * 256 + c4 * 16;
      LAS float* rw = Lf + L_REDG + w * 64 + col;
      pin(vo); pin(rw);
      for (unsigned spin = 0;; ++spin) {
        asm volatile("" ::: "memory");
        y0 = ldsc(ws, vo, WS_S5 + j * 72 * 256); y1 = ldsc(ws, vo, WS_S5 + j * 72 * 256 + 1024);
        __builtin_amdgcn_sched_barrier(0);
        y0 ^= T; y1 ^= T;
        if (allok(or4(y0) | or4(y1)) || spin > SPIN_MAX) break;
        __builtin_amdgcn_s_sleep(1);
      }
      *rw = tr4(asf4(y0) + asf4(y1));
    }
    bar();
    if (w == 0) {
      LAS float* r = Lf + L_REDG + col;
      const float y = ((r[0] + r[64]) + (r[128] + r[192])) + ((r[256] + r[320]) + (r[384] + r[448])) + r[512];
      f32x2 o = {x1 + rb(gbv * rb(y)), 0.f};
      p.out[64 * j + col] = (u16)(__builtin_bit_cast(unsigned, __builtin_convertvector(o, bf16x2)) & 0xFFFFu);
      if (j == 0 && lane == 0) __builtin_amdgcn_raw_buffer_store_b32(E + 1u, ws, 0, WS_EPOCH, 16);
    }
    STAMP(7);
    FINISH();
  }
}

}

extern "C" void kernel_launch(void* const* d_in, const int* in_sizes, int n_in, void* d_out, int out_size, void* d_ws, size_t ws_size,
                              hipStream_t stream) {
  Params p;
  p.x = (const u16*)d_in[0]; p.kc = (const u16*)d_in[1]; p.vc = (const u16*)d_in[2]; p.wq = (const u16*)d_in[3]; p.wo = (const u16*)d_in[4];
  p.wg = (const u16*)d_in[6]; p.wu = (const u16*)d_in[7]; p.wd = (const u16*)d_in[8]; p.c = (const u16*)d_in[9]; p.wm = (const u16*)d_in[10];
  p.out = (u16*)d_out; p.ws = (unsigned char*)d_ws;
  fused_decode<<<256, NTHR, 0, stream>>>(p);
}
```

```cpp
#include <hip/hip_runtime.h>
#include <hip/hip_bf16.h>
#include <stdint.h>

#define LDS_SLEEP 1
namespace {

typedef unsigned short u16;
typedef unsigned v4u __attribute__((ext_vector_type(4)));
typedef unsigned v2u __attribute__((ext_vector_type(2)));
typedef float f32x4 __attribute__((ext_vector_type(4)));
typedef float f32x2 __attribute__((ext_vector_type(2)));
typedef __bf16 bf16x8 __attribute__((ext_vector_type(8)));
typedef __bf16 bf16x2 __attribute__((ext_vector_type(2)));
typedef __amdgpu_buffer_rsrc_t rsrc_t;
#define LAS __attribute__((address_space(3)))

constexpr int D = 576, SK = 65536, WIN = 4096, FF = 4608;
constexpr int NTHR = 768;
constexpr int WS_EPOCH = 0;
constexpr int WS_S1 = 4096;
constexpr int WS_SQ = WS_S1 + 72 * 256;
constexpr int WS_SB = WS_SQ + 64 * 256;
constexpr int WS_S4 = WS_SB + 36 * 256;
constexpr int WS_S5 = WS_S4 + 144 * 256;
constexpr int WS_S3 = WS_S5 + 648 * 256;
constexpr int S3_STRIDE = 1280;
constexpr int WS_END = WS_S3 + 176 * S3_STRIDE;
constexpr int WS_STAMP = 1 << 20;
constexpr unsigned SPIN_MAX = 1u << 14;
#define POLL_SLEEP 2
#define PRE_WAIT 2
#define WO_POS 1
#define DELAY_A 0
#define DELAY_F 0

struct Params {
  const u16 *x, *kc, *vc, *wq, *wo, *wg, *wu, *wd, *c, *wm;
  u16* out;
  unsigned char* ws;
};

constexpr int SH_STRIDE = 160;
constexpr int L_SH = 0;
constexpr int L_REDA = L_SH + 12 * SH_STRIDE;
constexpr int L_REDG = L_REDA + 1536;
constexpr int L_ST = L_REDG + 1152;
constexpr int L_Q = L_ST + 64;
constexpr int L_MW = L_Q + 128;
constexpr int L_LW = L_MW + 32;
constexpr int L_CNT = L_LW + 32;
constexpr int L_O = L_CNT + 16;
constexpr int L_END = L_O + 12 * 256;

__device__ __forceinline__ rsrc_t mk(const void* p, unsigned bytes) {
  return __builtin_amdgcn_make_buffer_rsrc((void*)p, 0, (int)bytes, 0x00020000);
}
__device__ __forceinline__ v4u ldnt(rsrc_t r, int v, int s) { return __builtin_amdgcn_raw_buffer_load_b128(r, v, s, 2); }
__device__ __forceinline__ v4u ldsc(rsrc_t r, int v, int s) { return __builtin_amdgcn_raw_buffer_load_b128(r, v, s, 16); }
__device__ __forceinline__ v2u ldsc2(rsrc_t r, int v, int s) { return __builtin_amdgcn_raw_buffer_load_b64(r, v, s, 16); }
__device__ __forceinline__ unsigned ldsc1(rsrc_t r, int v, int s) { return __builtin_amdgcn_raw_buffer_load_b32(r, v, s, 16); }
__device__ __forceinline__ void stsc1(rsrc_t r, unsigned x, int v, int s) { __builtin_amdgcn_raw_buffer_store_b32(x, r, v, s, 16); }
__device__ __forceinline__ void stsc4(rsrc_t r, v4u x, int v, int s) { __builtin_amdgcn_raw_buffer_store_b128(x, r, v, s, 16); }
__device__ __forceinline__ float asf(unsigned x) { return __uint_as_float(x); }
__device__ __forceinline__ unsigned asu(float x) { return __float_as_uint(x); }
template <class V> __device__ __forceinline__ void pin(V& x) { asm volatile("" : "+v"(x)); }
__device__ __forceinline__ unsigned rep(float x) {
  f32x2 v = {x, x};
  return __builtin_bit_cast(unsigned, __builtin_convertvector(v, bf16x2));
}
__device__ __forceinline__ float rb(float x) { return asf(rep(x) & 0xFFFF0000u); }
__device__ __forceinline__ float bf(u16 x) { return asf((unsigned)x << 16); }
__device__ __forceinline__ unsigned tg(float x, unsigned T) { return (asu(x) & 0xFFFFFF00u) | T; }
__device__ __forceinline__ v4u tg4(f32x4 y, unsigned T) { return v4u{tg(y.x, T), tg(y.y, T), tg(y.z, T), tg(y.w, T)}; }
__device__ __forceinline__ unsigned or4(v4u d) { return (d.x | d.y) | (d.z | d.w); }
__device__ __forceinline__ bool allok(unsigned bad) { return __all((bad & 0xFFu) == 0u); }
template <int C> __device__ __forceinline__ float dpp(float x) {
  return asf((unsigned)__builtin_amdgcn_update_dpp(0, (int)asu(x), C, 0xF, 0xF, true));
}
__device__ __forceinline__ float xrow_sum(float x) {
  auto s = __builtin_amdgcn_permlane16_swap(asu(x), asu(x), false, false);
  x = asf(s[0]) + asf(s[1]);
  auto t = __builtin_amdgcn_permlane32_swap(asu(x), asu(x), false, false);
  return asf(t[0]) + asf(t[1]);
}
__device__ __forceinline__ float xrow_max(float x) {
  auto s = __builtin_amdgcn_permlane16_swap(asu(x), asu(x), false, false);
  x = __builtin_amdgcn_fmed3f(asf(s[0]), asf(s[1]), INFINITY);
  auto t = __builtin_amdgcn_permlane32_swap(asu(x), asu(x), false, false);
  return __builtin_amdgcn_fmed3f(asf(t[0]), asf(t[1]), INFINITY);
}
__device__ __forceinline__ float tr4(f32x4 v) {
  auto a = __builtin_amdgcn_permlane16_swap(asu(v.x), asu(v.y), false, false);
  auto b = __builtin_amdgcn_permlane16_swap(asu(v.z), asu(v.w), false, false);
  const float s01 = asf(a[0]) + asf(a[1]), s23 = asf(b[0]) + asf(b[1]);
  auto c = __builtin_amdgcn_permlane32_swap(asu(s01), asu(s23), false, false);
  return asf(c[0]) + asf(c[1]);
}
__device__ __forceinline__ float wave_sum2(float x, float y) {
  x += dpp<0xB1>(x); y += dpp<0xB1>(y);
  x += dpp<0x4E>(x); y += dpp<0x4E>(y);
  x += dpp<0x141>(x); y += dpp<0x141>(y);
  x += dpp<0x140>(x); y += dpp<0x140>(y);
  auto a = __builtin_amdgcn_permlane16_swap(asu(x), asu(y), false, false);
  const float s = asf(a[0]) + asf(a[1]);
  auto c = __builtin_amdgcn_permlane32_swap(asu(s), asu(s), false, false);
  return asf(c[0]) + asf(c[1]);
}
__device__ __forceinline__ f32x4 asf4(v4u d) { return __builtin_bit_cast(f32x4, d); }
__device__ __forceinline__ f32x4 mfma(v4u a, v4u b, f32x4 c) {
  return __builtin_amdgcn_mfma_f32_16x16x32_bf16(__builtin_bit_cast(bf16x8, a), __builtin_bit_cast(bf16x8, b), c, 0, 0, 0);
}
struct Mask { unsigned m0, m1, m2, m3; };
__device__ __forceinline__ v4u diag(unsigned hh, const Mask& k) { return v4u{hh & k.m0, hh & k.m1, hh & k.m2, hh & k.m3}; }
__device__ __forceinline__ void bar() {
  asm volatile("s_waitcnt lgkmcnt(0)" ::: "memory");
  __builtin_amdgcn_s_barrier();
  asm volatile("" ::: "memory");
}
__device__ __forceinline__ void rawbar() {
  __builtin_amdgcn_sched_barrier(0);
  __builtin_amdgcn_s_barrier();
  __builtin_amdgcn_sched_barrier(0);
}
__device__ __forceinline__ float silu(float v) { return v * __builtin_amdgcn_rcpf(1.f + __expf(-v)); }
__device__ __forceinline__ float fold1(float x) {
  auto t = __builtin_amdgcn_permlane32_swap(asu(x), asu(x), false, false);
  return asf(t[0]) + dpp<0x128>(asf(t[1]));
}
__device__ __forceinline__ f32x4 fold(f32x4 a) { return f32x4{fold1(a.x), fold1(a.y), fold1(a.z), fold1(a.w)}; }
__device__ __forceinline__ float fold2_1(float a, float b) {
  auto t = __builtin_amdgcn_permlane32_swap(asu(a), asu(b), false, false);
  return asf(t[0]) + dpp<0x128>(asf(t[1]));
}

#define STAMP(i) do {} while (0)
#define FINISH() do {} while (0)

struct Lane {
  Mask k;
  LAS unsigned* shw;
  LAS unsigned* shr;
  LAS float* put;
  bool putv;
};
__device__ __forceinline__ void shuf(LAS unsigned* wr, LAS unsigned* rd, float v, v4u& h0, v4u& h1) {
  *wr = rep(v);
  asm volatile("" ::: "memory");
  h0 = *(LAS v4u*)rd;
  h1 = *(LAS v4u*)(rd + 4);
}
__device__ __forceinline__ f32x4 gemv8(const v4u (&w)[8], v4u h0, v4u h1, const Mask& k) {
  f32x4 a = {0.f, 0.f, 0.f, 0.f}, b = {0.f, 0.f, 0.f, 0.f};
  const v4u a0 = diag(h0.x, k), a1 = diag(h0.y, k), a2 = diag(h0.z, k), a3 = diag(h0.w, k);
  a = mfma(a0, w[0], a); b = mfma(a1, w[1], b);
  const v4u a4 = diag(h1.x, k), a5 = diag(h1.y, k);
  a = mfma(a2, w[2], a); b = mfma(a3, w[3], b);
  const v4u a6 = diag(h1.z, k), a7 = diag(h1.w, k);
  a = mfma(a4, w[4], a); b = mfma(a5, w[5], b);
  a = mfma(a6, w[6], a); b = mfma(a7, w[7], b);
  return a + b;
}
__device__ __forceinline__ f32x2 ln_sums(LAS float* st) {
  const f32x4 a = *(LAS f32x4*)(st), b = *(LAS f32x4*)(st + 4), c = *(LAS f32x4*)(st + 8), d = *(LAS f32x4*)(st + 12);
  const f32x2 e = *(LAS f32x2*)(st + 16);
  const f32x4 t = (a + b) + (c + d);
  return f32x2{t.x + t.z + e.x, t.y + t.w + e.y};
}
template <int N> __device__ __forceinline__ float sumN(LAS float* red) {
  float a = 0.f, b = 0.f;
#pragma unroll
  for (int i = 0; i < N; i += 2) { a += red[i * 64]; b += red[i * 64 + 64]; }
  return a + b;
}
__device__ __forceinline__ void loads8(v4u (&w)[8], rsrc_t r, int stride, int lane, int base) {
  const int vo = (lane >> 3) * stride + (lane & 7) * 16;
#pragma unroll
  for (int s = 0; s < 8; ++s) w[s] = ldnt(r, vo, base + 8 * s * stride);
}
struct Tile18 { v4u w[18]; v2u c; };
__device__ __forceinline__ void tile18_load(Tile18& t, const u16* c, rsrc_t rwm, int ks, int cb, int lane) {
  t.c = *(const v2u*)(c + 144 * ks + 4 * (lane < 36 ? lane : 35));
  const int vo = (lane >> 3) * 6912 + (lane & 7) * 16;
#pragma unroll
  for (int s = 0; s < 18; ++s) t.w[s] = ldnt(rwm, vo, (144 * ks + 8 * s) * 6912 + cb);
}
__device__ __forceinline__ f32x4 tile18_mac(const Tile18& t, LAS unsigned* sh, int lane, const Mask& k) {
  if (lane < 36) {
    LAS unsigned* d = sh + (4 * (lane & 1)) * 20 + (lane >> 1);
    d[0] = rep(silu(asf(t.c.x << 16))); d[20] = rep(silu(asf(t.c.x & 0xFFFF0000u)));
    d[40] = rep(silu(asf(t.c.y << 16))); d[60] = rep(silu(asf(t.c.y & 0xFFFF0000u)));
  }
  asm volatile("" ::: "memory");
  LAS unsigned* s = sh + (lane >> 3) * 20;
  const v4u h0 = *(LAS v4u*)s, h1 = *(LAS v4u*)(s + 4), h2 = *(LAS v4u*)(s + 8), h3 = *(LAS v4u*)(s + 12);
  const v2u h4 = *(LAS v2u*)(s + 16);
  f32x4 a = {0.f, 0.f, 0.f, 0.f}, b = {0.f, 0.f, 0.f, 0.f};
  a = mfma(diag(h0.x, k), t.w[0], a); b = mfma(diag(h0.y, k), t.w[1], b); a = mfma(diag(h0.z, k), t.w[2], a); b = mfma(diag(h0.w, k), t.w[3], b);
  a = mfma(diag(h1.x, k), t.w[4], a); b = mfma(diag(h1.y, k), t.w[5], b); a = mfma(diag(h1.z, k), t.w[6], a); b = mfma(diag(h1.w, k), t.w[7], b);
  a = mfma(diag(h2.x, k), t.w[8], a); b = mfma(diag(h2.y, k), t.w[9], b); a = mfma(diag(h2.z, k), t.w[10], a); b = mfma(diag(h2.w, k), t.w[11], b);
  a = mfma(diag(h3.x, k), t.w[12], a); b = mfma(diag(h3.y, k), t.w[13], b); a = mfma(diag(h3.z, k), t.w[14], a); b = mfma(diag(h3.w, k), t.w[15], b);
  a = mfma(diag(h4.x, k), t.w[16], a); b = mfma(diag(h4.y, k), t.w[17], b);
  return a + b;
}
__device__ __forceinline__ void lds_arrive(LAS unsigned* c) {
  if ((threadIdx.x & 63) == 0) asm volatile("ds_add_u32 %0, %1" ::"v"(c), "v"(1u) : "memory");
  asm volatile("" ::: "memory");
}
__device__ __forceinline__ float vmax(float a, float b) { return __builtin_amdgcn_fmed3f(a, b, INFINITY); }
__device__ __forceinline__ float max12(LAS float* m) {
  const f32x4 b0 = *(LAS f32x4*)m, b1 = *(LAS f32x4*)(m + 4), b2 = *(LAS f32x4*)(m + 8);
  return vmax(vmax(vmax(vmax(b0.x, b0.y), vmax(b0.z, b0.w)), vmax(vmax(b1.x, b1.y), vmax(b1.z, b1.w))), vmax(vmax(b2.x, b2.y), vmax(b2.z, b2.w)));
}
__device__ __forceinline__ unsigned lds_inc(LAS unsigned* c) {
  asm volatile("" ::: "memory");
  unsigned old = 0;
  if ((threadIdx.x & 63) == 0) old = __hip_atomic_fetch_add(c, 1u, __ATOMIC_RELAXED, __HIP_MEMORY_SCOPE_WORKGROUP);
  old = (unsigned)__builtin_amdgcn_readfirstlane((int)old);
  asm volatile("" ::: "memory");
  return old;
}
__device__ __forceinline__ void lds_wait_sleep(LAS unsigned* c, unsigned need) {
  for (unsigned spin = 0; *(volatile LAS unsigned*)c < need && spin < (1u << 20); ++spin) __builtin_amdgcn_s_sleep(LDS_SLEEP);
  asm volatile("" ::: "memory");
}
__device__ __forceinline__ void lds_wait(LAS unsigned* c, unsigned need) {
  for (unsigned spin = 0; *(volatile LAS unsigned*)c < need && spin < (1u << 20); ++spin) {}
  asm volatile("" ::: "memory");
}
__device__ __forceinline__ void make_lane(Lane& ln, int lane, LAS unsigned* sh) {
  const unsigned mm = (lane & 1) ? 0xFFFF0000u : 0x0000FFFFu;
  const int d = (lane & 7) >> 1, c = lane & 15, g = lane >> 4;
  ln.k.m0 = d == 0 ? mm : 0u; ln.k.m1 = d == 1 ? mm : 0u; ln.k.m2 = d == 2 ? mm : 0u; ln.k.m3 = d == 3 ? mm : 0u;
  ln.shw = sh + 32 * (c & 1) + 8 * g + (c >> 1);
  ln.shr = sh + (lane >> 3) * 8;
  ln.put = (LAS float*)0 + (c >> 3) * 64 + 8 * (c & 7) + 4 * (g & 1);
  ln.putv = (c >> 3) == (g >> 1);
  pin(ln.k.m0); pin(ln.k.m1); pin(ln.k.m2); pin(ln.k.m3); pin(ln.shw); pin(ln.shr); pin(ln.put);
}
__device__ __forceinline__ void put_halves(const Lane& ln, int base_dw, f32x4 a) {
  if (ln.putv) *(LAS f32x4*)(ln.put + base_dw) = a;
}
__device__ __forceinline__ void pub_factors(LAS float* Lf, LAS unsigned* cnt, unsigned need, int wp, int row, float& f0, float& f1, float& f2) {
  lds_wait(cnt, need);
  LAS float* mw = Lf + L_MW + (wp >> 1) * 16;
  const float M = max12(mw);
  f0 = __expf(mw[row] - M); f1 = __expf(mw[row + 4] - M); f2 = __expf(mw[row + 8] - M);
  pin(f0); pin(f1); pin(f2);
}
__device__ __forceinline__ void modb_finish(const Tile18& t, LAS unsigned* sh, const Lane& ln, LAS float* Lf, int red_dw, LAS unsigned* cnt, int ks, int lane,
                                            rsrc_t ws, int tile, unsigned T) {
  put_halves(ln, red_dw + ks * 128, tile18_mac(t, sh, lane, ln.k));
  if (lds_inc(cnt) == 3u) stsc1(ws, tg(rb(sumN<8>(Lf + red_dw + lane)), T), lane * 4, WS_SB + tile * 256);
}

__global__ void __launch_bounds__(NTHR, 3) fused_decode(Params p) {
  __shared__ __attribute__((aligned(16))) unsigned smem[L_END];
  LAS unsigned* L = (LAS unsigned*)smem;
  LAS float* Lf = (LAS float*)smem;
  const int b = blockIdx.x, xcd = b & 7, slot = b >> 3;
  const int lane = threadIdx.x & 63, w = __builtin_amdgcn_readfirstlane(threadIdx.x >> 6);
  const int c4 = lane & 15, row = lane >> 4;
  const int col = 4 * c4 + row;
  STAMP(0);
  const unsigned long long t_start = __builtin_amdgcn_s_memrealtime();
  const rsrc_t ws = mk(p.ws, WS_END);
  const unsigned Ev = __builtin_amdgcn_raw_buffer_load_b32(ws, 0, WS_EPOCH, 16);
  __builtin_amdgcn_sched_barrier(0);
#define GET_TAG() const unsigned E = (unsigned)__builtin_amdgcn_readfirstlane((int)Ev); const unsigned T = (E + 1u) & 0xFFu; (void)E
  LAS unsigned* sh = L + L_SH + w * SH_STRIDE;
  const rsrc_t rwm = mk(p.wm, D * 6 * D * 2);
  const int lbase = (int)(size_t)(LAS void*)smem;
  Lane ln;

  const int rgate = (int)((size_t)p.wg >> 7), xstar = (3 - rgate) & 7;
  if (slot <= 9) {
    if (w >= 10) return;
    const bool spare = slot == 9;
    const int f = xcd * 9 + slot;
    if (w == 9) {
      __builtin_amdgcn_s_setprio(3);
      if (lane < 4) L[L_CNT + lane] = 0u;
      if (spare) { bar(); return; }
      Tile18 t;
      tile18_load(t, p.c, rwm, f / 18, (f % 18) * 128, lane);
      bar();
      make_lane(ln, lane, sh);
      GET_TAG();
      const f32x4 a = fold(tile18_mac(t, sh, lane, ln.k));
      if (c4 < 8 && row < 2) stsc4(ws, tg4(a, T), (8 * c4 + 4 * row) * 4, WS_S1 + f * 256);
      STAMP(1);
      FINISH();
      return;
    }
    const int j = w;
    const int s0 = (3 - rgate - xcd) & 7, ns = (3 - (int)((size_t)p.wq >> 7)) & 7;
    const int rho = slot - (slot > s0 ? 1 : 0);
    const int kind = spare ? 1 : (slot == s0 || (slot == 8 && s0 == 0)) ? 0 : slot == 8 ? 2 : 1;
    const bool hasq = kind == 1;
    const int n = xcd * 8 + (spare ? ns : rho + (rho >= ns ? 1 : 0));
    const int mtile = 16 + xcd - (xcd > xstar ? 1 : 0);
    const u16 xr = p.x[64 * j + col], x0r = p.x[0], cr = p.c[64 * j + col];
    rawbar();
    v4u wt[8], wg[8], wu[8], wd[8];
    const rsrc_t rg = mk(p.wg, D * FF * 2), ru = mk(p.wu, D * FF * 2), rd = mk(p.wd, FF * D * 2);
    {
      const int st = hasq ? 8192 : 6912;
      loads8(wt, mk(hasq ? (const void*)p.wq : (const void*)p.wm, kind == 0 ? 0 : hasq ? D * 4096 * 2 : D * 6 * D * 2), st, lane,
             64 * j * st + (hasq ? n * 128 : 2304 + mtile * 128));
    }
    __builtin_amdgcn_sched_barrier(0);
    make_lane(ln, lane, sh);
    ln.put += lbase / 4;
    GET_TAG();
    const float x0 = bf(x0r);
    float xv = bf(xr);
    {
      const float d = xv - x0;
      const float s = wave_sum2(d, d * d);
      if ((lane & 47) == 0) Lf[L_ST + 2 * j + (lane >> 4)] = s;
    }
    lds_arrive(L + L_CNT);
    lds_wait(L + L_CNT, 9u);
    float mean1, lnv;
    {
      const f32x2 s = ln_sums(Lf + L_ST);
      const float dm = s.x * (1.f / D);
      mean1 = x0 + dm;
      const float var = fmaxf(s.y * (1.f / D) - dm * dm, 0.f);
      const float den = rb(__builtin_amdgcn_sqrtf(rb(rb(var) + 1e-5f)));
      lnv = rb(rb(xv - rb(mean1)) / den);
      pin(lnv); pin(mean1); pin(xv);
    }
    STAMP(1);
    if (kind != 0) {
      float h;
      if (hasq) {
        int o1 = (row * 18 + j) * 256 + c4 * 16;
        pin(o1);
        __builtin_amdgcn_s_waitcnt(0x0F70 | PRE_WAIT);
        v4u sa, ca;
        for (unsigned spin = 0;; ++spin) {
          asm volatile("" ::: "memory");
          sa = ldsc(ws, o1, WS_S1); ca = ldsc(ws, o1, WS_S1 + 9 * 256);
          __builtin_amdgcn_sched_barrier(0);
          sa ^= T; ca ^= T;
          if (allok(or4(sa) | or4(ca)) || spin > SPIN_MAX) break;
          __builtin_amdgcn_s_sleep(2);
        }
        h = rb(lnv * rb(1.f + rb(tr4(asf4(ca))))) + rb(tr4(asf4(sa)));
      } else {
        h = silu(bf(cr));
      }
      STAMP(2);
      v4u h0, h1;
      shuf(ln.shw, ln.shr, h, h0, h1);
      put_halves(ln, L_REDA + j * 128, gemv8(wt, h0, h1, ln.k));
      bar();
      if (j == 0) stsc1(ws, tg(rb(sumN<18>(Lf + L_REDA + lane)), T), lane * 4, hasq ? WS_SQ + n * 256 : WS_SB + mtile * 256);
      rawbar();
    }
    if (spare) {
      STAMP(3);
      if (w == 8 || (w >= 4 && xcd >= 5)) { FINISH(); return; }
      const int tile = (w < 4 ? 23 : 31) + xcd;
      Tile18 t;
      tile18_load(t, p.c, rwm, w & 3, 2304 + tile * 128, lane);
      __builtin_amdgcn_sched_barrier(0);
      modb_finish(t, sh, ln, Lf, L_REDG + (w >> 2) * 512, L + L_CNT + 1 + (w >> 2), w & 3, lane, ws, tile, T);
      STAMP(7);
      FINISH();
      return;
    }
    if (DELAY_F) { if (hasq) while (__builtin_amdgcn_s_memrealtime() - t_start < DELAY_F) __builtin_amdgcn_s_sleep(1); }
    STAMP(3);
    loads8(wg, rg, FF * 2, lane, 64 * j * FF * 2 + f * 128);
    loads8(wu, ru, FF * 2, lane, 64 * j * FF * 2 + f * 128);
    loads8(wd, rd, 1152, lane, 64 * f * 1152 + j * 128);
    __builtin_amdgcn_sched_barrier(0);
    int o4 = (j * 16 + row) * 256 + c4 * 16, ob = j * 256 + col * 4, o5 = (8 * c4 + 4 * row) * 4;
    LAS float* redg_w = Lf + L_REDG + j * 128 + (row >> 1) * 64 + 8 * c4 + 4 * (row & 1);
    LAS float* redg_r = Lf + L_REDG + lane;
    LAS unsigned* shw2 = sh + (lane & 7) * 8 + (lane >> 3);
    pin(o4); pin(ob); pin(o5); pin(redg_w); pin(redg_r); pin(shw2);
    asm volatile("s_waitcnt vmcnt(0)" ::: "memory");
    STAMP(8);
    rawbar();
    STAMP(4);
    float x1, sb, cb;
    {
      unsigned a, s2, c2;
      v4u q0, q1, q2, q3;
      for (unsigned spin = 0;; ++spin) {
        asm volatile("" ::: "memory");
        q0 = ldsc(ws, o4, WS_S4); q1 = ldsc(ws, o4, WS_S4 + 1024); q2 = ldsc(ws, o4, WS_S4 + 2048); q3 = ldsc(ws, o4, WS_S4 + 3072);
        a = ldsc1(ws, ob, WS_SB); s2 = ldsc1(ws, ob, WS_SB + 9 * 256); c2 = ldsc1(ws, ob, WS_SB + 18 * 256);
        __builtin_amdgcn_sched_barrier(0);
        q0 ^= T; q1 ^= T; q2 ^= T; q3 ^= T; a ^= T; s2 ^= T; c2 ^= T;
        if (allok(or4(q0) | or4(q1) | or4(q2) | or4(q3) | a | s2 | c2) || spin > SPIN_MAX) break;
        __builtin_amdgcn_s_sleep(POLL_SLEEP);
      }
      x1 = rb(xv + rb(asf(a) * rb(tr4((asf4(q0) + asf4(q1)) + (asf4(q2) + asf4(q3))))));
      sb = asf(s2); cb = rb(1.f + asf(c2));
    }
    STAMP(5);
    {
      const float d = x1 - mean1;
      const float s = wave_sum2(d, d * d);
      if ((lane & 47) == 0) Lf[L_ST + 32 + 2 * j + (lane >> 4)] = s;
    }
    bar();
    {
      const f32x2 s = ln_sums(Lf + L_ST + 32);
      const float dm = s.x * (1.f / D);
      const float var = fmaxf(s.y * (1.f / D) - dm * dm, 0.f);
      const float den = rb(__builtin_amdgcn_sqrtf(rb(rb(var) + 1e-5f)));
      const float h2 = rb(rb(rb(x1 - rb(mean1 + dm)) * __builtin_amdgcn_rcpf(den)) * cb) + sb;
      v4u h0, h1;
      shuf(ln.shw, ln.shr, h2, h0, h1);
      const f32x4 g = gemv8(wg, h0, h1, ln.k), u = gemv8(wu, h0, h1, ln.k);
      const f32x4 t = {fold2_1(g.x, u.x), fold2_1(g.y, u.y), fold2_1(g.z, u.z), fold2_1(g.w, u.w)};
      if (c4 < 8) *(LAS f32x4*)redg_w = t;
    }
    bar();
    STAMP(6);
    {
      float g = 0.f, u = 0.f, g2 = 0.f, u2 = 0.f;
#pragma unroll
      for (int i = 0; i < 8; i += 2) {
        g += redg_r[i * 128]; u += redg_r[i * 128 + 64];
        g2 += redg_r[i * 128 + 128]; u2 += redg_r[i * 128 + 192];
      }
      g = rb((g + redg_r[1024]) + g2); u = rb((u + redg_r[1088]) + u2);
      const float z = 1.5957691216057308f * (g + 0.044715f * g * g * g);
      v4u h0, h1;
      shuf(shw2, ln.shr, rb(g * __builtin_amdgcn_rcpf(1.f + __expf(-z))) * u, h0, h1);
      const f32x4 y = fold(gemv8(wd, h0, h1, ln.k));
      if (c4 < 8 && row < 2) stsc4(ws, tg4(y, T), o5, WS_S5 + (j * 72 + f) * 256);
    }
    STAMP(7);
    FINISH();
    return;
  }

  const int a = slot - 10, g = 2 * xcd + (a >= 11 ? 1 : 0), k = a >= 11 ? a - 11 : a;
  const bool light = k == 10;
  if (DELAY_A) { while (__builtin_amdgcn_s_memrealtime() - t_start < DELAY_A) __builtin_amdgcn_s_sleep(1); }
  const int s3 = WS_S3 + (g * 11 + k) * S3_STRIDE;
  unsigned E = 0, T = 0;
  float f0 = 0.f, f1 = 0.f, f2 = 0.f;
  v4u wt[8];
  const rsrc_t rwo = mk(p.wo, (k < 9 && w < 4) ? 4096 * D * 2 : 0);
  if (light && w >= 8) {
    Tile18 t;
    tile18_load(t, p.c, rwm, w - 8, 2304 + g * 128, lane);
    if (w == 8 && lane < 32) Lf[L_MW + lane] = -INFINITY;
    if (w == 9 && lane < 32) Lf[L_LW + lane] = 0.f;
    if (w == 10 && lane < 4) L[L_CNT + lane] = 0u;
    bar();
    make_lane(ln, lane, sh);
    E = (unsigned)__builtin_amdgcn_readfirstlane((int)Ev); T = (E + 1u) & 0xFFu;
    modb_finish(t, sh, ln, Lf, L_REDA, L + L_CNT + 1, w - 8, lane, ws, g, T);
    STAMP(1);
  } else {
    const size_t kvoff = ((size_t)g * SK + (SK - WIN) + k * 384 + w * 32) * 128;
    const rsrc_t rk = mk(p.kc + kvoff, 8192), rv = mk(p.vc + kvoff, 8192);
    v4u kk[8], vv[8];
    if (WO_POS == 0) loads8(wt, rwo, 1152, lane, (256 * g + 64 * (w & 3)) * 1152 + k * 128);
    const int kvo = (lane & 7) * 256 + ((lane >> 3) & 1) * 64 + row * 16, vvo = row * 2048 + c4 * 16;
#pragma unroll
    for (int t = 0; t < 4; ++t) {
      kk[2 * t] = ldnt(rk, kvo, t * 2048);
      kk[2 * t + 1] = ldnt(rk, kvo + 128, t * 2048);
    }
    if (!light) {
      if (w == 8 && lane < 32) Lf[L_MW + lane] = -INFINITY;
      if (w == 9 && lane < 32) Lf[L_LW + lane] = 0.f;
      if (w == 10 && lane < 4) L[L_CNT + lane] = 0u;
    }
    __builtin_amdgcn_sched_barrier(0);
    bar();
    if (WO_POS == 1) loads8(wt, rwo, 1152, lane, (256 * g + 64 * (w & 3)) * 1152 + k * 128);
#pragma unroll
    for (int s = 0; s < 8; ++s) vv[s] = ldnt(rv, vvo + s * 256, 0);
    __builtin_amdgcn_sched_barrier(0);
    make_lane(ln, lane, sh);
    ln.put += lbase / 4;
    E = (unsigned)__builtin_amdgcn_readfirstlane((int)Ev); T = (E + 1u) & 0xFFu;
    LAS unsigned* qrd = L + L_Q + (lane & 1) * 64 + ((lane >> 1) & 1) * 16 + row * 4;
    LAS float* mwr = Lf + L_MW + ((lane >> 3) & 1) * 16;
    LAS float* owr = Lf + L_O + w * 256 + (row >> 1) * 128 + 8 * c4 + 4 * (row & 1);
    pin(qrd); pin(mwr); pin(owr);
    STAMP(1);
    if (w == 0) {
      __builtin_amdgcn_s_setprio(3);
      const int oq = lane * 16, sq = WS_SQ + g * 1024;
      v4u q;
      for (unsigned spin = 0;; ++spin) {
        asm volatile("" ::: "memory");
        q = ldsc(ws, oq, sq) ^ T;
        if (allok(or4(q)) || spin > SPIN_MAX) break;
        __builtin_amdgcn_s_sleep(2);
      }
      *(LAS v2u*)(L + L_Q + lane * 2) = v2u{(q.x >> 16) | q.y, (q.z >> 16) | q.w};
      lds_arrive(L + L_CNT + 3);
      __builtin_amdgcn_s_setprio(0);
    }
    lds_wait_sleep(L + L_CNT + 3, 1u);
    STAMP(2);
    {
      float sc = 0.f;
      const v4u q0 = *(LAS v4u*)qrd, q1 = *(LAS v4u*)(qrd + 32);
#pragma unroll
      for (int t = 0; t < 4; ++t) {
        f32x4 d = {0.f, 0.f, 0.f, 0.f};
        d = mfma(q0, kk[2 * t], d);
        d = mfma(q1, kk[2 * t + 1], d);
        const float h0 = d.x + dpp<0x128>(d.z), h1 = d.w + dpp<0x128>(d.y);
        const float v = c4 < 8 ? h0 : h1;
        sc = row == t ? v : sc;
      }
      sc = rb(rb(sc) * 0.08838834764831845f);
      float m = sc;
      m = vmax(m, dpp<0xB1>(m)); m = vmax(m, dpp<0x4E>(m)); m = vmax(m, dpp<0x141>(m));
      m = xrow_max(m);
      const float pe = __expf(sc - m);
      v4u h0, h1;
      shuf(sh + lane, ln.shr, pe, h0, h1);
      float l = pe;
      l += dpp<0xB1>(l); l += dpp<0x4E>(l); l += dpp<0x141>(l);
      l = xrow_sum(l);
      if ((lane & 55) == 0) { mwr[w] = m; mwr[32 + w] = l; }
      lds_arrive(L + L_CNT + 2);
      STAMP(3);
      const v4u A0 = diag(h0.x, ln.k), A1 = diag(h0.y, ln.k), A2 = diag(h0.z, ln.k), A3 = diag(h0.w, ln.k);
      const v4u A4 = diag(h1.x, ln.k), A5 = diag(h1.y, ln.k), A6 = diag(h1.z, ln.k), A7 = diag(h1.w, ln.k);
      if (w >= 8) pub_factors(Lf, L + L_CNT + 2, 12u, w - 8, row, f0, f1, f2);
      __builtin_amdgcn_sched_barrier(0);
      f32x4 o0 = {0.f, 0.f, 0.f, 0.f}, o1 = {0.f, 0.f, 0.f, 0.f};
      o0 = mfma(A0, vv[0], o0); o1 = mfma(A1, vv[1], o1); o0 = mfma(A2, vv[2], o0); o1 = mfma(A3, vv[3], o1);
      o0 = mfma(A4, vv[4], o0); o1 = mfma(A5, vv[5], o1); o0 = mfma(A6, vv[6], o0); o1 = mfma(A7, vv[7], o1);
      *(LAS f32x4*)owr = o0 + o1;
      lds_arrive(L + L_CNT);
    }
    STAMP(4);
    if (w >= 7) __builtin_amdgcn_s_setprio(3);
    if (w == 7) {
      lds_wait_sleep(L + L_CNT + 2, light ? 8u : 12u);
      LAS float* mw = Lf + L_MW + (lane & 1) * 16;
      const f32x4 b0 = *(LAS f32x4*)mw, b1 = *(LAS f32x4*)(mw + 4), b2 = *(LAS f32x4*)(mw + 8);
      const f32x4 a0 = *(LAS f32x4*)(mw + 32), a1 = *(LAS f32x4*)(mw + 36), a2 = *(LAS f32x4*)(mw + 40);
      const float M = max12(mw);
      const float l = ((a0.x * __expf(b0.x - M) + a0.y * __expf(b0.y - M)) + (a0.z * __expf(b0.z - M) + a0.w * __expf(b0.w - M))) +
                      ((a1.x * __expf(b1.x - M) + a1.y * __expf(b1.y - M)) + (a1.z * __expf(b1.z - M) + a1.w * __expf(b1.w - M))) +
                      ((a2.x * __expf(b2.x - M) + a2.y * __expf(b2.y - M)) + (a2.z * __expf(b2.z - M) + a2.w * __expf(b2.w - M)));
      if (lane < 2) __builtin_amdgcn_raw_buffer_store_b64(v2u{tg(M, T), tg(l, T)}, ws, lane * 8, s3 + 1024, 16);
    }
  }
  if (w >= 8) {
    __builtin_amdgcn_s_setprio(3);
    const int wp = w - 8;
    LAS float* o = Lf + L_O + row * 256 + wp * 64 + c4 * 4;
    int so = wp * 256 + col * 4;
    pin(o); pin(so);
    if (light) pub_factors(Lf, L + L_CNT + 2, 8u, wp, row, f0, f1, f2);
    lds_wait(L + L_CNT, light ? 8u : 12u);
    f32x4 v = *(LAS f32x4*)o * f0 + *(LAS f32x4*)(o + 1024) * f1;
    if (!light) v += *(LAS f32x4*)(o + 2048) * f2;
    stsc1(ws, tg(tr4(v), T), so, s3);
    STAMP(5);
    if (!(light && g < 9 && w == 8)) { FINISH(); return; }
  }
  if (k < 9) {
    if (w >= 4) { FINISH(); return; }
    if (WO_POS == 2) loads8(wt, rwo, 1152, lane, (256 * g + 64 * w) * 1152 + k * 128);
    __builtin_amdgcn_sched_barrier(0);
    const int k2 = row + 8 > 10 ? 10 : row + 8;
    const int gb = WS_S3 + g * 11 * S3_STRIDE, hd = (w >> 1) * 8;
    int oa = row * S3_STRIDE + w * 256 + c4 * 16, oc = k2 * S3_STRIDE + w * 256 + c4 * 16;
    int ma_ = row * S3_STRIDE + 1024 + hd, mc_ = k2 * S3_STRIDE + 1024 + hd;
    pin(oa); pin(oc); pin(ma_); pin(mc_);
    lds_wait_sleep(L + L_CNT, 12u);
    v2u m0, m1, m2;
    v4u o0, o1, o2;
    for (unsigned spin = 0;; ++spin) {
      asm volatile("" ::: "memory");
      m0 = ldsc2(ws, ma_, gb); m1 = ldsc2(ws, ma_, gb + 4 * S3_STRIDE); m2 = ldsc2(ws, mc_, gb);
      o0 = ldsc(ws, oa, gb); o1 = ldsc(ws, oa, gb + 4 * S3_STRIDE); o2 = ldsc(ws, oc, gb);
      __builtin_amdgcn_sched_barrier(0);
      m0 ^= T; m1 ^= T; m2 ^= T; o0 ^= T; o1 ^= T; o2 ^= T;
      if (allok((m0.x | m0.y) | (m1.x | m1.y) | (m2.x | m2.y) | or4(o0) | or4(o1) | or4(o2)) || spin > SPIN_MAX) break;
      __builtin_amdgcn_s_sleep(POLL_SLEEP);
    }
    const float ma = asf(m0.x), mb = asf(m1.x), mc = row == 3 ? -INFINITY : asf(m2.x);
    const float MM = xrow_max(vmax(vmax(ma, mb), mc));
    float fa = __expf(ma - MM), fb = __expf(mb - MM), fc = __expf(mc - MM);
    const float inv = __builtin_amdgcn_rcpf(xrow_sum(fa * asf(m0.y) + fb * asf(m1.y) + fc * asf(m2.y)));
    STAMP(6);
    v4u h0, h1;
    shuf(ln.shw, ln.shr, tr4(asf4(o0) * fa + asf4(o1) * fb + asf4(o2) * fc) * inv, h0, h1);
    put_halves(ln, L_REDA + w * 128, gemv8(wt, h0, h1, ln.k));
    bar();
    if (w == 0) stsc1(ws, tg(sumN<8>(Lf + L_REDA + lane), T), lane * 4, WS_S4 + (k * 16 + g) * 256);
    STAMP(7);
    FINISH();
    return;
  }
  if (!(light && g < 9) || w > 8) { FINISH(); return; }
  {
    const int j = g;
    float x1 = 0.f, gbv = 0.f;
    if (w == 0) {
      const float xv = bf(p.x[64 * j + col]);
      unsigned a2, b2;
      v4u q0, q1, q2, q3;
      for (unsigned spin = 0;; ++spin) {
        asm volatile("" ::: "memory");
        const int vo = (j * 16 + row) * 256 + c4 * 16;
        a2 = ldsc1(ws, col * 4, WS_SB + j * 256); b2 = ldsc1(ws, col * 4, WS_SB + (27 + j) * 256);
        q0 = ldsc(ws, vo, WS_S4); q1 = ldsc(ws, vo, WS_S4 + 1024); q2 = ldsc(ws, vo, WS_S4 + 2048); q3 = ldsc(ws, vo, WS_S4 + 3072);
        __builtin_amdgcn_sched_barrier(0);
        q0 ^= T; q1 ^= T; q2 ^= T; q3 ^= T; a2 ^= T; b2 ^= T;
        if (allok(a2 | b2 | or4(q0) | or4(q1) | or4(q2) | or4(q3)) || spin > SPIN_MAX) break;
        __builtin_amdgcn_s_sleep(8);
      }
      x1 = rb(xv + rb(asf(a2) * rb(tr4((asf4(q0) + asf4(q1)) + (asf4(q2) + asf4(q3))))));
      gbv = asf(b2);
      pin(x1); pin(gbv);
    }
    STAMP(6);
    {
      v4u y0, y1;
      int vo = (8 * w + row) * 256 + c4 * 16;
      LAS float* rw = Lf + L_REDG + w * 64 + col;
      pin(vo); pin(rw);
      for (unsigned spin = 0;; ++spin) {
        asm volatile("" ::: "memory");
        y0 = ldsc(ws, vo, WS_S5 + j * 72 * 256); y1 = ldsc(ws, vo, WS_S5 + j * 72 * 256 + 1024);
        __builtin_amdgcn_sched_barrier(0);
        y0 ^= T; y1 ^= T;
        if (allok(or4(y0) | or4(y1)) || spin > SPIN_MAX) break;
        __builtin_amdgcn_s_sleep(1);
      }
      *rw = tr4(asf4(y0) + asf4(y1));
    }
    bar();
    if (w == 0) {
      LAS float* r = Lf + L_REDG + col;
      const float y = ((r[0] + r[64]) + (r[128] + r[192])) + ((r[256] + r[320]) + (r[384] + r[448])) + r[512];
      f32x2 o = {x1 + rb(gbv * rb(y)), 0.f};
      p.out[64 * j + col] = (u16)(__builtin_bit_cast(unsigned, __builtin_convertvector(o, bf16x2)) & 0xFFFFu);
      if (j == 0 && lane == 0) __builtin_amdgcn_raw_buffer_store_b32(E + 1u, ws, 0, WS_EPOCH, 16);
    }
    STAMP(7);
    FINISH();
  }
}

}

extern "C" void kernel_launch(void* const* d_in, const int* in_sizes, int n_in, void* d_out, int out_size, void* d_ws, size_t ws_size,
                              hipStream_t stream) {
  Params p;
  p.x = (const u16*)d_in[0]; p.kc = (const u16*)d_in[1]; p.vc = (const u16*)d_in[2]; p.wq = (const u16*)d_in[3]; p.wo = (const u16*)d_in[4];
  p.wg = (const u16*)d_in[6]; p.wu = (const u16*)d_in[7]; p.wd = (const u16*)d_in[8]; p.c = (const u16*)d_in[9]; p.wm = (const u16*)d_in[10];
  p.out = (u16*)d_out; p.ws = (unsigned char*)d_ws;
  fused_decode<<<256, NTHR, 0, stream>>>(p);
}
```

```cpp
#include <hip/hip_runtime.h>
#include <hip/hip_bf16.h>
#include <stdint.h>

namespace {

typedef unsigned short u16;
typedef unsigned v4u __attribute__((ext_vector_type(4)));
typedef unsigned v2u __attribute__((ext_vector_type(2)));
typedef float f32x4 __attribute__((ext_vector_type(4)));
typedef float f32x2 __attribute__((ext_vector_type(2)));
typedef __bf16 bf16x8 __attribute__((ext_vector_type(8)));
typedef __bf16 bf16x2 __attribute__((ext_vector_type(2)));
typedef __amdgpu_buffer_rsrc_t rsrc_t;
#define LAS __attribute__((address_space(3)))

constexpr int D = 576, SK = 65536, WIN = 4096, FF = 4608;
constexpr int NTHR = 768;
constexpr int WS_EPOCH = 0;
constexpr int WS_S1 = 4096;
constexpr int WS_SQ = WS_S1 + 72 * 256;
constexpr int WS_SB = WS_SQ + 64 * 256;
constexpr int WS_S4 = WS_SB + 36 * 256;
constexpr int WS_S5 = WS_S4 + 144 * 256;
constexpr int WS_S3 = WS_S5 + 648 * 256;
constexpr int S3_STRIDE = 1280;
constexpr int WS_END = WS_S3 + 176 * S3_STRIDE;
constexpr unsigned SPIN_MAX = 1u << 14;

struct Params {
  const u16 *x, *kc, *vc, *wq, *wo, *wg, *wu, *wd, *c, *wm;
  u16* out;
  unsigned char* ws;
};

constexpr int SH_STRIDE = 160;
constexpr int L_SH = 0;
constexpr int L_REDA = L_SH + 12 * SH_STRIDE;
constexpr int L_REDG = L_REDA + 1536;
constexpr int L_ST = L_REDG + 1152;
constexpr int L_Q = L_ST + 64;
constexpr int L_MW = L_Q + 128;
constexpr int L_LW = L_MW + 32;
constexpr int L_CNT = L_LW + 32;
constexpr int L_O = L_CNT + 16;
constexpr int L_END = L_O + 12 * 256;

__device__ __forceinline__ rsrc_t mk(const void* p, unsigned bytes) {
  return __builtin_amdgcn_make_buffer_rsrc((void*)p, 0, (int)bytes, 0x00020000);
}
__device__ __forceinline__ v4u ldnt(rsrc_t r, int v, int s) { return __builtin_amdgcn_raw_buffer_load_b128(r, v, s, 2); }
__device__ __forceinline__ v4u ldsc(rsrc_t r, int v, int s) { return __builtin_amdgcn_raw_buffer_load_b128(r, v, s, 16); }
__device__ __forceinline__ v2u ldsc2(rsrc_t r, int v, int s) { return __builtin_amdgcn_raw_buffer_load_b64(r, v, s, 16); }
__device__ __forceinline__ unsigned ldsc1(rsrc_t r, int v, int s) { return __builtin_amdgcn_raw_buffer_load_b32(r, v, s, 16); }
__device__ __forceinline__ void stsc1(rsrc_t r, unsigned x, int v, int s) { __builtin_amdgcn_raw_buffer_store_b32(x, r, v, s, 16); }
__device__ __forceinline__ void stsc4(rsrc_t r, v4u x, int v, int s) { __builtin_amdgcn_raw_buffer_store_b128(x, r, v, s, 16); }
__device__ __forceinline__ float asf(unsigned x) { return __uint_as_float(x); }
__device__ __forceinline__ unsigned asu(float x) { return __float_as_uint(x); }
template <class V> __device__ __forceinline__ void pin(V& x) { asm volatile("" : "+v"(x)); }
__device__ __forceinline__ unsigned rep(float x) {
  f32x2 v = {x, x};
  return __builtin_bit_cast(unsigned, __builtin_convertvector(v, bf16x2));
}
__device__ __forceinline__ float rb(float x) { return asf(rep(x) & 0xFFFF0000u); }
__device__ __forceinline__ float bf(u16 x) { return asf((unsigned)x << 16); }
__device__ __forceinline__ unsigned tg(float x, unsigned T) { return (asu(x) & 0xFFFFFF00u) | T; }
__device__ __forceinline__ unsigned tgr(float x, unsigned T) { return ((asu(x) + 0x80u) & 0xFFFFFF00u) | T; }
__device__ __forceinline__ v4u tg4(f32x4 y, unsigned T) { return v4u{tg(y.x, T), tg(y.y, T), tg(y.z, T), tg(y.w, T)}; }
__device__ __forceinline__ unsigned or4(v4u d) { return (d.x | d.y) | (d.z | d.w); }
__device__ __forceinline__ bool allok(unsigned bad) { return __all((bad & 0xFFu) == 0u); }
template <int C> __device__ __forceinline__ float dpp(float x) {
  return asf((unsigned)__builtin_amdgcn_update_dpp(0, (int)asu(x), C, 0xF, 0xF, true));
}
__device__ __forceinline__ float xrow_sum(float x) {
  auto s = __builtin_amdgcn_permlane16_swap(asu(x), asu(x), false, false);
  x = asf(s[0]) + asf(s[1]);
  auto t = __builtin_amdgcn_permlane32_swap(asu(x), asu(x), false, false);
  return asf(t[0]) + asf(t[1]);
}
__device__ __forceinline__ float xrow_max(float x) {
  auto s = __builtin_amdgcn_permlane16_swap(asu(x), asu(x), false, false);
  x = __builtin_amdgcn_fmed3f(asf(s[0]), asf(s[1]), INFINITY);
  auto t = __builtin_amdgcn_permlane32_swap(asu(x), asu(x), false, false);
  return __builtin_amdgcn_fmed3f(asf(t[0]), asf(t[1]), INFINITY);
}
__device__ __forceinline__ float tr4(f32x4 v) {
  auto a = __builtin_amdgcn_permlane16_swap(asu(v.x), asu(v.y), false, false);
  auto b = __builtin_amdgcn_permlane16_swap(asu(v.z), asu(v.w), false, false);
  const float s01 = asf(a[0]) + asf(a[1]), s23 = asf(b[0]) + asf(b[1]);
  auto c = __builtin_amdgcn_permlane32_swap(asu(s01), asu(s23), false, false);
  return asf(c[0]) + asf(c[1]);
}
__device__ __forceinline__ float wave_sum2(float x, float y) {
  x += dpp<0xB1>(x); y += dpp<0xB1>(y);
  x += dpp<0x4E>(x); y += dpp<0x4E>(y);
  x += dpp<0x141>(x); y += dpp<0x141>(y);
  x += dpp<0x140>(x); y += dpp<0x140>(y);
  auto a = __builtin_amdgcn_permlane16_swap(asu(x), asu(y), false, false);
  const float s = asf(a[0]) + asf(a[1]);
  auto c = __builtin_amdgcn_permlane32_swap(asu(s), asu(s), false, false);
  return asf(c[0]) + asf(c[1]);
}
__device__ __forceinline__ f32x4 asf4(v4u d) { return __builtin_bit_cast(f32x4, d); }
__device__ __forceinline__ f32x4 mfma(v4u a, v4u b, f32x4 c) {
  return __builtin_amdgcn_mfma_f32_16x16x32_bf16(__builtin_bit_cast(bf16x8, a), __builtin_bit_cast(bf16x8, b), c, 0, 0, 0);
}
struct Mask { unsigned m0, m1, m2, m3; };
__device__ __forceinline__ v4u diag(unsigned hh, const Mask& k) { return v4u{hh & k.m0, hh & k.m1, hh & k.m2, hh & k.m3}; }
__device__ __forceinline__ void bar() {
  asm volatile("s_waitcnt lgkmcnt(0)" ::: "memory");
  __builtin_amdgcn_s_barrier();
  asm volatile("" ::: "memory");
}
__device__ __forceinline__ void rawbar() {
  __builtin_amdgcn_sched_barrier(0);
  __builtin_amdgcn_s_barrier();
  __builtin_amdgcn_sched_barrier(0);
}
__device__ __forceinline__ float silu(float v) { return v * __builtin_amdgcn_rcpf(1.f + __expf(-v)); }
__device__ __forceinline__ float fold1(float x) {
  auto t = __builtin_amdgcn_permlane32_swap(asu(x), asu(x), false, false);
  return asf(t[0]) + dpp<0x128>(asf(t[1]));
}
__device__ __forceinline__ f32x4 fold(f32x4 a) { return f32x4{fold1(a.x), fold1(a.y), fold1(a.z), fold1(a.w)}; }
__device__ __forceinline__ float fold2_1(float a, float b) {
  auto t = __builtin_amdgcn_permlane32_swap(asu(a), asu(b), false, false);
  return asf(t[0]) + dpp<0x128>(asf(t[1]));
}


struct Lane {
  Mask k;
  LAS unsigned* shw;
  LAS unsigned* shr;
  LAS float* put;
  bool putv;
};
__device__ __forceinline__ void shuf(LAS unsigned* wr, LAS unsigned* rd, float v, v4u& h0, v4u& h1) {
  *wr = rep(v);
  asm volatile("" ::: "memory");
  h0 = *(LAS v4u*)rd;
  h1 = *(LAS v4u*)(rd + 4);
}
__device__ __forceinline__ f32x4 gemv8(const v4u (&w)[8], v4u h0, v4u h1, const Mask& k) {
  f32x4 a = {0.f, 0.f, 0.f, 0.f}, b = {0.f, 0.f, 0.f, 0.f};
  const v4u a0 = diag(h0.x, k), a1 = diag(h0.y, k), a2 = diag(h0.z, k), a3 = diag(h0.w, k);
  a = mfma(a0, w[0], a); b = mfma(a1, w[1], b);
  const v4u a4 = diag(h1.x, k), a5 = diag(h1.y, k);
  a = mfma(a2, w[2], a); b = mfma(a3, w[3], b);
  const v4u a6 = diag(h1.z, k), a7 = diag(h1.w, k);
  a = mfma(a4, w[4], a); b = mfma(a5, w[5], b);
  a = mfma(a6, w[6], a); b = mfma(a7, w[7], b);
  return a + b;
}
__device__ __forceinline__ f32x2 ln_sums(LAS float* st) {
  const f32x4 a = *(LAS f32x4*)(st), b = *(LAS f32x4*)(st + 4), c = *(LAS f32x4*)(st + 8), d = *(LAS f32x4*)(st + 12);
  const f32x2 e = *(LAS f32x2*)(st + 16);
  const f32x4 t = (a + b) + (c + d);
  return f32x2{t.x + t.z + e.x, t.y + t.w + e.y};
}
template <int N> __device__ __forceinline__ float sumN(LAS float* red) {
  float a = 0.f, b = 0.f;
#pragma unroll
  for (int i = 0; i < N; i += 2) { a += red[i * 64]; b += red[i * 64 + 64]; }
  return a + b;
}
__device__ __forceinline__ void loads8(v4u (&w)[8], rsrc_t r, int stride, int lane, int base) {
  const int vo = (lane >> 3) * stride + (lane & 7) * 16;
#pragma unroll
  for (int s = 0; s < 8; ++s) w[s] = ldnt(r, vo, base + 8 * s * stride);
}
struct Tile18 { v4u w[18]; v2u c; };
__device__ __forceinline__ void tile18_load(Tile18& t, const u16* c, rsrc_t rwm, int ks, int cb, int lane) {
  t.c = *(const v2u*)(c + 144 * ks + 4 * (lane < 36 ? lane : 35));
  const int vo = (lane >> 3) * 6912 + (lane & 7) * 16;
#pragma unroll
  for (int s = 0; s < 18; ++s) t.w[s] = ldnt(rwm, vo, (144 * ks + 8 * s) * 6912 + cb);
}
__device__ __forceinline__ f32x4 tile18_mac(const Tile18& t, LAS unsigned* sh, int lane, const Mask& k) {
  if (lane < 36) {
    LAS unsigned* d = sh + (4 * (lane & 1)) * 20 + (lane >> 1);
    d[0] = rep(silu(asf(t.c.x << 16))); d[20] = rep(silu(asf(t.c.x & 0xFFFF0000u)));
    d[40] = rep(silu(asf(t.c.y << 16))); d[60] = rep(silu(asf(t.c.y & 0xFFFF0000u)));
  }
  asm volatile("" ::: "memory");
  LAS unsigned* s = sh + (lane >> 3) * 20;
  const v4u h0 = *(LAS v4u*)s, h1 = *(LAS v4u*)(s + 4), h2 = *(LAS v4u*)(s + 8), h3 = *(LAS v4u*)(s + 12);
  const v2u h4 = *(LAS v2u*)(s + 16);
  f32x4 a = {0.f, 0.f, 0.f, 0.f}, b = {0.f, 0.f, 0.f, 0.f};
  a = mfma(diag(h0.x, k), t.w[0], a); b = mfma(diag(h0.y, k), t.w[1], b); a = mfma(diag(h0.z, k), t.w[2], a); b = mfma(diag(h0.w, k), t.w[3], b);
  a = mfma(diag(h1.x, k), t.w[4], a); b = mfma(diag(h1.y, k), t.w[5], b); a = mfma(diag(h1.z, k), t.w[6], a); b = mfma(diag(h1.w, k), t.w[7], b);
  a = mfma(diag(h2.x, k), t.w[8], a); b = mfma(diag(h2.y, k), t.w[9], b); a = mfma(diag(h2.z, k), t.w[10], a); b = mfma(diag(h2.w, k), t.w[11], b);
  a = mfma(diag(h3.x, k), t.w[12], a); b = mfma(diag(h3.y, k), t.w[13], b); a = mfma(diag(h3.z, k), t.w[14], a); b = mfma(diag(h3.w, k), t.w[15], b);
  a = mfma(diag(h4.x, k), t.w[16], a); b = mfma(diag(h4.y, k), t.w[17], b);
  return a + b;
}
__device__ __forceinline__ void lds_arrive(LAS unsigned* c) {
  if ((threadIdx.x & 63) == 0) asm volatile("ds_add_u32 %0, %1" ::"v"(c), "v"(1u) : "memory");
  asm volatile("" ::: "memory");
}
__device__ __forceinline__ float vmax(float a, float b) { return __builtin_amdgcn_fmed3f(a, b, INFINITY); }
__device__ __forceinline__ float max12(LAS float* m) {
  const f32x4 b0 = *(LAS f32x4*)m, b1 = *(LAS f32x4*)(m + 4), b2 = *(LAS f32x4*)(m + 8);
  return vmax(vmax(vmax(vmax(b0.x, b0.y), vmax(b0.z, b0.w)), vmax(vmax(b1.x, b1.y), vmax(b1.z, b1.w))), vmax(vmax(b2.x, b2.y), vmax(b2.z, b2.w)));
}
__device__ __forceinline__ unsigned lds_inc(LAS unsigned* c) {
  asm volatile("" ::: "memory");
  unsigned old = 0;
  if ((threadIdx.x & 63) == 0) old = __hip_atomic_fetch_add(c, 1u, __ATOMIC_RELAXED, __HIP_MEMORY_SCOPE_WORKGROUP);
  old = (unsigned)__builtin_amdgcn_readfirstlane((int)old);
  asm volatile("" ::: "memory");
  return old;
}
__device__ __forceinline__ void lds_wait(LAS unsigned* c, unsigned need) {
  for (unsigned spin = 0; *(volatile LAS unsigned*)c < need && spin < (1u << 20); ++spin) {}
  asm volatile("" ::: "memory");
}
__device__ __forceinline__ void make_lane(Lane& ln, int lane, LAS unsigned* sh) {
  const unsigned mm = (lane & 1) ? 0xFFFF0000u : 0x0000FFFFu;
  const int d = (lane & 7) >> 1, c = lane & 15, g = lane >> 4;
  ln.k.m0 = d == 0 ? mm : 0u; ln.k.m1 = d == 1 ? mm : 0u; ln.k.m2 = d == 2 ? mm : 0u; ln.k.m3 = d == 3 ? mm : 0u;
  ln.shw = sh + 32 * (c & 1) + 8 * g + (c >> 1);
  ln.shr = sh + (lane >> 3) * 8;
  ln.put = (LAS float*)0 + (c >> 3) * 64 + 8 * (c & 7) + 4 * (g & 1);
  ln.putv = (c >> 3) == (g >> 1);
  pin(ln.k.m0); pin(ln.k.m1); pin(ln.k.m2); pin(ln.k.m3); pin(ln.shw); pin(ln.shr); pin(ln.put);
}
__device__ __forceinline__ void put_halves(const Lane& ln, int base_dw, f32x4 a) {
  if (ln.putv) *(LAS f32x4*)(ln.put + base_dw) = a;
}
__device__ __forceinline__ void modb_finish(const Tile18& t, LAS unsigned* sh, const Lane& ln, LAS float* Lf, int red_dw, LAS unsigned* cnt, int ks, int lane,
                                            rsrc_t ws, int tile, unsigned T) {
  put_halves(ln, red_dw + ks * 128, tile18_mac(t, sh, lane, ln.k));
  if (lds_inc(cnt) == 3u) stsc1(ws, tg(rb(sumN<8>(Lf + red_dw + lane)), T), lane * 4, WS_SB + tile * 256);
}

__global__ void __launch_bounds__(NTHR, 3) fused_decode(Params p) {
  __shared__ __attribute__((aligned(16))) unsigned smem[L_END];
  LAS unsigned* L = (LAS unsigned*)smem;
  LAS float* Lf = (LAS float*)smem;
  const int b = blockIdx.x, xcd = b & 7, slot = b >> 3;
  const int lane = threadIdx.x & 63, w = __builtin_amdgcn_readfirstlane(threadIdx.x >> 6);
  const int c4 = lane & 15, row = lane >> 4;
  const int col = 4 * c4 + row;
#define LOAD_EPOCH() __builtin_amdgcn_sched_barrier(0); const unsigned Ev = __builtin_amdgcn_raw_buffer_load_b32(ws, 0, WS_EPOCH, 16); __builtin_amdgcn_sched_barrier(0)
#define GET_TAG() const unsigned E = (unsigned)__builtin_amdgcn_readfirstlane((int)Ev); const unsigned T = (E + 1u) & 0xFFu; (void)E
  LAS unsigned* sh = L + L_SH + w * SH_STRIDE;
  const int lbase = (int)(size_t)(LAS void*)smem;
  Lane ln;

  if (slot <= 9) {
  const int rgate = (int)((size_t)p.wg >> 7), xstar = (3 - rgate) & 7;
  const bool qonly = slot == 9 && xcd == xstar;
  if (slot < 9 || qonly) {
    if (w >= 10) return;
    const int f = xcd * 9 + slot;
    __builtin_amdgcn_sched_barrier(0);
    const rsrc_t ws = mk(p.ws, WS_END), rwm = mk(p.wm, D * 6 * D * 2);
    if (w == 9) {
      __builtin_amdgcn_s_setprio(3);
      if (lane == 0) L[L_CNT] = 0u;
      if (qonly) { bar(); return; }
      Tile18 t;
      tile18_load(t, p.c, rwm, f / 18, (f % 18) * 128, lane);
      LOAD_EPOCH();
      bar();
      make_lane(ln, lane, sh);
      GET_TAG();
      const f32x4 a = fold(tile18_mac(t, sh, lane, ln.k));
      if (c4 < 8 && row < 2) stsc4(ws, v4u{tgr(a.x, T), tgr(a.y, T), tgr(a.z, T), tgr(a.w, T)}, (8 * c4 + 4 * row) * 4, WS_S1 + f * 256);
      return;
    }
    const int j = w;
    const int s0 = (3 - rgate - xcd) & 7;
    const bool hasq = qonly || !(slot == s0 || (s0 == 0 && slot == 8));
    const int n = qonly ? xcd * 8 + 7 : xcd * 8 + slot - (slot > s0 ? 1 : 0);
    const u16 xr = p.x[64 * j + col], x0r = p.x[0];
    LOAD_EPOCH();
    rawbar();
    v4u wt[8], wg[8], wu[8], wd[8];
    const rsrc_t rg = mk(p.wg, D * FF * 2), ru = mk(p.wu, D * FF * 2), rd = mk(p.wd, FF * D * 2);
    loads8(wt, mk(p.wq, hasq ? D * 4096 * 2 : 0), 8192, lane, 64 * j * 8192 + n * 128);
    __builtin_amdgcn_sched_barrier(0);
    make_lane(ln, lane, sh);
    ln.put += lbase / 4;
    GET_TAG();
    const float x0 = bf(x0r);
    float xv = bf(xr);
    {
      const float d = xv - x0;
      const float s = wave_sum2(d, d * d);
      if ((lane & 47) == 0) Lf[L_ST + 2 * j + (lane >> 4)] = s;
    }
    lds_arrive(L + L_CNT);
    lds_wait(L + L_CNT, 9u);
    float mean1, lnv;
    {
      const f32x2 s = ln_sums(Lf + L_ST);
      const float dm = s.x * (1.f / D);
      mean1 = x0 + dm;
      const float var = fmaxf(s.y * (1.f / D) - dm * dm, 0.f);
      const float den = rb(__builtin_amdgcn_sqrtf(rb(rb(var) + 1e-5f)));
      lnv = rb(rb(xv - rb(mean1)) / den);
      pin(lnv); pin(mean1); pin(xv);
    }
    if (hasq) {
      int o1 = (row * 18 + j) * 256 + c4 * 16;
      pin(o1);
      __builtin_amdgcn_s_waitcnt(0x0F70 | 2);
      v4u sa, ca;
      for (unsigned spin = 0;; ++spin) {
        asm volatile("" ::: "memory");
        sa = ldsc(ws, o1, WS_S1); ca = ldsc(ws, o1, WS_S1 + 9 * 256);
        __builtin_amdgcn_sched_barrier(0);
        sa ^= T; ca ^= T;
        if (allok(or4(sa) | or4(ca)) || spin > SPIN_MAX) break;
        __builtin_amdgcn_s_sleep(2);
      }
      const float h = rb(lnv * rb(1.f + rb(tr4(asf4(ca))))) + rb(tr4(asf4(sa)));
      v4u h0, h1;
      shuf(ln.shw, ln.shr, h, h0, h1);
      put_halves(ln, L_REDA + j * 128, gemv8(wt, h0, h1, ln.k));
      bar();
      if (j == 0) stsc1(ws, tg(rb(sumN<18>(Lf + L_REDA + lane)), T), lane * 4, WS_SQ + n * 256);
      rawbar();
    }
    if (qonly) return;
    loads8(wg, rg, FF * 2, lane, 64 * j * FF * 2 + f * 128);
    loads8(wu, ru, FF * 2, lane, 64 * j * FF * 2 + f * 128);
    loads8(wd, rd, 1152, lane, 64 * f * 1152 + j * 128);
    __builtin_amdgcn_sched_barrier(0);
    int o4 = (j * 16 + row) * 256 + c4 * 16, ob = j * 256 + col * 4, o5 = (8 * c4 + 4 * row) * 4;
    LAS float* redg_w = Lf + L_REDG + j * 128 + (row >> 1) * 64 + 8 * c4 + 4 * (row & 1);
    LAS float* redg_r = Lf + L_REDG + lane;
    LAS unsigned* shw2 = L + L_Q + (lane & 7) * 8 + (lane >> 3);
    LAS unsigned* shr2 = L + L_Q + (lane >> 3) * 8;
    pin(o4); pin(ob); pin(o5); pin(redg_w); pin(redg_r); pin(shw2); pin(shr2);
    asm volatile("s_waitcnt vmcnt(0)" ::: "memory");
    rawbar();
    float x1, sb, cb;
    {
      unsigned a, s2, c2;
      v4u q0, q1, q2, q3;
      q0 = ldsc(ws, o4, WS_S4); q1 = ldsc(ws, o4, WS_S4 + 1024); q2 = ldsc(ws, o4, WS_S4 + 2048); q3 = ldsc(ws, o4, WS_S4 + 3072);
      a = ldsc1(ws, ob, WS_SB); s2 = ldsc1(ws, ob, WS_SB + 9 * 256); c2 = ldsc1(ws, ob, WS_SB + 18 * 256);
      for (unsigned spin = 0; spin <= SPIN_MAX; ++spin) {
        asm volatile("" ::: "memory");
        bool again = false;
        if (!allok(or4(q0 ^ T))) { q0 = ldsc(ws, o4, WS_S4); again = true; }
        if (!allok(or4(q1 ^ T))) { q1 = ldsc(ws, o4, WS_S4 + 1024); again = true; }
        if (!allok(or4(q2 ^ T))) { q2 = ldsc(ws, o4, WS_S4 + 2048); again = true; }
        if (!allok(or4(q3 ^ T))) { q3 = ldsc(ws, o4, WS_S4 + 3072); again = true; }
        if (!allok((a ^ T) | (s2 ^ T) | (c2 ^ T))) {
          a = ldsc1(ws, ob, WS_SB); s2 = ldsc1(ws, ob, WS_SB + 9 * 256); c2 = ldsc1(ws, ob, WS_SB + 18 * 256);
          again = true;
        }
        if (!again) break;
      }
      q0 ^= T; q1 ^= T; q2 ^= T; q3 ^= T; a ^= T; s2 ^= T; c2 ^= T;
      x1 = rb(xv + rb(asf(a) * rb(tr4((asf4(q0) + asf4(q1)) + (asf4(q2) + asf4(q3))))));
      sb = asf(s2); cb = rb(1.f + asf(c2));
    }
    {
      const float d = x1 - mean1;
      const float s = wave_sum2(d, d * d);
      if ((lane & 47) == 0) Lf[L_ST + 32 + 2 * j + (lane >> 4)] = s;
    }
    bar();
    {
      const f32x2 s = ln_sums(Lf + L_ST + 32);
      const float dm = s.x * (1.f / D);
      const float var = fmaxf(s.y * (1.f / D) - dm * dm, 0.f);
      const float den = rb(__builtin_amdgcn_sqrtf(rb(rb(var) + 1e-5f)));
      const float h2 = rb(rb(rb(x1 - rb(mean1 + dm)) * __builtin_amdgcn_rcpf(den)) * cb) + sb;
      v4u h0, h1;
      shuf(ln.shw, ln.shr, h2, h0, h1);
      const f32x4 g = gemv8(wg, h0, h1, ln.k), u = gemv8(wu, h0, h1, ln.k);
      const f32x4 t = {fold2_1(g.x, u.x), fold2_1(g.y, u.y), fold2_1(g.z, u.z), fold2_1(g.w, u.w)};
      if (c4 < 8) *(LAS f32x4*)redg_w = t;
    }
    bar();
    if (j == 1) {
      float g = 0.f, u = 0.f, g2 = 0.f, u2 = 0.f;
#pragma unroll
      for (int i = 0; i < 8; i += 2) {
        g += redg_r[i * 128]; u += redg_r[i * 128 + 64];
        g2 += redg_r[i * 128 + 128]; u2 += redg_r[i * 128 + 192];
      }
      g = rb((g + redg_r[1024]) + g2); u = rb((u + redg_r[1088]) + u2);
      const float z = 1.5957691216057308f * (g + 0.044715f * g * g * g);
      *shw2 = rep(rb(g * __builtin_amdgcn_rcpf(1.f + __expf(-z))) * u);
    }
    bar();
    {
      const v4u h0 = *(LAS v4u*)shr2, h1 = *(LAS v4u*)(shr2 + 4);
      const f32x4 y = fold(gemv8(wd, h0, h1, ln.k));
      if (c4 < 8 && row < 2) stsc4(ws, tg4(y, T), o5, WS_S5 + (j * 72 + f) * 256);
    }
    return;
  }

  {
    int tile = 16 + (w >> 2) * 8 + xcd;
    if (w >= 8 && xcd >= 4) {
      const int r = xcd - 4 - ((xstar >= 4 && xcd > xstar) ? 1 : 0);
      if (r >= (xstar < 4 ? 3 : 2)) return;
      tile = 16 + r * 8 + xstar;
    }
    if (threadIdx.x < 3) L[L_CNT + threadIdx.x] = 0u;
    const rsrc_t ws = mk(p.ws, WS_END), rwm = mk(p.wm, D * 6 * D * 2);
    Tile18 t;
    tile18_load(t, p.c, rwm, w & 3, 2304 + tile * 128, lane);
    LOAD_EPOCH();
    __builtin_amdgcn_sched_barrier(0);
    bar();
    make_lane(ln, lane, sh);
    GET_TAG();
    modb_finish(t, sh, ln, Lf, L_REDA + (w >> 2) * 512, L + L_CNT + (w >> 2), w & 3, lane, ws, tile, T);
    return;
  }

  }
  const int g1 = (slot + 11) >> 5, g = 2 * xcd + g1, k = slot - 10 - 11 * g1;
  const bool light = k == 10;
  const int s3 = WS_S3 + (g * 11 + k) * S3_STRIDE;
  __builtin_amdgcn_sched_barrier(0);
  const rsrc_t ws = mk(p.ws, WS_END), rwm = mk(p.wm, D * 6 * D * 2);
  unsigned E = 0, T = 0;
  v4u wt[8];
  const rsrc_t rwo = mk(p.wo, (k < 9 && w < 4) ? 4096 * D * 2 : 0);
  if (light && w >= 8) {
    Tile18 t;
    tile18_load(t, p.c, rwm, w - 8, 2304 + g * 128, lane);
    LOAD_EPOCH();
    if (w == 8 && lane < 32) Lf[L_MW + lane] = -INFINITY;
    if (w == 9 && lane < 32) Lf[L_LW + lane] = 0.f;
    if (w == 10 && lane < 3) L[L_CNT + lane] = 0u;
    bar();
    make_lane(ln, lane, sh);
    E = (unsigned)__builtin_amdgcn_readfirstlane((int)Ev); T = (E + 1u) & 0xFFu;
    modb_finish(t, sh, ln, Lf, L_REDA, L + L_CNT + 1, w - 8, lane, ws, g, T);
    rawbar();
    rawbar();
  } else {
    const size_t kvoff = ((size_t)g * SK + (SK - WIN) + k * 384 + w * 32) * 128;
    const rsrc_t rk = mk(p.kc + kvoff, 8192), rv = mk(p.vc + kvoff, 8192);
    v4u kk[8], vv[8];
    const int kvo = (lane & 7) * 256 + ((lane >> 3) & 1) * 64 + row * 16, vvo = row * 2048 + c4 * 16;
#pragma unroll
    for (int t = 0; t < 4; ++t) {
      kk[2 * t] = ldnt(rk, kvo, t * 2048);
      kk[2 * t + 1] = ldnt(rk, kvo + 128, t * 2048);
    }
    LOAD_EPOCH();
    if (!light) {
      if (w == 8 && lane < 32) Lf[L_MW + lane] = -INFINITY;
      if (w == 9 && lane < 32) Lf[L_LW + lane] = 0.f;
      if (w == 10 && lane < 3) L[L_CNT + lane] = 0u;
    }
    __builtin_amdgcn_sched_barrier(0);
    bar();
    loads8(wt, rwo, 1152, lane, (256 * g + 64 * (w & 3)) * 1152 + k * 128);
#pragma unroll
    for (int s = 0; s < 8; ++s) vv[s] = ldnt(rv, vvo + s * 256, 0);
    __builtin_amdgcn_sched_barrier(0);
    make_lane(ln, lane, sh);
    ln.put += lbase / 4;
    E = (unsigned)__builtin_amdgcn_readfirstlane((int)Ev); T = (E + 1u) & 0xFFu;
    LAS unsigned* qrd = L + L_Q + (lane & 1) * 64 + ((lane >> 1) & 1) * 16 + row * 4;
    LAS float* mwr = Lf + L_MW + ((lane >> 3) & 1) * 16;
    LAS float* owr = Lf + L_O + w * 256 + (row >> 1) * 128 + 8 * c4 + 4 * (row & 1);
    pin(qrd); pin(mwr); pin(owr);
    if (w == 0) {
      const int oq = lane * 16, sq = WS_SQ + g * 1024;
      v4u q;
      for (unsigned spin = 0;; ++spin) {
        asm volatile("" ::: "memory");
        q = ldsc(ws, oq, sq) ^ T;
        if (allok(or4(q)) || spin > SPIN_MAX) break;
        __builtin_amdgcn_s_sleep(2);
      }
      *(LAS v2u*)(L + L_Q + lane * 2) = v2u{(q.x >> 16) | q.y, (q.z >> 16) | q.w};
    }
    bar();
    float sc = 0.f;
    {
      const v4u q0 = *(LAS v4u*)qrd, q1 = *(LAS v4u*)(qrd + 32);
#pragma unroll
      for (int t = 0; t < 4; ++t) {
        f32x4 d = {0.f, 0.f, 0.f, 0.f};
        d = mfma(q0, kk[2 * t], d);
        d = mfma(q1, kk[2 * t + 1], d);
        const float h0 = d.x + dpp<0x128>(d.z), h1 = d.w + dpp<0x128>(d.y);
        const float v = c4 < 8 ? h0 : h1;
        sc = row == t ? v : sc;
      }
      sc = rb(rb(sc) * 0.08838834764831845f);
      float m = sc;
      m = vmax(m, dpp<0xB1>(m)); m = vmax(m, dpp<0x4E>(m)); m = vmax(m, dpp<0x141>(m));
      m = xrow_max(m);
      if ((lane & 55) == 0) mwr[w] = m;
    }
    bar();
    {
      const float M = max12(mwr);
      const float pe = __expf(sc - M);
      v4u h0, h1;
      shuf(sh + lane, ln.shr, pe, h0, h1);
      float l = pe;
      l += dpp<0xB1>(l); l += dpp<0x4E>(l); l += dpp<0x141>(l);
      l = xrow_sum(l);
      if ((lane & 55) == 0) mwr[32 + w] = l;
      lds_arrive(L + L_CNT + 2);
      v4u A[8];
      A[0] = diag(h0.x, ln.k); A[1] = diag(h0.y, ln.k); A[2] = diag(h0.z, ln.k); A[3] = diag(h0.w, ln.k);
      A[4] = diag(h1.x, ln.k); A[5] = diag(h1.y, ln.k); A[6] = diag(h1.z, ln.k); A[7] = diag(h1.w, ln.k);
      __builtin_amdgcn_sched_barrier(0);
      f32x4 o0 = {0.f, 0.f, 0.f, 0.f}, o1 = {0.f, 0.f, 0.f, 0.f};
      o0 = mfma(A[0], vv[0], o0); o1 = mfma(A[1], vv[1], o1); o0 = mfma(A[2], vv[2], o0); o1 = mfma(A[3], vv[3], o1);
      o0 = mfma(A[4], vv[4], o0); o1 = mfma(A[5], vv[5], o1); o0 = mfma(A[6], vv[6], o0); o1 = mfma(A[7], vv[7], o1);
      *(LAS f32x4*)owr = o0 + o1;
      lds_arrive(L + L_CNT);
    }
    if (w >= 7) __builtin_amdgcn_s_setprio(3);
    if (w == 7) {
      lds_wait(L + L_CNT + 2, light ? 8u : 12u);
      LAS float* lw = Lf + L_LW + (lane & 1) * 16;
      LAS float* mw = Lf + L_MW + (lane & 1) * 16;
      const f32x4 a0 = *(LAS f32x4*)lw, a1 = *(LAS f32x4*)(lw + 4), a2 = *(LAS f32x4*)(lw + 8);
      const float l = ((a0.x + a0.y) + (a0.z + a0.w)) + ((a1.x + a1.y) + (a1.z + a1.w)) + ((a2.x + a2.y) + (a2.z + a2.w));
      const float M = max12(mw);
      if (lane < 2) __builtin_amdgcn_raw_buffer_store_b64(v2u{tg(M, T), tg(l, T)}, ws, lane * 8, s3 + 1024, 16);
    }
  }
  if (w >= 8) {
    __builtin_amdgcn_s_setprio(3);
    const int wp = w - 8;
    LAS float* o = Lf + L_O + row * 256 + wp * 64 + c4 * 4;
    int so = wp * 256 + col * 4;
    pin(o); pin(so);
    lds_wait(L + L_CNT, light ? 8u : 12u);
    f32x4 v = *(LAS f32x4*)o + *(LAS f32x4*)(o + 1024);
    if (!light) v += *(LAS f32x4*)(o + 2048);
    stsc1(ws, tg(tr4(v), T), so, s3);
    if (!(light && g < 9 && w == 8)) return;
  }
  if (k < 9) {
    if (w >= 4) return;
    const int k2 = row + 8 > 10 ? 10 : row + 8;
    const int gb = WS_S3 + g * 11 * S3_STRIDE, hd = (w >> 1) * 8;
    int oa = row * S3_STRIDE + w * 256 + c4 * 16, oc = k2 * S3_STRIDE + w * 256 + c4 * 16;
    int ma_ = row * S3_STRIDE + 1024 + hd, mc_ = k2 * S3_STRIDE + 1024 + hd;
    pin(oa); pin(oc); pin(ma_); pin(mc_);
    v2u m0, m1, m2;
    v4u o0, o1, o2;
    for (unsigned spin = 0;; ++spin) {
      asm volatile("" ::: "memory");
      m0 = ldsc2(ws, ma_, gb); m1 = ldsc2(ws, ma_, gb + 4 * S3_STRIDE); m2 = ldsc2(ws, mc_, gb);
      o0 = ldsc(ws, oa, gb); o1 = ldsc(ws, oa, gb + 4 * S3_STRIDE); o2 = ldsc(ws, oc, gb);
      __builtin_amdgcn_sched_barrier(0);
      m0 ^= T; m1 ^= T; m2 ^= T; o0 ^= T; o1 ^= T; o2 ^= T;
      if (allok((m0.x | m0.y) | (m1.x | m1.y) | (m2.x | m2.y) | or4(o0) | or4(o1) | or4(o2)) || spin > SPIN_MAX) break;
      __builtin_amdgcn_s_sleep(1);
    }
    const float ma = asf(m0.x), mb = asf(m1.x), mc = row == 3 ? -INFINITY : asf(m2.x);
    const float MM = xrow_max(vmax(vmax(ma, mb), mc));
    float fa = __expf(ma - MM), fb = __expf(mb - MM), fc = __expf(mc - MM);
    const float inv = __builtin_amdgcn_rcpf(xrow_sum(fa * asf(m0.y) + fb * asf(m1.y) + fc * asf(m2.y)));
    v4u h0, h1;
    shuf(ln.shw, ln.shr, tr4(asf4(o0) * fa + asf4(o1) * fb + asf4(o2) * fc) * inv, h0, h1);
    put_halves(ln, L_REDA + w * 128, gemv8(wt, h0, h1, ln.k));
    bar();
    if (w == 0) stsc1(ws, tg(sumN<8>(Lf + L_REDA + lane), T), lane * 4, WS_S4 + (k * 16 + g) * 256);
    return;
  }
  if (!(light && g < 9) || w > 8) return;
  {
    const int j = g;
    float x1 = 0.f, gbv = 0.f;
    if (w == 0) {
      const float xv = bf(p.x[64 * j + col]);
      unsigned a2, b2;
      v4u q0, q1, q2, q3;
      for (unsigned spin = 0;; ++spin) {
        asm volatile("" ::: "memory");
        const int vo = (j * 16 + row) * 256 + c4 * 16;
        a2 = ldsc1(ws, col * 4, WS_SB + j * 256); b2 = ldsc1(ws, col * 4, WS_SB + (27 + j) * 256);
        q0 = ldsc(ws, vo, WS_S4); q1 = ldsc(ws, vo, WS_S4 + 1024); q2 = ldsc(ws, vo, WS_S4 + 2048); q3 = ldsc(ws, vo, WS_S4 + 3072);
        __builtin_amdgcn_sched_barrier(0);
        q0 ^= T; q1 ^= T; q2 ^= T; q3 ^= T; a2 ^= T; b2 ^= T;
        if (allok(a2 | b2 | or4(q0) | or4(q1) | or4(q2) | or4(q3)) || spin > SPIN_MAX) break;
        __builtin_amdgcn_s_sleep(8);
      }
      x1 = rb(xv + rb(asf(a2) * rb(tr4((asf4(q0) + asf4(q1)) + (asf4(q2) + asf4(q3))))));
      gbv = asf(b2);
      pin(x1); pin(gbv);
    }
    {
      v4u y0, y1;
      int vo = (8 * w + row) * 256 + c4 * 16;
      LAS float* rw = Lf + L_REDG + w * 64 + col;
      pin(vo); pin(rw);
      for (unsigned spin = 0;; ++spin) {
        asm volatile("" ::: "memory");
        y0 = ldsc(ws, vo, WS_S5 + j * 72 * 256); y1 = ldsc(ws, vo, WS_S5 + j * 72 * 256 + 1024);
        __builtin_amdgcn_sched_barrier(0);
        y0 ^= T; y1 ^= T;
        if (allok(or4(y0) | or4(y1)) || spin > SPIN_MAX) break;
        __builtin_amdgcn_s_sleep(1);
      }
      *rw = tr4(asf4(y0) + asf4(y1));
    }
    bar();
    if (w == 0) {
      LAS float* r = Lf + L_REDG + col;
      const float y = ((r[0] + r[64]) + (r[128] + r[192])) + ((r[256] + r[320]) + (r[384] + r[448])) + r[512];
      f32x2 o = {x1 + rb(gbv * rb(y)), 0.f};
      p.out[64 * j + col] = (u16)(__builtin_bit_cast(unsigned, __builtin_convertvector(o, bf16x2)) & 0xFFFFu);
      if (j == 0 && lane == 0) __builtin_amdgcn_raw_buffer_store_b32(E + 1u, ws, 0, WS_EPOCH, 16);
    }
  }
}

}

extern "C" void kernel_launch(void* const* d_in, const int* in_sizes, int n_in, void* d_out, int out_size, void* d_ws, size_t ws_size,
                              hipStream_t stream) {
  Params p;
  p.x = (const u16*)d_in[0]; p.kc = (const u16*)d_in[1]; p.vc = (const u16*)d_in[2]; p.wq = (const u16*)d_in[3]; p.wo = (const u16*)d_in[4];
  p.wg = (const u16*)d_in[6]; p.wu = (const u16*)d_in[7]; p.wd = (const u16*)d_in[8]; p.c = (const u16*)d_in[9]; p.wm = (const u16*)d_in[10];
  p.out = (u16*)d_out; p.ws = (unsigned char*)d_ws;
  fused_decode<<<256, NTHR, 0, stream>>>(p);
}
```

```cpp
#include <hip/hip_runtime.h>
#include <hip/hip_bf16.h>
#include <stdint.h>

namespace {

typedef unsigned short u16;
typedef unsigned v4u __attribute__((ext_vector_type(4)));
typedef unsigned v2u __attribute__((ext_vector_type(2)));
typedef float f32x4 __attribute__((ext_vector_type(4)));
typedef float f32x2 __attribute__((ext_vector_type(2)));
typedef __bf16 bf16x8 __attribute__((ext_vector_type(8)));
typedef __bf16 bf16x2 __attribute__((ext_vector_type(2)));
typedef __amdgpu_buffer_rsrc_t rsrc_t;
#define LAS __attribute__((address_space(3)))

constexpr int D = 576, SK = 65536, WIN = 4096, FF = 4608;
constexpr int NTHR = 768;
constexpr int WS_EPOCH = 0;
constexpr int WS_S1 = 4096;
constexpr int WS_SQ = WS_S1 + 72 * 256;
constexpr int WS_SB = WS_SQ + 64 * 256;
constexpr int WS_S4 = WS_SB + 36 * 256;
constexpr int WS_S5 = WS_S4 + 144 * 256;
constexpr int WS_S3 = WS_S5 + 648 * 256;
constexpr int S3_STRIDE = 1280;
constexpr int WS_END = WS_S3 + 176 * S3_STRIDE;
constexpr int WS_STAMP = 1 << 20;
constexpr unsigned SPIN_MAX = 1u << 14;
#define POLL_SLEEP 1
#define PRE_WAIT 2
#define WO_POS 1
#define DELAY_A 0
#define DELAY_F 0

struct Params {
  const u16 *x, *kc, *vc, *wq, *wo, *wg, *wu, *wd, *c, *wm;
  u16* out;
  unsigned char* ws;
};

constexpr int SH_STRIDE = 160;
constexpr int L_SH = 0;
constexpr int L_REDA = L_SH + 12 * SH_STRIDE;
constexpr int L_REDG = L_REDA + 1536;
constexpr int L_ST = L_REDG + 1152;
constexpr int L_Q = L_ST + 64;
constexpr int L_MW = L_Q + 128;
constexpr int L_LW = L_MW + 32;
constexpr int L_CNT = L_LW + 32;
constexpr int L_O = L_CNT + 16;
constexpr int L_END = L_O + 12 * 256;

__device__ __forceinline__ rsrc_t mk(const void* p, unsigned bytes) {
  return __builtin_amdgcn_make_buffer_rsrc((void*)p, 0, (int)bytes, 0x00020000);
}
#define AUX_W 0
#define AUX_K 2
#define AUX_V 2
__device__ __forceinline__ v4u ldnt(rsrc_t r, int v, int s) { return __builtin_amdgcn_raw_buffer_load_b128(r, v, s, AUX_W); }
__device__ __forceinline__ v4u ldk(rsrc_t r, int v, int s) { return __builtin_amdgcn_raw_buffer_load_b128(r, v, s, AUX_K); }
__device__ __forceinline__ v4u ldv(rsrc_t r, int v, int s) { return __builtin_amdgcn_raw_buffer_load_b128(r, v, s, AUX_V); }
__device__ __forceinline__ v4u ldsc(rsrc_t r, int v, int s) { return __builtin_amdgcn_raw_buffer_load_b128(r, v, s, 16); }
__device__ __forceinline__ v2u ldsc2(rsrc_t r, int v, int s) { return __builtin_amdgcn_raw_buffer_load_b64(r, v, s, 16); }
__device__ __forceinline__ unsigned ldsc1(rsrc_t r, int v, int s) { return __builtin_amdgcn_raw_buffer_load_b32(r, v, s, 16); }
__device__ __forceinline__ void stsc1(rsrc_t r, unsigned x, int v, int s) { __builtin_amdgcn_raw_buffer_store_b32(x, r, v, s, 16); }
__device__ __forceinline__ void stsc4(rsrc_t r, v4u x, int v, int s) { __builtin_amdgcn_raw_buffer_store_b128(x, r, v, s, 16); }
__device__ __forceinline__ float asf(unsigned x) { return __uint_as_float(x); }
__device__ __forceinline__ unsigned asu(float x) { return __float_as_uint(x); }
template <class V> __device__ __forceinline__ void pin(V& x) { asm volatile("" : "+v"(x)); }
__device__ __forceinline__ unsigned rep(float x) {
  f32x2 v = {x, x};
  return __builtin_bit_cast(unsigned, __builtin_convertvector(v, bf16x2));
}
__device__ __forceinline__ float rb(float x) { return asf(rep(x) & 0xFFFF0000u); }
__device__ __forceinline__ float bf(u16 x) { return asf((unsigned)x << 16); }
__device__ __forceinline__ unsigned tg(float x, unsigned T) { return (asu(x) & 0xFFFFFF00u) | T; }
__device__ __forceinline__ unsigned tgr(float x, unsigned T) { return ((asu(x) + 0x80u) & 0xFFFFFF00u) | T; }
__device__ __forceinline__ v4u tg4(f32x4 y, unsigned T) { return v4u{tg(y.x, T), tg(y.y, T), tg(y.z, T), tg(y.w, T)}; }
__device__ __forceinline__ unsigned or4(v4u d) { return (d.x | d.y) | (d.z | d.w); }
__device__ __forceinline__ bool allok(unsigned bad) { return __all((bad & 0xFFu) == 0u); }
template <int C> __device__ __forceinline__ float dpp(float x) {
  return asf((unsigned)__builtin_amdgcn_update_dpp(0, (int)asu(x), C, 0xF, 0xF, true));
}
__device__ __forceinline__ float xrow_sum(float x) {
  auto s = __builtin_amdgcn_permlane16_swap(asu(x), asu(x), false, false);
  x = asf(s[0]) + asf(s[1]);
  auto t = __builtin_amdgcn_permlane32_swap(asu(x), asu(x), false, false);
  return asf(t[0]) + asf(t[1]);
}
__device__ __forceinline__ float xrow_max(float x) {
  auto s = __builtin_amdgcn_permlane16_swap(asu(x), asu(x), false, false);
  x = __builtin_amdgcn_fmed3f(asf(s[0]), asf(s[1]), INFINITY);
  auto t = __builtin_amdgcn_permlane32_swap(asu(x), asu(x), false, false);
  return __builtin_amdgcn_fmed3f(asf(t[0]), asf(t[1]), INFINITY);
}
__device__ __forceinline__ float tr4(f32x4 v) {
  auto a = __builtin_amdgcn_permlane16_swap(asu(v.x), asu(v.y), false, false);
  auto b = __builtin_amdgcn_permlane16_swap(asu(v.z), asu(v.w), false, false);
  const float s01 = asf(a[0]) + asf(a[1]), s23 = asf(b[0]) + asf(b[1]);
  auto c = __builtin_amdgcn_permlane32_swap(asu(s01), asu(s23), false, false);
  return asf(c[0]) + asf(c[1]);
}
__device__ __forceinline__ float wave_sum2(float x, float y) {
  x += dpp<0xB1>(x); y += dpp<0xB1>(y);
  x += dpp<0x4E>(x); y += dpp<0x4E>(y);
  x += dpp<0x141>(x); y += dpp<0x141>(y);
  x += dpp<0x140>(x); y += dpp<0x140>(y);
  auto a = __builtin_amdgcn_permlane16_swap(asu(x), asu(y), false, false);
  const float s = asf(a[0]) + asf(a[1]);
  auto c = __builtin_amdgcn_permlane32_swap(asu(s), asu(s), false, false);
  return asf(c[0]) + asf(c[1]);
}
__device__ __forceinline__ f32x4 asf4(v4u d) { return __builtin_bit_cast(f32x4, d); }
__device__ __forceinline__ f32x4 mfma(v4u a, v4u b, f32x4 c) {
  return __builtin_amdgcn_mfma_f32_16x16x32_bf16(__builtin_bit_cast(bf16x8, a), __builtin_bit_cast(bf16x8, b), c, 0, 0, 0);
}
struct Mask { unsigned m0, m1, m2, m3; };
__device__ __forceinline__ v4u diag(unsigned hh, const Mask& k) { return v4u{hh & k.m0, hh & k.m1, hh & k.m2, hh & k.m3}; }
__device__ __forceinline__ void bar() {
  asm volatile("s_waitcnt lgkmcnt(0)" ::: "memory");
  __builtin_amdgcn_s_barrier();
  asm volatile("" ::: "memory");
}
__device__ __forceinline__ void rawbar() {
  __builtin_amdgcn_sched_barrier(0);
  __builtin_amdgcn_s_barrier();
  __builtin_amdgcn_sched_barrier(0);
}
__device__ __forceinline__ float silu(float v) { return v * __builtin_amdgcn_rcpf(1.f + __expf(-v)); }
__device__ __forceinline__ float fold1(float x) {
  auto t = __builtin_amdgcn_permlane32_swap(asu(x), asu(x), false, false);
  return asf(t[0]) + dpp<0x128>(asf(t[1]));
}
__device__ __forceinline__ f32x4 fold(f32x4 a) { return f32x4{fold1(a.x), fold1(a.y), fold1(a.z), fold1(a.w)}; }
__device__ __forceinline__ float fold2_1(float a, float b) {
  auto t = __builtin_amdgcn_permlane32_swap(asu(a), asu(b), false, false);
  return asf(t[0]) + dpp<0x128>(asf(t[1]));
}

#define STAMP(i) do {} while (0)
#define FINISH() do {} while (0)

struct Lane {
  Mask k;
  LAS unsigned* shw;
  LAS unsigned* shr;
  LAS float* put;
  bool putv;
};
__device__ __forceinline__ void shuf(LAS unsigned* wr, LAS unsigned* rd, float v, v4u& h0, v4u& h1) {
  *wr = rep(v);
  asm volatile("" ::: "memory");
  h0 = *(LAS v4u*)rd;
  h1 = *(LAS v4u*)(rd + 4);
}
__device__ __forceinline__ f32x4 gemv8(const v4u (&w)[8], v4u h0, v4u h1, const Mask& k) {
  f32x4 a = {0.f, 0.f, 0.f, 0.f}, b = {0.f, 0.f, 0.f, 0.f};
  const v4u a0 = diag(h0.x, k), a1 = diag(h0.y, k), a2 = diag(h0.z, k), a3 = diag(h0.w, k);
  a = mfma(a0, w[0], a); b = mfma(a1, w[1], b);
  const v4u a4 = diag(h1.x, k), a5 = diag(h1.y, k);
  a = mfma(a2, w[2], a); b = mfma(a3, w[3], b);
  const v4u a6 = diag(h1.z, k), a7 = diag(h1.w, k);
  a = mfma(a4, w[4], a); b = mfma(a5, w[5], b);
  a = mfma(a6, w[6], a); b = mfma(a7, w[7], b);
  return a + b;
}
__device__ __forceinline__ f32x2 ln_sums(LAS float* st) {
  const f32x4 a = *(LAS f32x4*)(st), b = *(LAS f32x4*)(st + 4), c = *(LAS f32x4*)(st + 8), d = *(LAS f32x4*)(st + 12);
  const f32x2 e = *(LAS f32x2*)(st + 16);
  const f32x4 t = (a + b) + (c + d);
  return f32x2{t.x + t.z + e.x, t.y + t.w + e.y};
}
template <int N> __device__ __forceinline__ float sumN(LAS float* red) {
  float a = 0.f, b = 0.f;
#pragma unroll
  for (int i = 0; i < N; i += 2) { a += red[i * 64]; b += red[i * 64 + 64]; }
  return a + b;
}
__device__ __forceinline__ void loads8(v4u (&w)[8], rsrc_t r, int stride, int lane, int base) {
  const int vo = (lane >> 3) * stride + (lane & 7) * 16;
#pragma unroll
  for (int s = 0; s < 8; ++s) w[s] = ldnt(r, vo, base + 8 * s * stride);
}
struct Tile18 { v4u w[18]; v2u c; };
__device__ __forceinline__ void tile18_load(Tile18& t, const u16* c, rsrc_t rwm, int ks, int cb, int lane) {
  t.c = *(const v2u*)(c + 144 * ks + 4 * (lane < 36 ? lane : 35));
  const int vo = (lane >> 3) * 6912 + (lane & 7) * 16;
#pragma unroll
  for (int s = 0; s < 18; ++s) t.w[s] = ldnt(rwm, vo, (144 * ks + 8 * s) * 6912 + cb);
}
__device__ __forceinline__ f32x4 tile18_mac(const Tile18& t, LAS unsigned* sh, int lane, const Mask& k) {
  if (lane < 36) {
    LAS unsigned* d = sh + (4 * (lane & 1)) * 20 + (lane >> 1);
    d[0] = rep(silu(asf(t.c.x << 16))); d[20] = rep(silu(asf(t.c.x & 0xFFFF0000u)));
    d[40] = rep(silu(asf(t.c.y << 16))); d[60] = rep(silu(asf(t.c.y & 0xFFFF0000u)));
  }
  asm volatile("" ::: "memory");
  LAS unsigned* s = sh + (lane >> 3) * 20;
  const v4u h0 = *(LAS v4u*)s, h1 = *(LAS v4u*)(s + 4), h2 = *(LAS v4u*)(s + 8), h3 = *(LAS v4u*)(s + 12);
  const v2u h4 = *(LAS v2u*)(s + 16);
  f32x4 a = {0.f, 0.f, 0.f, 0.f}, b = {0.f, 0.f, 0.f, 0.f};
  a = mfma(diag(h0.x, k), t.w[0], a); b = mfma(diag(h0.y, k), t.w[1], b); a = mfma(diag(h0.z, k), t.w[2], a); b = mfma(diag(h0.w, k), t.w[3], b);
  a = mfma(diag(h1.x, k), t.w[4], a); b = mfma(diag(h1.y, k), t.w[5], b); a = mfma(diag(h1.z, k), t.w[6], a); b = mfma(diag(h1.w, k), t.w[7], b);
  a = mfma(diag(h2.x, k), t.w[8], a); b = mfma(diag(h2.y, k), t.w[9], b); a = mfma(diag(h2.z, k), t.w[10], a); b = mfma(diag(h2.w, k), t.w[11], b);
  a = mfma(diag(h3.x, k), t.w[12], a); b = mfma(diag(h3.y, k), t.w[13], b); a = mfma(diag(h3.z, k), t.w[14], a); b = mfma(diag(h3.w, k), t.w[15], b);
  a = mfma(diag(h4.x, k), t.w[16], a); b = mfma(diag(h4.y, k), t.w[17], b);
  return a + b;
}
__device__ __forceinline__ void lds_arrive(LAS unsigned* c) {
  if ((threadIdx.x & 63) == 0) asm volatile("ds_add_u32 %0, %1" ::"v"(c), "v"(1u) : "memory");
  asm volatile("" ::: "memory");
}
__device__ __forceinline__ float vmax(float a, float b) { return __builtin_amdgcn_fmed3f(a, b, INFINITY); }
__device__ __forceinline__ float max12(LAS float* m) {
  const f32x4 b0 = *(LAS f32x4*)m, b1 = *(LAS f32x4*)(m + 4), b2 = *(LAS f32x4*)(m + 8);
  return vmax(vmax(vmax(vmax(b0.x, b0.y), vmax(b0.z, b0.w)), vmax(vmax(b1.x, b1.y), vmax(b1.z, b1.w))), vmax(vmax(b2.x, b2.y), vmax(b2.z, b2.w)));
}
__device__ __forceinline__ unsigned lds_inc(LAS unsigned* c) {
  asm volatile("" ::: "memory");
  unsigned old = 0;
  if ((threadIdx.x & 63) == 0) old = __hip_atomic_fetch_add(c, 1u, __ATOMIC_RELAXED, __HIP_MEMORY_SCOPE_WORKGROUP);
  old = (unsigned)__builtin_amdgcn_readfirstlane((int)old);
  asm volatile("" ::: "memory");
  return old;
}
__device__ __forceinline__ void lds_wait(LAS unsigned* c, unsigned need) {
  for (unsigned spin = 0; *(volatile LAS unsigned*)c < need && spin < (1u << 20); ++spin) {}
  asm volatile("" ::: "memory");
}
__device__ __forceinline__ void make_lane(Lane& ln, int lane, LAS unsigned* sh) {
  const unsigned mm = (lane & 1) ? 0xFFFF0000u : 0x0000FFFFu;
  const int d = (lane & 7) >> 1, c = lane & 15, g = lane >> 4;
  ln.k.m0 = d == 0 ? mm : 0u; ln.k.m1 = d == 1 ? mm : 0u; ln.k.m2 = d == 2 ? mm : 0u; ln.k.m3 = d == 3 ? mm : 0u;
  ln.shw = sh + 32 * (c & 1) + 8 * g + (c >> 1);
  ln.shr = sh + (lane >> 3) * 8;
  ln.put = (LAS float*)0 + (c >> 3) * 64 + 8 * (c & 7) + 4 * (g & 1);
  ln.putv = (c >> 3) == (g >> 1);
  pin(ln.k.m0); pin(ln.k.m1); pin(ln.k.m2); pin(ln.k.m3); pin(ln.shw); pin(ln.shr); pin(ln.put);
}
__device__ __forceinline__ void put_halves(const Lane& ln, int base_dw, f32x4 a) {
  if (ln.putv) *(LAS f32x4*)(ln.put + base_dw) = a;
}
__device__ __forceinline__ void modb_finish(const Tile18& t, LAS unsigned* sh, const Lane& ln, LAS float* Lf, int red_dw, LAS unsigned* cnt, int ks, int lane,
                                            rsrc_t ws, int tile, unsigned T) {
  put_halves(ln, red_dw + ks * 128, tile18_mac(t, sh, lane, ln.k));
  if (lds_inc(cnt) == 3u) stsc1(ws, tg(rb(sumN<8>(Lf + red_dw + lane)), T), lane * 4, WS_SB + tile * 256);
}

__global__ void __launch_bounds__(NTHR, 3) fused_decode(Params p) {
  __shared__ __attribute__((aligned(16))) unsigned smem[L_END];
  LAS unsigned* L = (LAS unsigned*)smem;
  LAS float* Lf = (LAS float*)smem;
  const int b = blockIdx.x, xcd = b & 7, slot = b >> 3;
  const int lane = threadIdx.x & 63, w = __builtin_amdgcn_readfirstlane(threadIdx.x >> 6);
  const int c4 = lane & 15, row = lane >> 4;
  const int col = 4 * c4 + row;
  STAMP(0);
#define LOAD_EPOCH() __builtin_amdgcn_sched_barrier(0); const unsigned Ev = __builtin_amdgcn_raw_buffer_load_b32(ws, 0, WS_EPOCH, 16); __builtin_amdgcn_sched_barrier(0)
#define GET_TAG() const unsigned E = (unsigned)__builtin_amdgcn_readfirstlane((int)Ev); const unsigned T = (E + 1u) & 0xFFu; (void)E
  LAS unsigned* sh = L + L_SH + w * SH_STRIDE;
  const int lbase = (int)(size_t)(LAS void*)smem;
  Lane ln;

  if (slot <= 9) {
  const int rgate = (int)((size_t)p.wg >> 7), xstar = (3 - rgate) & 7;
  const bool qonly = slot == 9 && xcd == xstar;
  if (slot < 9 || qonly) {
    if (w >= 10) return;
    const int f = xcd * 9 + slot;
    __builtin_amdgcn_sched_barrier(0);
    const rsrc_t ws = mk(p.ws, WS_END), rwm = mk(p.wm, D * 6 * D * 2);
    if (w == 9) {
      __builtin_amdgcn_s_setprio(3);
      if (lane == 0) L[L_CNT] = 0u;
      if (qonly) { bar(); return; }
      Tile18 t;
      tile18_load(t, p.c, rwm, f / 18, (f % 18) * 128, lane);
      LOAD_EPOCH();
      bar();
      make_lane(ln, lane, sh);
      GET_TAG();
      const f32x4 a = fold(tile18_mac(t, sh, lane, ln.k));
      if (c4 < 8 && row < 2) stsc4(ws, v4u{tgr(a.x, T), tgr(a.y, T), tgr(a.z, T), tgr(a.w, T)}, (8 * c4 + 4 * row) * 4, WS_S1 + f * 256);
      STAMP(1);
      FINISH();
      return;
    }
    const int j = w;
    const int s0 = (3 - rgate - xcd) & 7;
    const bool hasq = qonly || !(slot == s0 || (s0 == 0 && slot == 8));
    const int n = qonly ? xcd * 8 + 7 : xcd * 8 + slot - (slot > s0 ? 1 : 0);
    const u16 xr = p.x[64 * j + col], x0r = p.x[0];
    LOAD_EPOCH();
    rawbar();
    v4u wt[8], wg[8], wu[8], wd[8];
    const rsrc_t rg = mk(p.wg, D * FF * 2), ru = mk(p.wu, D * FF * 2), rd = mk(p.wd, FF * D * 2);
    loads8(wt, mk(p.wq, hasq ? D * 4096 * 2 : 0), 8192, lane, 64 * j * 8192 + n * 128);
    __builtin_amdgcn_sched_barrier(0);
    make_lane(ln, lane, sh);
    ln.put += lbase / 4;
    GET_TAG();
    const float x0 = bf(x0r);
    float xv = bf(xr);
    {
      const float d = xv - x0;
      const float s = wave_sum2(d, d * d);
      if ((lane & 47) == 0) Lf[L_ST + 2 * j + (lane >> 4)] = s;
    }
    lds_arrive(L + L_CNT);
    lds_wait(L + L_CNT, 9u);
    float mean1, lnv;
    {
      const f32x2 s = ln_sums(Lf + L_ST);
      const float dm = s.x * (1.f / D);
      mean1 = x0 + dm;
      const float var = fmaxf(s.y * (1.f / D) - dm * dm, 0.f);
      const float den = rb(__builtin_amdgcn_sqrtf(rb(rb(var) + 1e-5f)));
      lnv = rb(rb(xv - rb(mean1)) / den);
      pin(lnv); pin(mean1); pin(xv);
    }
    STAMP(1);
    if (hasq) {
      int o1 = (row * 18 + j) * 256 + c4 * 16;
      pin(o1);
      __builtin_amdgcn_s_waitcnt(0x0F70 | PRE_WAIT);
      v4u sa, ca;
      for (unsigned spin = 0;; ++spin) {
        asm volatile("" ::: "memory");
        sa = ldsc(ws, o1, WS_S1); ca = ldsc(ws, o1, WS_S1 + 9 * 256);
        __builtin_amdgcn_sched_barrier(0);
        sa ^= T; ca ^= T;
        if (allok(or4(sa) | or4(ca)) || spin > SPIN_MAX) break;
        __builtin_amdgcn_s_sleep(2);
      }
      const float h = rb(lnv * rb(1.f + rb(tr4(asf4(ca))))) + rb(tr4(asf4(sa)));
      STAMP(2);
      v4u h0, h1;
      shuf(ln.shw, ln.shr, h, h0, h1);
      put_halves(ln, L_REDA + j * 128, gemv8(wt, h0, h1, ln.k));
      bar();
      if (j == 0) stsc1(ws, tg(rb(sumN<18>(Lf + L_REDA + lane)), T), lane * 4, WS_SQ + n * 256);
      rawbar();
    }
    if (qonly) { FINISH(); return; }
    STAMP(3);
    loads8(wg, rg, FF * 2, lane, 64 * j * FF * 2 + f * 128);
    loads8(wu, ru, FF * 2, lane, 64 * j * FF * 2 + f * 128);
    loads8(wd, rd, 1152, lane, 64 * f * 1152 + j * 128);
    __builtin_amdgcn_sched_barrier(0);
    int o4 = (j * 16 + row) * 256 + c4 * 16, ob = j * 256 + col * 4, o5 = (8 * c4 + 4 * row) * 4;
    LAS float* redg_w = Lf + L_REDG + j * 128 + (row >> 1) * 64 + 8 * c4 + 4 * (row & 1);
    LAS float* redg_r = Lf + L_REDG + lane;
    LAS unsigned* shw2 = L + L_Q + (lane & 7) * 8 + (lane >> 3);
    LAS unsigned* shr2 = L + L_Q + (lane >> 3) * 8;
    pin(o4); pin(ob); pin(o5); pin(redg_w); pin(redg_r); pin(shw2); pin(shr2);
    asm volatile("s_waitcnt vmcnt(0)" ::: "memory");
    STAMP(8);
    rawbar();
    STAMP(4);
    float x1, sb, cb;
    {
      unsigned a, s2, c2;
      v4u q0, q1, q2, q3;
      q0 = ldsc(ws, o4, WS_S4); q1 = ldsc(ws, o4, WS_S4 + 1024); q2 = ldsc(ws, o4, WS_S4 + 2048); q3 = ldsc(ws, o4, WS_S4 + 3072);
      a = ldsc1(ws, ob, WS_SB); s2 = ldsc1(ws, ob, WS_SB + 9 * 256); c2 = ldsc1(ws, ob, WS_SB + 18 * 256);
      for (unsigned spin = 0; spin <= SPIN_MAX; ++spin) {
        asm volatile("" ::: "memory");
        bool again = false;
        if (!allok(or4(q0 ^ T))) { q0 = ldsc(ws, o4, WS_S4); again = true; }
        if (!allok(or4(q1 ^ T))) { q1 = ldsc(ws, o4, WS_S4 + 1024); again = true; }
        if (!allok(or4(q2 ^ T))) { q2 = ldsc(ws, o4, WS_S4 + 2048); again = true; }
        if (!allok(or4(q3 ^ T))) { q3 = ldsc(ws, o4, WS_S4 + 3072); again = true; }
        if (!allok((a ^ T) | (s2 ^ T) | (c2 ^ T))) {
          a = ldsc1(ws, ob, WS_SB); s2 = ldsc1(ws, ob, WS_SB + 9 * 256); c2 = ldsc1(ws, ob, WS_SB + 18 * 256);
          again = true;
        }
        if (!again) break;
      }
      q0 ^= T; q1 ^= T; q2 ^= T; q3 ^= T; a ^= T; s2 ^= T; c2 ^= T;
      x1 = rb(xv + rb(asf(a) * rb(tr4((asf4(q0) + asf4(q1)) + (asf4(q2) + asf4(q3))))));
      sb = asf(s2); cb = rb(1.f + asf(c2));
    }
    STAMP(5);
    {
      const float d = x1 - mean1;
      const float s = wave_sum2(d, d * d);
      if ((lane & 47) == 0) Lf[L_ST + 32 + 2 * j + (lane >> 4)] = s;
    }
    bar();
    {
      const f32x2 s = ln_sums(Lf + L_ST + 32);
      const float dm = s.x * (1.f / D);
      const float var = fmaxf(s.y * (1.f / D) - dm * dm, 0.f);
      const float den = rb(__builtin_amdgcn_sqrtf(rb(rb(var) + 1e-5f)));
      const float h2 = rb(rb(rb(x1 - rb(mean1 + dm)) * __builtin_amdgcn_rcpf(den)) * cb) + sb;
      v4u h0, h1;
      shuf(ln.shw, ln.shr, h2, h0, h1);
      const f32x4 g = gemv8(wg, h0, h1, ln.k), u = gemv8(wu, h0, h1, ln.k);
      const f32x4 t = {fold2_1(g.x, u.x), fold2_1(g.y, u.y), fold2_1(g.z, u.z), fold2_1(g.w, u.w)};
      if (c4 < 8) *(LAS f32x4*)redg_w = t;
    }
    bar();
    STAMP(6);
    if (j == 1) {
      float g = 0.f, u = 0.f, g2 = 0.f, u2 = 0.f;
#pragma unroll
      for (int i = 0; i < 8; i += 2) {
        g += redg_r[i * 128]; u += redg_r[i * 128 + 64];
        g2 += redg_r[i * 128 + 128]; u2 += redg_r[i * 128 + 192];
      }
      g = rb((g + redg_r[1024]) + g2); u = rb((u + redg_r[1088]) + u2);
      const float z = 1.5957691216057308f * (g + 0.044715f * g * g * g);
      *shw2 = rep(rb(g * __builtin_amdgcn_rcpf(1.f + __expf(-z))) * u);
    }
    bar();
    {
      const v4u h0 = *(LAS v4u*)shr2, h1 = *(LAS v4u*)(shr2 + 4);
      const f32x4 y = fold(gemv8(wd, h0, h1, ln.k));
      if (c4 < 8 && row < 2) stsc4(ws, tg4(y, T), o5, WS_S5 + (j * 72 + f) * 256);
    }
    STAMP(7);
    FINISH();
    return;
  }

  {
    int tile = 16 + (w >> 2) * 8 + xcd;
    if (w >= 8 && xcd >= 4) {
      const int r = xcd - 4 - ((xstar >= 4 && xcd > xstar) ? 1 : 0);
      if (r >= (xstar < 4 ? 3 : 2)) return;
      tile = 16 + r * 8 + xstar;
    }
    if (threadIdx.x < 3) L[L_CNT + threadIdx.x] = 0u;
    const rsrc_t ws = mk(p.ws, WS_END), rwm = mk(p.wm, D * 6 * D * 2);
    Tile18 t;
    tile18_load(t, p.c, rwm, w & 3, 2304 + tile * 128, lane);
    LOAD_EPOCH();
    __builtin_amdgcn_sched_barrier(0);
    bar();
    make_lane(ln, lane, sh);
    GET_TAG();
    modb_finish(t, sh, ln, Lf, L_REDA + (w >> 2) * 512, L + L_CNT + (w >> 2), w & 3, lane, ws, tile, T);
    STAMP(7);
    FINISH();
    return;
  }

  }
  const int g1 = (slot + 11) >> 5, g = 2 * xcd + g1, k = slot - 10 - 11 * g1;
  const bool light = k == 10;
  const int s3 = WS_S3 + (g * 11 + k) * S3_STRIDE;
  __builtin_amdgcn_sched_barrier(0);
  const rsrc_t ws = mk(p.ws, WS_END), rwm = mk(p.wm, D * 6 * D * 2);
  unsigned E = 0, T = 0;
  v4u wt[8];
  const rsrc_t rwo = mk(p.wo, (k < 9 && w < 4) ? 4096 * D * 2 : 0);
  if (light && w >= 8) {
    Tile18 t;
    tile18_load(t, p.c, rwm, w - 8, 2304 + g * 128, lane);
    LOAD_EPOCH();
    if (w == 8 && lane < 32) Lf[L_MW + lane] = -INFINITY;
    if (w == 9 && lane < 32) Lf[L_LW + lane] = 0.f;
    if (w == 10 && lane < 3) L[L_CNT + lane] = 0u;
    bar();
    make_lane(ln, lane, sh);
    E = (unsigned)__builtin_amdgcn_readfirstlane((int)Ev); T = (E + 1u) & 0xFFu;
    modb_finish(t, sh, ln, Lf, L_REDA, L + L_CNT + 1, w - 8, lane, ws, g, T);
    STAMP(1);
    rawbar();
    rawbar();
  } else {
    const size_t kvoff = ((size_t)g * SK + (SK - WIN) + k * 384 + w * 32) * 128;
    const rsrc_t rk = mk(p.kc + kvoff, 8192), rv = mk(p.vc + kvoff, 8192);
    v4u kk[8], vv[8];
    if (WO_POS == 0) loads8(wt, rwo, 1152, lane, (256 * g + 64 * (w & 3)) * 1152 + k * 128);
    const int kvo = (lane & 7) * 256 + ((lane >> 3) & 1) * 64 + row * 16, vvo = row * 2048 + c4 * 16;
#pragma unroll
    for (int t = 0; t < 4; ++t) {
      kk[2 * t] = ldk(rk, kvo, t * 2048);
      kk[2 * t + 1] = ldk(rk, kvo + 128, t * 2048);
    }
    LOAD_EPOCH();
    if (!light) {
      if (w == 8 && lane < 32) Lf[L_MW + lane] = -INFINITY;
      if (w == 9 && lane < 32) Lf[L_LW + lane] = 0.f;
      if (w == 10 && lane < 3) L[L_CNT + lane] = 0u;
    }
    __builtin_amdgcn_sched_barrier(0);
    bar();
    if (WO_POS == 1) loads8(wt, rwo, 1152, lane, (256 * g + 64 * (w & 3)) * 1152 + k * 128);
#pragma unroll
    for (int s = 0; s < 8; ++s) vv[s] = ldv(rv, vvo + s * 256, 0);
    __builtin_amdgcn_sched_barrier(0);
    make_lane(ln, lane, sh);
    ln.put += lbase / 4;
    E = (unsigned)__builtin_amdgcn_readfirstlane((int)Ev); T = (E + 1u) & 0xFFu;
    LAS unsigned* qrd = L + L_Q + (lane & 1) * 64 + ((lane >> 1) & 1) * 16 + row * 4;
    LAS float* mwr = Lf + L_MW + ((lane >> 3) & 1) * 16;
    LAS float* owr = Lf + L_O + w * 256 + (row >> 1) * 128 + 8 * c4 + 4 * (row & 1);
    pin(qrd); pin(mwr); pin(owr);
    STAMP(1);
    if (w == 0) {
      const int oq = lane * 16, sq = WS_SQ + g * 1024;
      v4u q;
      for (unsigned spin = 0;; ++spin) {
        asm volatile("" ::: "memory");
        q = ldsc(ws, oq, sq) ^ T;
        if (allok(or4(q)) || spin > SPIN_MAX) break;
        __builtin_amdgcn_s_sleep(2);
      }
      *(LAS v2u*)(L + L_Q + lane * 2) = v2u{(q.x >> 16) | q.y, (q.z >> 16) | q.w};
    }
    bar();
    STAMP(2);
    float sc = 0.f;
    {
      const v4u q0 = *(LAS v4u*)qrd, q1 = *(LAS v4u*)(qrd + 32);
#pragma unroll
      for (int t = 0; t < 4; ++t) {
        f32x4 d = {0.f, 0.f, 0.f, 0.f};
        d = mfma(q0, kk[2 * t], d);
        d = mfma(q1, kk[2 * t + 1], d);
        const float h0 = d.x + dpp<0x128>(d.z), h1 = d.w + dpp<0x128>(d.y);
        const float v = c4 < 8 ? h0 : h1;
        sc = row == t ? v : sc;
      }
      sc = rb(rb(sc) * 0.08838834764831845f);
      float m = sc;
      m = vmax(m, dpp<0xB1>(m)); m = vmax(m, dpp<0x4E>(m)); m = vmax(m, dpp<0x141>(m));
      m = xrow_max(m);
      if ((lane & 55) == 0) mwr[w] = m;
    }
    bar();
    STAMP(3);
    {
      const float M = max12(mwr);
      const float pe = __expf(sc - M);
      v4u h0, h1;
      shuf(sh + lane, ln.shr, pe, h0, h1);
      float l = pe;
      l += dpp<0xB1>(l); l += dpp<0x4E>(l); l += dpp<0x141>(l);
      l = xrow_sum(l);
      if ((lane & 55) == 0) mwr[32 + w] = l;
      lds_arrive(L + L_CNT + 2);
      v4u A[8];
      A[0] = diag(h0.x, ln.k); A[1] = diag(h0.y, ln.k); A[2] = diag(h0.z, ln.k); A[3] = diag(h0.w, ln.k);
      A[4] = diag(h1.x, ln.k); A[5] = diag(h1.y, ln.k); A[6] = diag(h1.z, ln.k); A[7] = diag(h1.w, ln.k);
      __builtin_amdgcn_sched_barrier(0);
      f32x4 o0 = {0.f, 0.f, 0.f, 0.f}, o1 = {0.f, 0.f, 0.f, 0.f};
      o0 = mfma(A[0], vv[0], o0); o1 = mfma(A[1], vv[1], o1); o0 = mfma(A[2], vv[2], o0); o1 = mfma(A[3], vv[3], o1);
      o0 = mfma(A[4], vv[4], o0); o1 = mfma(A[5], vv[5], o1); o0 = mfma(A[6], vv[6], o0); o1 = mfma(A[7], vv[7], o1);
      *(LAS f32x4*)owr = o0 + o1;
      lds_arrive(L + L_CNT);
    }
    STAMP(4);
    if (w >= 7) __builtin_amdgcn_s_setprio(3);
    if (w == 7) {
      lds_wait(L + L_CNT + 2, light ? 8u : 12u);
      LAS float* lw = Lf + L_LW + (lane & 1) * 16;
      LAS float* mw = Lf + L_MW + (lane & 1) * 16;
      const f32x4 a0 = *(LAS f32x4*)lw, a1 = *(LAS f32x4*)(lw + 4), a2 = *(LAS f32x4*)(lw + 8);
      const float l = ((a0.x + a0.y) + (a0.z + a0.w)) + ((a1.x + a1.y) + (a1.z + a1.w)) + ((a2.x + a2.y) + (a2.z + a2.w));
      const float M = max12(mw);
      if (lane < 2) __builtin_amdgcn_raw_buffer_store_b64(v2u{tg(M, T), tg(l, T)}, ws, lane * 8, s3 + 1024, 16);
    }
  }
  if (w >= 8) {
    __builtin_amdgcn_s_setprio(3);
    const int wp = w - 8;
    LAS float* o = Lf + L_O + row * 256 + wp * 64 + c4 * 4;
    int so = wp * 256 + col * 4;
    pin(o); pin(so);
    lds_wait(L + L_CNT, light ? 8u : 12u);
    f32x4 v = *(LAS f32x4*)o + *(LAS f32x4*)(o + 1024);
    if (!light) v += *(LAS f32x4*)(o + 2048);
    stsc1(ws, tg(tr4(v), T), so, s3);
    STAMP(5);
    if (!(light && g < 9 && w == 8)) { FINISH(); return; }
  }
  if (k < 9) {
    if (w >= 4) { FINISH(); return; }
    if (WO_POS == 2) loads8(wt, rwo, 1152, lane, (256 * g + 64 * w) * 1152 + k * 128);
    __builtin_amdgcn_sched_barrier(0);
    const int k2 = row + 8 > 10 ? 10 : row + 8;
    const int gb = WS_S3 + g * 11 * S3_STRIDE, hd = (w >> 1) * 8;
    int oa = row * S3_STRIDE + w * 256 + c4 * 16, oc = k2 * S3_STRIDE + w * 256 + c4 * 16;
    int ma_ = row * S3_STRIDE + 1024 + hd, mc_ = k2 * S3_STRIDE + 1024 + hd;
    pin(oa); pin(oc); pin(ma_); pin(mc_);
    v2u m0, m1, m2;
    v4u o0, o1, o2;
    for (unsigned spin = 0;; ++spin) {
      asm volatile("" ::: "memory");
      m0 = ldsc2(ws, ma_, gb); m1 = ldsc2(ws, ma_, gb + 4 * S3_STRIDE); m2 = ldsc2(ws, mc_, gb);
      o0 = ldsc(ws, oa, gb); o1 = ldsc(ws, oa, gb + 4 * S3_STRIDE); o2 = ldsc(ws, oc, gb);
      __builtin_amdgcn_sched_barrier(0);
      m0 ^= T; m1 ^= T; m2 ^= T; o0 ^= T; o1 ^= T; o2 ^= T;
      if (allok((m0.x | m0.y) | (m1.x | m1.y) | (m2.x | m2.y) | or4(o0) | or4(o1) | or4(o2)) || spin > SPIN_MAX) break;
      __builtin_amdgcn_s_sleep(POLL_SLEEP);
    }
    const float ma = asf(m0.x), mb = asf(m1.x), mc = row == 3 ? -INFINITY : asf(m2.x);
    const float MM = xrow_max(vmax(vmax(ma, mb), mc));
    float fa = __expf(ma - MM), fb = __expf(mb - MM), fc = __expf(mc - MM);
    const float inv = __builtin_amdgcn_rcpf(xrow_sum(fa * asf(m0.y) + fb * asf(m1.y) + fc * asf(m2.y)));
    STAMP(6);
    v4u h0, h1;
    shuf(ln.shw, ln.shr, tr4(asf4(o0) * fa + asf4(o1) * fb + asf4(o2) * fc) * inv, h0, h1);
    put_halves(ln, L_REDA + w * 128, gemv8(wt, h0, h1, ln.k));
    bar();
    if (w == 0) stsc1(ws, tg(sumN<8>(Lf + L_REDA + lane), T), lane * 4, WS_S4 + (k * 16 + g) * 256);
    STAMP(7);
    FINISH();
    return;
  }
  if (!(light && g < 9) || w > 8) { FINISH(); return; }
  {
    const int j = g;
    float x1 = 0.f, gbv = 0.f;
    if (w == 0) {
      const float xv = bf(p.x[64 * j + col]);
      unsigned a2, b2;
      v4u q0, q1, q2, q3;
      for (unsigned spin = 0;; ++spin) {
        asm volatile("" ::: "memory");
        const int vo = (j * 16 + row) * 256 + c4 * 16;
        a2 = ldsc1(ws, col * 4, WS_SB + j * 256); b2 = ldsc1(ws, col * 4, WS_SB + (27 + j) * 256);
        q0 = ldsc(ws, vo, WS_S4); q1 = ldsc(ws, vo, WS_S4 + 1024); q2 = ldsc(ws, vo, WS_S4 + 2048); q3 = ldsc(ws, vo, WS_S4 + 3072);
        __builtin_amdgcn_sched_barrier(0);
        q0 ^= T; q1 ^= T; q2 ^= T; q3 ^= T; a2 ^= T; b2 ^= T;
        if (allok(a2 | b2 | or4(q0) | or4(q1) | or4(q2) | or4(q3)) || spin > SPIN_MAX) break;
        __builtin_amdgcn_s_sleep(8);
      }
      x1 = rb(xv + rb(asf(a2) * rb(tr4((asf4(q0) + asf4(q1)) + (asf4(q2) + asf4(q3))))));
      gbv = asf(b2);
      pin(x1); pin(gbv);
    }
    STAMP(6);
    {
      v4u y0, y1;
      int vo = (8 * w + row) * 256 + c4 * 16;
      LAS float* rw = Lf + L_REDG + w * 64 + col;
      pin(vo); pin(rw);
      for (unsigned spin = 0;; ++spin) {
        asm volatile("" ::: "memory");
        y0 = ldsc(ws, vo, WS_S5 + j * 72 * 256); y1 = ldsc(ws, vo, WS_S5 + j * 72 * 256 + 1024);
        __builtin_amdgcn_sched_barrier(0);
        y0 ^= T; y1 ^= T;
        if (allok(or4(y0) | or4(y1)) || spin > SPIN_MAX) break;
        __builtin_amdgcn_s_sleep(1);
      }
      *rw = tr4(asf4(y0) + asf4(y1));
    }
    bar();
    if (w == 0) {
      LAS float* r = Lf + L_REDG + col;
      const float y = ((r[0] + r[64]) + (r[128] + r[192])) + ((r[256] + r[320]) + (r[384] + r[448])) + r[512];
      f32x2 o = {x1 + rb(gbv * rb(y)), 0.f};
      p.out[64 * j + col] = (u16)(__builtin_bit_cast(unsigned, __builtin_convertvector(o, bf16x2)) & 0xFFFFu);
      if (j == 0 && lane == 0) __builtin_amdgcn_raw_buffer_store_b32(E + 1u, ws, 0, WS_EPOCH, 16);
    }
    STAMP(7);
    FINISH();
  }
}

}

extern "C" void kernel_launch(void* const* d_in, const int* in_sizes, int n_in, void* d_out, int out_size, void* d_ws, size_t ws_size,
                              hipStream_t stream) {
  Params p;
  p.x = (const u16*)d_in[0]; p.kc = (const u16*)d_in[1]; p.vc = (const u16*)d_in[2]; p.wq = (const u16*)d_in[3]; p.wo = (const u16*)d_in[4];
  p.wg = (const u16*)d_in[6]; p.wu = (const u16*)d_in[7]; p.wd = (const u16*)d_in[8]; p.c = (const u16*)d_in[9]; p.wm = (const u16*)d_in[10];
  p.out = (u16*)d_out; p.ws = (unsigned char*)d_ws;
  fused_decode<<<256, NTHR, 0, stream>>>(p);
}
```

```cpp
#include <hip/hip_runtime.h>
#include <hip/hip_bf16.h>
#include <stdint.h>

namespace {

typedef unsigned short u16;
typedef unsigned v4u __attribute__((ext_vector_type(4)));
typedef unsigned v2u __attribute__((ext_vector_type(2)));
typedef float f32x4 __attribute__((ext_vector_type(4)));
typedef float f32x2 __attribute__((ext_vector_type(2)));
typedef __bf16 bf16x8 __attribute__((ext_vector_type(8)));
typedef __bf16 bf16x2 __attribute__((ext_vector_type(2)));
typedef __amdgpu_buffer_rsrc_t rsrc_t;
#define LAS __attribute__((address_space(3)))

constexpr int D = 576, SK = 65536, WIN = 4096, FF = 4608;
constexpr int NTHR = 768;
constexpr int WS_EPOCH = 0;
constexpr int WS_S1 = 4096;
constexpr int WS_SQ = WS_S1 + 72 * 256;
constexpr int WS_SB = WS_SQ + 64 * 256;
constexpr int WS_S4 = WS_SB + 36 * 256;
constexpr int WS_S5 = WS_S4 + 144 * 256;
constexpr int WS_S3 = WS_S5 + 648 * 256;
constexpr int S3_STRIDE = 1280;
constexpr int WS_END = WS_S3 + 176 * S3_STRIDE;
constexpr int WS_STAMP = 1 << 20;
constexpr unsigned SPIN_MAX = 1u << 14;
#define POLL_SLEEP 1
#define PRE_WAIT 2
#define WO_POS 1
#define DELAY_A 0
#define DELAY_F 0

struct Params {
  const u16 *x, *kc, *vc, *wq, *wo, *wg, *wu, *wd, *c, *wm;
  u16* out;
  unsigned char* ws;
};

constexpr int SH_STRIDE = 160;
constexpr int L_SH = 0;
constexpr int L_REDA = L_SH + 12 * SH_STRIDE;
constexpr int L_REDG = L_REDA + 1536;
constexpr int L_ST = L_REDG + 1152;
constexpr int L_Q = L_ST + 64;
constexpr int L_MW = L_Q + 128;
constexpr int L_LW = L_MW + 32;
constexpr int L_CNT = L_LW + 32;
constexpr int L_O = L_CNT + 16;
constexpr int L_END = L_O + 12 * 256;

__device__ __forceinline__ rsrc_t mk(const void* p, unsigned bytes) {
  return __builtin_amdgcn_make_buffer_rsrc((void*)p, 0, (int)bytes, 0x00020000);
}
__device__ __forceinline__ v4u ldnt(rsrc_t r, int v, int s) { return __builtin_amdgcn_raw_buffer_load_b128(r, v, s, 2); }
__device__ __forceinline__ v4u ldsc(rsrc_t r, int v, int s) { return __builtin_amdgcn_raw_buffer_load_b128(r, v, s, 16); }
__device__ __forceinline__ v2u ldsc2(rsrc_t r, int v, int s) { return __builtin_amdgcn_raw_buffer_load_b64(r, v, s, 16); }
__device__ __forceinline__ unsigned ldsc1(rsrc_t r, int v, int s) { return __builtin_amdgcn_raw_buffer_load_b32(r, v, s, 16); }
__device__ __forceinline__ void stsc1(rsrc_t r, unsigned x, int v, int s) { __builtin_amdgcn_raw_buffer_store_b32(x, r, v, s, 16); }
__device__ __forceinline__ void stsc4(rsrc_t r, v4u x, int v, int s) { __builtin_amdgcn_raw_buffer_store_b128(x, r, v, s, 16); }
__device__ __forceinline__ float asf(unsigned x) { return __uint_as_float(x); }
__device__ __forceinline__ unsigned asu(float x) { return __float_as_uint(x); }
template <class V> __device__ __forceinline__ void pin(V& x) { asm volatile("" : "+v"(x)); }
__device__ __forceinline__ unsigned rep(float x) {
  f32x2 v = {x, x};
  return __builtin_bit_cast(unsigned, __builtin_convertvector(v, bf16x2));
}
__device__ __forceinline__ float rb(float x) { return asf(rep(x) & 0xFFFF0000u); }
__device__ __forceinline__ float bf(u16 x) { return asf((unsigned)x << 16); }
__device__ __forceinline__ unsigned tg(float x, unsigned T) { return (asu(x) & 0xFFFFFF00u) | T; }
__device__ __forceinline__ unsigned tgr(float x, unsigned T) { return ((asu(x) + 0x80u) & 0xFFFFFF00u) | T; }
__device__ __forceinline__ v4u tg4(f32x4 y, unsigned T) { return v4u{tg(y.x, T), tg(y.y, T), tg(y.z, T), tg(y.w, T)}; }
__device__ __forceinline__ unsigned or4(v4u d) { return (d.x | d.y) | (d.z | d.w); }
__device__ __forceinline__ bool allok(unsigned bad) { return __all((bad & 0xFFu) == 0u); }
template <int C> __device__ __forceinline__ float dpp(float x) {
  return asf((unsigned)__builtin_amdgcn_update_dpp(0, (int)asu(x), C, 0xF, 0xF, true));
}
__device__ __forceinline__ float xrow_sum(float x) {
  auto s = __builtin_amdgcn_permlane16_swap(asu(x), asu(x), false, false);
  x = asf(s[0]) + asf(s[1]);
  auto t = __builtin_amdgcn_permlane32_swap(asu(x), asu(x), false, false);
  return asf(t[0]) + asf(t[1]);
}
__device__ __forceinline__ float xrow_max(float x) {
  auto s = __builtin_amdgcn_permlane16_swap(asu(x), asu(x), false, false);
  x = __builtin_amdgcn_fmed3f(asf(s[0]), asf(s[1]), INFINITY);
  auto t = __builtin_amdgcn_permlane32_swap(asu(x), asu(x), false, false);
  return __builtin_amdgcn_fmed3f(asf(t[0]), asf(t[1]), INFINITY);
}
__device__ __forceinline__ float tr4(f32x4 v) {
  auto a = __builtin_amdgcn_permlane16_swap(asu(v.x), asu(v.y), false, false);
  auto b = __builtin_amdgcn_permlane16_swap(asu(v.z), asu(v.w), false, false);
  const float s01 = asf(a[0]) + asf(a[1]), s23 = asf(b[0]) + asf(b[1]);
  auto c = __builtin_amdgcn_permlane32_swap(asu(s01), asu(s23), false, false);
  return asf(c[0]) + asf(c[1]);
}
__device__ __forceinline__ float wave_sum2(float x, float y) {
  x += dpp<0xB1>(x); y += dpp<0xB1>(y);
  x += dpp<0x4E>(x); y += dpp<0x4E>(y);
  x += dpp<0x141>(x); y += dpp<0x141>(y);
  x += dpp<0x140>(x); y += dpp<0x140>(y);
  auto a = __builtin_amdgcn_permlane16_swap(asu(x), asu(y), false, false);
  const float s = asf(a[0]) + asf(a[1]);
  auto c = __builtin_amdgcn_permlane32_swap(asu(s), asu(s), false, false);
  return asf(c[0]) + asf(c[1]);
}
__device__ __forceinline__ f32x4 asf4(v4u d) { return __builtin_bit_cast(f32x4, d); }
__device__ __forceinline__ f32x4 mfma(v4u a, v4u b, f32x4 c) {
  return __builtin_amdgcn_mfma_f32_16x16x32_bf16(__builtin_bit_cast(bf16x8, a), __builtin_bit_cast(bf16x8, b), c, 0, 0, 0);
}
struct Mask { unsigned m0, m1, m2, m3; };
__device__ __forceinline__ v4u diag(unsigned hh, const Mask& k) { return v4u{hh & k.m0, hh & k.m1, hh & k.m2, hh & k.m3}; }
__device__ __forceinline__ void bar() {
  asm volatile("s_waitcnt lgkmcnt(0)" ::: "memory");
  __builtin_amdgcn_s_barrier();
  asm volatile("" ::: "memory");
}
__device__ __forceinline__ void rawbar() {
  __builtin_amdgcn_sched_barrier(0);
  __builtin_amdgcn_s_barrier();
  __builtin_amdgcn_sched_barrier(0);
}
__device__ __forceinline__ float silu(float v) { return v * __builtin_amdgcn_rcpf(1.f + __expf(-v)); }
__device__ __forceinline__ float fold1(float x) {
  auto t = __builtin_amdgcn_permlane32_swap(asu(x), asu(x), false, false);
  return asf(t[0]) + dpp<0x128>(asf(t[1]));
}
__device__ __forceinline__ f32x4 fold(f32x4 a) { return f32x4{fold1(a.x), fold1(a.y), fold1(a.z), fold1(a.w)}; }
__device__ __forceinline__ float fold2_1(float a, float b) {
  auto t = __builtin_amdgcn_permlane32_swap(asu(a), asu(b), false, false);
  return asf(t[0]) + dpp<0x128>(asf(t[1]));
}

#define STAMP(i) do {} while (0)
#define FINISH() do {} while (0)

struct Lane {
  Mask k;
  LAS unsigned* shw;
  LAS unsigned* shr;
  LAS float* put;
  bool putv;
};
__device__ __forceinline__ void shuf(LAS unsigned* wr, LAS unsigned* rd, float v, v4u& h0, v4u& h1) {
  *wr = rep(v);
  asm volatile("" ::: "memory");
  h0 = *(LAS v4u*)rd;
  h1 = *(LAS v4u*)(rd + 4);
}
__device__ __forceinline__ f32x4 gemv8(const v4u (&w)[8], v4u h0, v4u h1, const Mask& k) {
  f32x4 a = {0.f, 0.f, 0.f, 0.f}, b = {0.f, 0.f, 0.f, 0.f};
  const v4u a0 = diag(h0.x, k), a1 = diag(h0.y, k), a2 = diag(h0.z, k), a3 = diag(h0.w, k);
  a = mfma(a0, w[0], a); b = mfma(a1, w[1], b);
  const v4u a4 = diag(h1.x, k), a5 = diag(h1.y, k);
  a = mfma(a2, w[2], a); b = mfma(a3, w[3], b);
  const v4u a6 = diag(h1.z, k), a7 = diag(h1.w, k);
  a = mfma(a4, w[4], a); b = mfma(a5, w[5], b);
  a = mfma(a6, w[6], a); b = mfma(a7, w[7], b);
  return a + b;
}
__device__ __forceinline__ f32x2 ln_sums(LAS float* st) {
  const f32x4 a = *(LAS f32x4*)(st), b = *(LAS f32x4*)(st + 4), c = *(LAS f32x4*)(st + 8), d = *(LAS f32x4*)(st + 12);
  const f32x2 e = *(LAS f32x2*)(st + 16);
  const f32x4 t = (a + b) + (c + d);
  return f32x2{t.x + t.z + e.x, t.y + t.w + e.y};
}
template <int N> __device__ __forceinline__ float sumN(LAS float* red) {
  float a = 0.f, b = 0.f;
#pragma unroll
  for (int i = 0; i < N; i += 2) { a += red[i * 64]; b += red[i * 64 + 64]; }
  return a + b;
}
__device__ __forceinline__ void loads8(v4u (&w)[8], rsrc_t r, int stride, int lane, int base) {
  const int vo = (lane >> 3) * stride + (lane & 7) * 16;
#pragma unroll
  for (int s = 0; s < 8; ++s) w[s] = ldnt(r, vo, base + 8 * s * stride);
}
struct Tile18 { v4u w[18]; v2u c; };
__device__ __forceinline__ void tile18_load(Tile18& t, const u16* c, rsrc_t rwm, int ks, int cb, int lane) {
  t.c = *(const v2u*)(c + 144 * ks + 4 * (lane < 36 ? lane : 35));
  const int vo = (lane >> 3) * 6912 + (lane & 7) * 16;
#pragma unroll
  for (int s = 0; s < 18; ++s) t.w[s] = ldnt(rwm, vo, (144 * ks + 8 * s) * 6912 + cb);
}
__device__ __forceinline__ f32x4 tile18_mac(const Tile18& t, LAS unsigned* sh, int lane, const Mask& k) {
  if (lane < 36) {
    LAS unsigned* d = sh + (4 * (lane & 1)) * 20 + (lane >> 1);
    d[0] = rep(silu(asf(t.c.x << 16))); d[20] = rep(silu(asf(t.c.x & 0xFFFF0000u)));
    d[40] = rep(silu(asf(t.c.y << 16))); d[60] = rep(silu(asf(t.c.y & 0xFFFF0000u)));
  }
  asm volatile("" ::: "memory");
  LAS unsigned* s = sh + (lane >> 3) * 20;
  const v4u h0 = *(LAS v4u*)s, h1 = *(LAS v4u*)(s + 4), h2 = *(LAS v4u*)(s + 8), h3 = *(LAS v4u*)(s + 12);
  const v2u h4 = *(LAS v2u*)(s + 16);
  f32x4 a = {0.f, 0.f, 0.f, 0.f}, b = {0.f, 0.f, 0.f, 0.f};
  a = mfma(diag(h0.x, k), t.w[0], a); b = mfma(diag(h0.y, k), t.w[1], b); a = mfma(diag(h0.z, k), t.w[2], a); b = mfma(diag(h0.w, k), t.w[3], b);
  a = mfma(diag(h1.x, k), t.w[4], a); b = mfma(diag(h1.y, k), t.w[5], b); a = mfma(diag(h1.z, k), t.w[6], a); b = mfma(diag(h1.w, k), t.w[7], b);
  a = mfma(diag(h2.x, k), t.w[8], a); b = mfma(diag(h2.y, k), t.w[9], b); a = mfma(diag(h2.z, k), t.w[10], a); b = mfma(diag(h2.w, k), t.w[11], b);
  a = mfma(diag(h3.x, k), t.w[12], a); b = mfma(diag(h3.y, k), t.w[13], b); a = mfma(diag(h3.z, k), t.w[14], a); b = mfma(diag(h3.w, k), t.w[15], b);
  a = mfma(diag(h4.x, k), t.w[16], a); b = mfma(diag(h4.y, k), t.w[17], b);
  return a + b;
}
__device__ __forceinline__ void lds_arrive(LAS unsigned* c) {
  if ((threadIdx.x & 63) == 0) asm volatile("ds_add_u32 %0, %1" ::"v"(c), "v"(1u) : "memory");
  asm volatile("" ::: "memory");
}
__device__ __forceinline__ float vmax(float a, float b) { return __builtin_amdgcn_fmed3f(a, b, INFINITY); }
__device__ __forceinline__ float max12(LAS float* m) {
  const f32x4 b0 = *(LAS f32x4*)m, b1 = *(LAS f32x4*)(m + 4), b2 = *(LAS f32x4*)(m + 8);
  return vmax(vmax(vmax(vmax(b0.x, b0.y), vmax(b0.z, b0.w)), vmax(vmax(b1.x, b1.y), vmax(b1.z, b1.w))), vmax(vmax(b2.x, b2.y), vmax(b2.z, b2.w)));
}
__device__ __forceinline__ unsigned lds_inc(LAS unsigned* c) {
  asm volatile("" ::: "memory");
  unsigned old = 0;
  if ((threadIdx.x & 63) == 0) old = __hip_atomic_fetch_add(c, 1u, __ATOMIC_RELAXED, __HIP_MEMORY_SCOPE_WORKGROUP);
  old = (unsigned)__builtin_amdgcn_readfirstlane((int)old);
  asm volatile("" ::: "memory");
  return old;
}
__device__ __forceinline__ void lds_wait(LAS unsigned* c, unsigned need) {
  for (unsigned spin = 0; *(volatile LAS unsigned*)c < need && spin < (1u << 20); ++spin) {}
  asm volatile("" ::: "memory");
}
__device__ __forceinline__ void make_lane(Lane& ln, int lane, LAS unsigned* sh) {
  const unsigned mm = (lane & 1) ? 0xFFFF0000u : 0x0000FFFFu;
  const int d = (lane & 7) >> 1, c = lane & 15, g = lane >> 4;
  ln.k.m0 = d == 0 ? mm : 0u; ln.k.m1 = d == 1 ? mm : 0u; ln.k.m2 = d == 2 ? mm : 0u; ln.k.m3 = d == 3 ? mm : 0u;
  ln.shw = sh + 32 * (c & 1) + 8 * g + (c >> 1);
  ln.shr = sh + (lane >> 3) * 8;
  ln.put = (LAS float*)0 + (c >> 3) * 64 + 8 * (c & 7) + 4 * (g & 1);
  ln.putv = (c >> 3) == (g >> 1);
  pin(ln.k.m0); pin(ln.k.m1); pin(ln.k.m2); pin(ln.k.m3); pin(ln.shw); pin(ln.shr); pin(ln.put);
}
__device__ __forceinline__ void put_halves(const Lane& ln, int base_dw, f32x4 a) {
  if (ln.putv) *(LAS f32x4*)(ln.put + base_dw) = a;
}
__device__ __forceinline__ void modb_finish(const Tile18& t, LAS unsigned* sh, const Lane& ln, LAS float* Lf, int red_dw, LAS unsigned* cnt, int ks, int lane,
                                            rsrc_t ws, int tile, unsigned T) {
  put_halves(ln, red_dw + ks * 128, tile18_mac(t, sh, lane, ln.k));
  if (lds_inc(cnt) == 3u) stsc1(ws, tg(rb(sumN<8>(Lf + red_dw + lane)), T), lane * 4, WS_SB + tile * 256);
}

__global__ void __launch_bounds__(NTHR, 3) fused_decode(Params p) {
  __shared__ __attribute__((aligned(16))) unsigned smem[L_END];
  LAS unsigned* L = (LAS unsigned*)smem;
  LAS float* Lf = (LAS float*)smem;
  const int b = blockIdx.x, xcd = b & 7, slot = b >> 3;
  const int lane = threadIdx.x & 63, w = __builtin_amdgcn_readfirstlane(threadIdx.x >> 6);
  const int c4 = lane & 15, row = lane >> 4;
  const int col = 4 * c4 + row;
  STAMP(0);
#define LOAD_EPOCH() __builtin_amdgcn_sched_barrier(0); const unsigned Ev = __builtin_amdgcn_raw_buffer_load_b32(ws, 0, WS_EPOCH, 16); __builtin_amdgcn_sched_barrier(0)
#define GET_TAG() const unsigned E = (unsigned)__builtin_amdgcn_readfirstlane((int)Ev); const unsigned T = (E + 1u) & 0xFFu; (void)E
  LAS unsigned* sh = L + L_SH + w * SH_STRIDE;
  const int lbase = (int)(size_t)(LAS void*)smem;
  Lane ln;

  if (slot <= 9) {
  const int rgate = (int)((size_t)p.wg >> 7), xstar = (3 - rgate) & 7;
  const bool qonly = slot == 9 && xcd == xstar;
  if (slot < 9 || qonly) {
    if (w >= 10) return;
    const int f = xcd * 9 + slot;
    __builtin_amdgcn_sched_barrier(0);
    const rsrc_t ws = mk(p.ws, WS_END), rwm = mk(p.wm, D * 6 * D * 2);
    if (w == 9) {
      __builtin_amdgcn_s_setprio(3);
      if (lane == 0) L[L_CNT] = 0u;
      if (qonly) { bar(); return; }
      Tile18 t;
      tile18_load(t, p.c, rwm, f / 18, (f % 18) * 128, lane);
      LOAD_EPOCH();
      bar();
      make_lane(ln, lane, sh);
      GET_TAG();
      const f32x4 a = fold(tile18_mac(t, sh, lane, ln.k));
      if (c4 < 8 && row < 2) stsc4(ws, v4u{tgr(a.x, T), tgr(a.y, T), tgr(a.z, T), tgr(a.w, T)}, (8 * c4 + 4 * row) * 4, WS_S1 + f * 256);
      STAMP(1);
      FINISH();
      return;
    }
    const int j = w;
    const int s0 = (3 - rgate - xcd) & 7;
    const bool hasq = qonly || !(slot == s0 || (s0 == 0 && slot == 8));
    const int n = qonly ? xcd * 8 + 7 : xcd * 8 + slot - (slot > s0 ? 1 : 0);
    const u16 xr = p.x[64 * j + col], x0r = p.x[0];
    LOAD_EPOCH();
    rawbar();
    v4u wt[8], wg[8], wu[8], wd[8];
    const rsrc_t rg = mk(p.wg, D * FF * 2), ru = mk(p.wu, D * FF * 2), rd = mk(p.wd, FF * D * 2);
    loads8(wt, mk(p.wq, hasq ? D * 4096 * 2 : 0), 8192, lane, 64 * j * 8192 + n * 128);
    unsigned tg0 = __builtin_amdgcn_raw_buffer_load_b32(rg, 0, 64 * j * FF * 2 + f * 128, 2);
    unsigned tg1 = __builtin_amdgcn_raw_buffer_load_b32(ru, 0, 64 * j * FF * 2 + f * 128, 2);
    unsigned tg2 = __builtin_amdgcn_raw_buffer_load_b32(rd, 0, 64 * f * 1152 + j * 128, 2);
    __builtin_amdgcn_sched_barrier(0);
    make_lane(ln, lane, sh);
    ln.put += lbase / 4;
    GET_TAG();
    const float x0 = bf(x0r);
    float xv = bf(xr);
    {
      const float d = xv - x0;
      const float s = wave_sum2(d, d * d);
      if ((lane & 47) == 0) Lf[L_ST + 2 * j + (lane >> 4)] = s;
    }
    lds_arrive(L + L_CNT);
    lds_wait(L + L_CNT, 9u);
    float mean1, lnv;
    {
      const f32x2 s = ln_sums(Lf + L_ST);
      const float dm = s.x * (1.f / D);
      mean1 = x0 + dm;
      const float var = fmaxf(s.y * (1.f / D) - dm * dm, 0.f);
      const float den = rb(__builtin_amdgcn_sqrtf(rb(rb(var) + 1e-5f)));
      lnv = rb(rb(xv - rb(mean1)) / den);
      pin(lnv); pin(mean1); pin(xv);
    }
    STAMP(1);
    if (hasq) {
      int o1 = (row * 18 + j) * 256 + c4 * 16;
      pin(o1);
      __builtin_amdgcn_s_waitcnt(0x0F70 | (PRE_WAIT + 3));
      v4u sa, ca;
      for (unsigned spin = 0;; ++spin) {
        asm volatile("" ::: "memory");
        sa = ldsc(ws, o1, WS_S1); ca = ldsc(ws, o1, WS_S1 + 9 * 256);
        __builtin_amdgcn_sched_barrier(0);
        sa ^= T; ca ^= T;
        if (allok(or4(sa) | or4(ca)) || spin > SPIN_MAX) break;
        __builtin_amdgcn_s_sleep(2);
      }
      const float h = rb(lnv * rb(1.f + rb(tr4(asf4(ca))))) + rb(tr4(asf4(sa)));
      STAMP(2);
      v4u h0, h1;
      shuf(ln.shw, ln.shr, h, h0, h1);
      put_halves(ln, L_REDA + j * 128, gemv8(wt, h0, h1, ln.k));
      bar();
      if (j == 0) stsc1(ws, tg(rb(sumN<18>(Lf + L_REDA + lane)), T), lane * 4, WS_SQ + n * 256);
      rawbar();
    }
    asm volatile("" ::"v"(tg0), "v"(tg1), "v"(tg2));
    if (qonly) { FINISH(); return; }
    STAMP(3);
    loads8(wg, rg, FF * 2, lane, 64 * j * FF * 2 + f * 128);
    loads8(wu, ru, FF * 2, lane, 64 * j * FF * 2 + f * 128);
    loads8(wd, rd, 1152, lane, 64 * f * 1152 + j * 128);
    __builtin_amdgcn_sched_barrier(0);
    int o4 = (j * 16 + row) * 256 + c4 * 16, ob = j * 256 + col * 4, o5 = (8 * c4 + 4 * row) * 4;
    LAS float* redg_w = Lf + L_REDG + j * 128 + (row >> 1) * 64 + 8 * c4 + 4 * (row & 1);
    LAS float* redg_r = Lf + L_REDG + lane;
    LAS unsigned* shw2 = L + L_Q + (lane & 7) * 8 + (lane >> 3);
    LAS unsigned* shr2 = L + L_Q + (lane >> 3) * 8;
    pin(o4); pin(ob); pin(o5); pin(redg_w); pin(redg_r); pin(shw2); pin(shr2);
    asm volatile("s_waitcnt vmcnt(0)" ::: "memory");
    STAMP(8);
    rawbar();
    STAMP(4);
    float x1, sb, cb;
    {
      unsigned a, s2, c2;
      v4u q0, q1, q2, q3;
      q0 = ldsc(ws, o4, WS_S4); q1 = ldsc(ws, o4, WS_S4 + 1024); q2 = ldsc(ws, o4, WS_S4 + 2048); q3 = ldsc(ws, o4, WS_S4 + 3072);
      a = ldsc1(ws, ob, WS_SB); s2 = ldsc1(ws, ob, WS_SB + 9 * 256); c2 = ldsc1(ws, ob, WS_SB + 18 * 256);
      for (unsigned spin = 0; spin <= SPIN_MAX; ++spin) {
        asm volatile("" ::: "memory");
        bool again = false;
        if (!allok(or4(q0 ^ T))) { q0 = ldsc(ws, o4, WS_S4); again = true; }
        if (!allok(or4(q1 ^ T))) { q1 = ldsc(ws, o4, WS_S4 + 1024); again = true; }
        if (!allok(or4(q2 ^ T))) { q2 = ldsc(ws, o4, WS_S4 + 2048); again = true; }
        if (!allok(or4(q3 ^ T))) { q3 = ldsc(ws, o4, WS_S4 + 3072); again = true; }
        if (!allok((a ^ T) | (s2 ^ T) | (c2 ^ T))) {
          a = ldsc1(ws, ob, WS_SB); s2 = ldsc1(ws, ob, WS_SB + 9 * 256); c2 = ldsc1(ws, ob, WS_SB + 18 * 256);
          again = true;
        }
        if (!again) break;
      }
      q0 ^= T; q1 ^= T; q2 ^= T; q3 ^= T; a ^= T; s2 ^= T; c2 ^= T;
      x1 = rb(xv + rb(asf(a) * rb(tr4((asf4(q0) + asf4(q1)) + (asf4(q2) + asf4(q3))))));
      sb = asf(s2); cb = rb(1.f + asf(c2));
    }
    STAMP(5);
    {
      const float d = x1 - mean1;
      const float s = wave_sum2(d, d * d);
      if ((lane & 47) == 0) Lf[L_ST + 32 + 2 * j + (lane >> 4)] = s;
    }
    bar();
    {
      const f32x2 s = ln_sums(Lf + L_ST + 32);
      const float dm = s.x * (1.f / D);
      const float var = fmaxf(s.y * (1.f / D) - dm * dm, 0.f);
      const float den = rb(__builtin_amdgcn_sqrtf(rb(rb(var) + 1e-5f)));
      const float h2 = rb(rb(rb(x1 - rb(mean1 + dm)) * __builtin_amdgcn_rcpf(den)) * cb) + sb;
      v4u h0, h1;
      shuf(ln.shw, ln.shr, h2, h0, h1);
      const f32x4 g = gemv8(wg, h0, h1, ln.k), u = gemv8(wu, h0, h1, ln.k);
      const f32x4 t = {fold2_1(g.x, u.x), fold2_1(g.y, u.y), fold2_1(g.z, u.z), fold2_1(g.w, u.w)};
      if (c4 < 8) *(LAS f32x4*)redg_w = t;
    }
    bar();
    STAMP(6);
    if (j == 1) {
      float g = 0.f, u = 0.f, g2 = 0.f, u2 = 0.f;
#pragma unroll
      for (int i = 0; i < 8; i += 2) {
        g += redg_r[i * 128]; u += redg_r[i * 128 + 64];
        g2 += redg_r[i * 128 + 128]; u2 += redg_r[i * 128 + 192];
      }
      g = rb((g + redg_r[1024]) + g2); u = rb((u + redg_r[1088]) + u2);
      const float z = 1.5957691216057308f * (g + 0.044715f * g * g * g);
      *shw2 = rep(rb(g * __builtin_amdgcn_rcpf(1.f + __expf(-z))) * u);
    }
    bar();
    {
      const v4u h0 = *(LAS v4u*)shr2, h1 = *(LAS v4u*)(shr2 + 4);
      const f32x4 y = fold(gemv8(wd, h0, h1, ln.k));
      if (c4 < 8 && row < 2) stsc4(ws, tg4(y, T), o5, WS_S5 + (j * 72 + f) * 256);
    }
    STAMP(7);
    FINISH();
    return;
  }

  {
    int tile = 16 + (w >> 2) * 8 + xcd;
    if (w >= 8 && xcd >= 4) {
      const int r = xcd - 4 - ((xstar >= 4 && xcd > xstar) ? 1 : 0);
      if (r >= (xstar < 4 ? 3 : 2)) return;
      tile = 16 + r * 8 + xstar;
    }
    if (threadIdx.x < 3) L[L_CNT + threadIdx.x] = 0u;
    const rsrc_t ws = mk(p.ws, WS_END), rwm = mk(p.wm, D * 6 * D * 2);
    Tile18 t;
    tile18_load(t, p.c, rwm, w & 3, 2304 + tile * 128, lane);
    LOAD_EPOCH();
    __builtin_amdgcn_sched_barrier(0);
    bar();
    make_lane(ln, lane, sh);
    GET_TAG();
    modb_finish(t, sh, ln, Lf, L_REDA + (w >> 2) * 512, L + L_CNT + (w >> 2), w & 3, lane, ws, tile, T);
    STAMP(7);
    FINISH();
    return;
  }

  }
  const int g1 = (slot + 11) >> 5, g = 2 * xcd + g1, k = slot - 10 - 11 * g1;
  const bool light = k == 10;
  const int s3 = WS_S3 + (g * 11 + k) * S3_STRIDE;
  __builtin_amdgcn_sched_barrier(0);
  const rsrc_t ws = mk(p.ws, WS_END), rwm = mk(p.wm, D * 6 * D * 2);
  unsigned E = 0, T = 0;
  v4u wt[8];
  const rsrc_t rwo = mk(p.wo, (k < 9 && w < 4) ? 4096 * D * 2 : 0);
  if (light && w >= 8) {
    Tile18 t;
    tile18_load(t, p.c, rwm, w - 8, 2304 + g * 128, lane);
    LOAD_EPOCH();
    if (w == 8 && lane < 32) Lf[L_MW + lane] = -INFINITY;
    if (w == 9 && lane < 32) Lf[L_LW + lane] = 0.f;
    if (w == 10 && lane < 3) L[L_CNT + lane] = 0u;
    bar();
    make_lane(ln, lane, sh);
    E = (unsigned)__builtin_amdgcn_readfirstlane((int)Ev); T = (E + 1u) & 0xFFu;
    modb_finish(t, sh, ln, Lf, L_REDA, L + L_CNT + 1, w - 8, lane, ws, g, T);
    STAMP(1);
    rawbar();
    rawbar();
  } else {
    const size_t kvoff = ((size_t)g * SK + (SK - WIN) + k * 384 + w * 32) * 128;
    const rsrc_t rk = mk(p.kc + kvoff, 8192), rv = mk(p.vc + kvoff, 8192);
    v4u kk[8], vv[8];
    if (WO_POS == 0) loads8(wt, rwo, 1152, lane, (256 * g + 64 * (w & 3)) * 1152 + k * 128);
    const int kvo = (lane & 7) * 256 + ((lane >> 3) & 1) * 64 + row * 16, vvo = row * 2048 + c4 * 16;
#pragma unroll
    for (int t = 0; t < 4; ++t) {
      kk[2 * t] = ldnt(rk, kvo, t * 2048);
      kk[2 * t + 1] = ldnt(rk, kvo + 128, t * 2048);
    }
    const unsigned tv0 = __builtin_amdgcn_raw_buffer_load_b32(rv, 0, 0, 2);
    const unsigned tv1 = __builtin_amdgcn_raw_buffer_load_b32(mk(p.out, (light && w == 0) ? D * 2 : 0), 0, 0, 0);
    const unsigned tv2 = __builtin_amdgcn_raw_buffer_load_b32(rwo, 0, (256 * g + 64 * (w & 3)) * 1152 + k * 128, 2);
    LOAD_EPOCH();
    if (!light) {
      if (w == 8 && lane < 32) Lf[L_MW + lane] = -INFINITY;
      if (w == 9 && lane < 32) Lf[L_LW + lane] = 0.f;
      if (w == 10 && lane < 3) L[L_CNT + lane] = 0u;
    }
    __builtin_amdgcn_sched_barrier(0);
    bar();
    if (WO_POS == 1) loads8(wt, rwo, 1152, lane, (256 * g + 64 * (w & 3)) * 1152 + k * 128);
#pragma unroll
    for (int s = 0; s < 8; ++s) vv[s] = ldnt(rv, vvo + s * 256, 0);
    __builtin_amdgcn_sched_barrier(0);
    make_lane(ln, lane, sh);
    ln.put += lbase / 4;
    E = (unsigned)__builtin_amdgcn_readfirstlane((int)Ev); T = (E + 1u) & 0xFFu;
    LAS unsigned* qrd = L + L_Q + (lane & 1) * 64 + ((lane >> 1) & 1) * 16 + row * 4;
    LAS float* mwr = Lf + L_MW + ((lane >> 3) & 1) * 16;
    LAS float* owr = Lf + L_O + w * 256 + (row >> 1) * 128 + 8 * c4 + 4 * (row & 1);
    pin(qrd); pin(mwr); pin(owr);
    asm volatile("" ::"v"(tv0), "v"(tv1), "v"(tv2));
    STAMP(1);
    if (w == 0) {
      const int oq = lane * 16, sq = WS_SQ + g * 1024;
      v4u q;
      for (unsigned spin = 0;; ++spin) {
        asm volatile("" ::: "memory");
        q = ldsc(ws, oq, sq) ^ T;
        if (allok(or4(q)) || spin > SPIN_MAX) break;
        __builtin_amdgcn_s_sleep(2);
      }
      *(LAS v2u*)(L + L_Q + lane * 2) = v2u{(q.x >> 16) | q.y, (q.z >> 16) | q.w};
    }
    bar();
    STAMP(2);
    float sc = 0.f;
    {
      const v4u q0 = *(LAS v4u*)qrd, q1 = *(LAS v4u*)(qrd + 32);
#pragma unroll
      for (int t = 0; t < 4; ++t) {
        f32x4 d = {0.f, 0.f, 0.f, 0.f};
        d = mfma(q0, kk[2 * t], d);
        d = mfma(q1, kk[2 * t + 1], d);
        const float h0 = d.x + dpp<0x128>(d.z), h1 = d.w + dpp<0x128>(d.y);
        const float v = c4 < 8 ? h0 : h1;
        sc = row == t ? v : sc;
      }
      sc = rb(rb(sc) * 0.08838834764831845f);
      float m = sc;
      m = vmax(m, dpp<0xB1>(m)); m = vmax(m, dpp<0x4E>(m)); m = vmax(m, dpp<0x141>(m));
      m = xrow_max(m);
      if ((lane & 55) == 0) mwr[w] = m;
    }
    bar();
    STAMP(3);
    {
      const float M = max12(mwr);
      const float pe = __expf(sc - M);
      v4u h0, h1;
      shuf(sh + lane, ln.shr, pe, h0, h1);
      float l = pe;
      l += dpp<0xB1>(l); l += dpp<0x4E>(l); l += dpp<0x141>(l);
      l = xrow_sum(l);
      if ((lane & 55) == 0) mwr[32 + w] = l;
      lds_arrive(L + L_CNT + 2);
      v4u A[8];
      A[0] = diag(h0.x, ln.k); A[1] = diag(h0.y, ln.k); A[2] = diag(h0.z, ln.k); A[3] = diag(h0.w, ln.k);
      A[4] = diag(h1.x, ln.k); A[5] = diag(h1.y, ln.k); A[6] = diag(h1.z, ln.k); A[7] = diag(h1.w, ln.k);
      __builtin_amdgcn_sched_barrier(0);
      f32x4 o0 = {0.f, 0.f, 0.f, 0.f}, o1 = {0.f, 0.f, 0.f, 0.f};
      o0 = mfma(A[0], vv[0], o0); o1 = mfma(A[1], vv[1], o1); o0 = mfma(A[2], vv[2], o0); o1 = mfma(A[3], vv[3], o1);
      o0 = mfma(A[4], vv[4], o0); o1 = mfma(A[5], vv[5], o1); o0 = mfma(A[6], vv[6], o0); o1 = mfma(A[7], vv[7], o1);
      *(LAS f32x4*)owr = o0 + o1;
      lds_arrive(L + L_CNT);
    }
    STAMP(4);
    if (w >= 7) __builtin_amdgcn_s_setprio(3);
    if (w == 7) {
      lds_wait(L + L_CNT + 2, light ? 8u : 12u);
      LAS float* lw = Lf + L_LW + (lane & 1) * 16;
      LAS float* mw = Lf + L_MW + (lane & 1) * 16;
      const f32x4 a0 = *(LAS f32x4*)lw, a1 = *(LAS f32x4*)(lw + 4), a2 = *(LAS f32x4*)(lw + 8);
      const float l = ((a0.x + a0.y) + (a0.z + a0.w)) + ((a1.x + a1.y) + (a1.z + a1.w)) + ((a2.x + a2.y) + (a2.z + a2.w));
      const float M = max12(mw);
      if (lane < 2) __builtin_amdgcn_raw_buffer_store_b64(v2u{tg(M, T), tg(l, T)}, ws, lane * 8, s3 + 1024, 16);
    }
  }
  if (w >= 8) {
    __builtin_amdgcn_s_setprio(3);
    const int wp = w - 8;
    LAS float* o = Lf + L_O + row * 256 + wp * 64 + c4 * 4;
    int so = wp * 256 + col * 4;
    pin(o); pin(so);
    lds_wait(L + L_CNT, light ? 8u : 12u);
    f32x4 v = *(LAS f32x4*)o + *(LAS f32x4*)(o + 1024);
    if (!light) v += *(LAS f32x4*)(o + 2048);
    stsc1(ws, tg(tr4(v), T), so, s3);
    STAMP(5);
    if (!(light && g < 9 && w == 8)) { FINISH(); return; }
  }
  if (k < 9) {
    if (w >= 4) { FINISH(); return; }
    if (WO_POS == 2) loads8(wt, rwo, 1152, lane, (256 * g + 64 * w) * 1152 + k * 128);
    __builtin_amdgcn_sched_barrier(0);
    const int k2 = row + 8 > 10 ? 10 : row + 8;
    const int gb = WS_S3 + g * 11 * S3_STRIDE, hd = (w >> 1) * 8;
    int oa = row * S3_STRIDE + w * 256 + c4 * 16, oc = k2 * S3_STRIDE + w * 256 + c4 * 16;
    int ma_ = row * S3_STRIDE + 1024 + hd, mc_ = k2 * S3_STRIDE + 1024 + hd;
    pin(oa); pin(oc); pin(ma_); pin(mc_);
    v2u m0, m1, m2;
    v4u o0, o1, o2;
    for (unsigned spin = 0;; ++spin) {
      asm volatile("" ::: "memory");
      m0 = ldsc2(ws, ma_, gb); m1 = ldsc2(ws, ma_, gb + 4 * S3_STRIDE); m2 = ldsc2(ws, mc_, gb);
      o0 = ldsc(ws, oa, gb); o1 = ldsc(ws, oa, gb + 4 * S3_STRIDE); o2 = ldsc(ws, oc, gb);
      __builtin_amdgcn_sched_barrier(0);
      m0 ^= T; m1 ^= T; m2 ^= T; o0 ^= T; o1 ^= T; o2 ^= T;
      if (allok((m0.x | m0.y) | (m1.x | m1.y) | (m2.x | m2.y) | or4(o0) | or4(o1) | or4(o2)) || spin > SPIN_MAX) break;
      __builtin_amdgcn_s_sleep(POLL_SLEEP);
    }
    const float ma = asf(m0.x), mb = asf(m1.x), mc = row == 3 ? -INFINITY : asf(m2.x);
    const float MM = xrow_max(vmax(vmax(ma, mb), mc));
    float fa = __expf(ma - MM), fb = __expf(mb - MM), fc = __expf(mc - MM);
    const float inv = __builtin_amdgcn_rcpf(xrow_sum(fa * asf(m0.y) + fb * asf(m1.y) + fc * asf(m2.y)));
    STAMP(6);
    v4u h0, h1;
    shuf(ln.shw, ln.shr, tr4(asf4(o0) * fa + asf4(o1) * fb + asf4(o2) * fc) * inv, h0, h1);
    put_halves(ln, L_REDA + w * 128, gemv8(wt, h0, h1, ln.k));
    bar();
    if (w == 0) stsc1(ws, tg(sumN<8>(Lf + L_REDA + lane), T), lane * 4, WS_S4 + (k * 16 + g) * 256);
    STAMP(7);
    FINISH();
    return;
  }
  if (!(light && g < 9) || w > 8) { FINISH(); return; }
  {
    const int j = g;
    float x1 = 0.f, gbv = 0.f;
    if (w == 0) {
      const float xv = bf(p.x[64 * j + col]);
      unsigned a2, b2;
      v4u q0, q1, q2, q3;
      for (unsigned spin = 0;; ++spin) {
        asm volatile("" ::: "memory");
        const int vo = (j * 16 + row) * 256 + c4 * 16;
        a2 = ldsc1(ws, col * 4, WS_SB + j * 256); b2 = ldsc1(ws, col * 4, WS_SB + (27 + j) * 256);
        q0 = ldsc(ws, vo, WS_S4); q1 = ldsc(ws, vo, WS_S4 + 1024); q2 = ldsc(ws, vo, WS_S4 + 2048); q3 = ldsc(ws, vo, WS_S4 + 3072);
        __builtin_amdgcn_sched_barrier(0);
        q0 ^= T; q1 ^= T; q2 ^= T; q3 ^= T; a2 ^= T; b2 ^= T;
        if (allok(a2 | b2 | or4(q0) | or4(q1) | or4(q2) | or4(q3)) || spin > SPIN_MAX) break;
        __builtin_amdgcn_s_sleep(8);
      }
      x1 = rb(xv + rb(asf(a2) * rb(tr4((asf4(q0) + asf4(q1)) + (asf4(q2) + asf4(q3))))));
      gbv = asf(b2);
      pin(x1); pin(gbv);
    }
    STAMP(6);
    {
      v4u y0, y1;
      int vo = (8 * w + row) * 256 + c4 * 16;
      LAS float* rw = Lf + L_REDG + w * 64 + col;
      pin(vo); pin(rw);
      for (unsigned spin = 0;; ++spin) {
        asm volatile("" ::: "memory");
        y0 = ldsc(ws, vo, WS_S5 + j * 72 * 256); y1 = ldsc(ws, vo, WS_S5 + j * 72 * 256 + 1024);
        __builtin_amdgcn_sched_barrier(0);
        y0 ^= T; y1 ^= T;
        if (allok(or4(y0) | or4(y1)) || spin > SPIN_MAX) break;
        __builtin_amdgcn_s_sleep(1);
      }
      *rw = tr4(asf4(y0) + asf4(y1));
    }
    bar();
    if (w == 0) {
      LAS float* r = Lf + L_REDG + col;
      const float y = ((r[0] + r[64]) + (r[128] + r[192])) + ((r[256] + r[320]) + (r[384] + r[448])) + r[512];
      f32x2 o = {x1 + rb(gbv * rb(y)), 0.f};
      p.out[64 * j + col] = (u16)(__builtin_bit_cast(unsigned, __builtin_convertvector(o, bf16x2)) & 0xFFFFu);
      if (j == 0 && lane == 0) __builtin_amdgcn_raw_buffer_store_b32(E + 1u, ws, 0, WS_EPOCH, 16);
    }
    STAMP(7);
    FINISH();
  }
}

}

extern "C" void kernel_launch(void* const* d_in, const int* in_sizes, int n_in, void* d_out, int out_size, void* d_ws, size_t ws_size,
                              hipStream_t stream) {
  Params p;
  p.x = (const u16*)d_in[0]; p.kc = (const u16*)d_in[1]; p.vc = (const u16*)d_in[2]; p.wq = (const u16*)d_in[3]; p.wo = (const u16*)d_in[4];
  p.wg = (const u16*)d_in[6]; p.wu = (const u16*)d_in[7]; p.wd = (const u16*)d_in[8]; p.c = (const u16*)d_in[9]; p.wm = (const u16*)d_in[10];
  p.out = (u16*)d_out; p.ws = (unsigned char*)d_ws;
  fused_decode<<<256, NTHR, 0, stream>>>(p);
}
```

```cpp
#include <hip/hip_runtime.h>
#include <hip/hip_bf16.h>
#include <stdint.h>

namespace {

typedef unsigned short u16;
typedef unsigned v4u __attribute__((ext_vector_type(4)));
typedef unsigned v2u __attribute__((ext_vector_type(2)));
typedef float f32x4 __attribute__((ext_vector_type(4)));
typedef float f32x2 __attribute__((ext_vector_type(2)));
typedef __bf16 bf16x8 __attribute__((ext_vector_type(8)));
typedef __bf16 bf16x2 __attribute__((ext_vector_type(2)));
typedef __amdgpu_buffer_rsrc_t rsrc_t;
#define LAS __attribute__((address_space(3)))

constexpr int D = 576, SK = 65536, WIN = 4096, FF = 4608;
constexpr int NTHR = 768;
constexpr int WS_EPOCH = 0;
constexpr int WS_S1 = 4096;
constexpr int WS_SQ = WS_S1 + 72 * 256;
constexpr int WS_SB = WS_SQ + 64 * 256;
constexpr int WS_S4 = WS_SB + 36 * 256;
constexpr int WS_S5 = WS_S4 + 144 * 256;
constexpr int WS_S3 = WS_S5 + 648 * 256;
constexpr int S3_STRIDE = 1280;
constexpr int WS_END = WS_S3 + 176 * S3_STRIDE;
constexpr int WS_STAMP = 1 << 20;
constexpr unsigned SPIN_MAX = 1u << 14;
#define POLL_SLEEP 1
#define PRE_WAIT 2
#define WO_POS 1
#define DELAY_A 0
#define DELAY_F 0

struct Params {
  const u16 *x, *kc, *vc, *wq, *wo, *wg, *wu, *wd, *c, *wm;
  u16* out;
  unsigned char* ws;
};

constexpr int SH_STRIDE = 160;
constexpr int L_SH = 0;
constexpr int L_REDA = L_SH + 12 * SH_STRIDE;
constexpr int L_REDG = L_REDA + 1536;
constexpr int L_ST = L_REDG + 1152;
constexpr int L_Q = L_ST + 64;
constexpr int L_MW = L_Q + 128;
constexpr int L_LW = L_MW + 32;
constexpr int L_CNT = L_LW + 32;
constexpr int L_O = L_CNT + 16;
constexpr int L_END = L_O + 12 * 256;

__device__ __forceinline__ rsrc_t mk(const void* p, unsigned bytes) {
  return __builtin_amdgcn_make_buffer_rsrc((void*)p, 0, (int)bytes, 0x00020000);
}
__device__ __forceinline__ v4u ldnt(rsrc_t r, int v, int s) { return __builtin_amdgcn_raw_buffer_load_b128(r, v, s, 2); }
__device__ __forceinline__ v4u ldsc(rsrc_t r, int v, int s) { return __builtin_amdgcn_raw_buffer_load_b128(r, v, s, 16); }
__device__ __forceinline__ v2u ldsc2(rsrc_t r, int v, int s) { return __builtin_amdgcn_raw_buffer_load_b64(r, v, s, 16); }
__device__ __forceinline__ unsigned ldsc1(rsrc_t r, int v, int s) { return __builtin_amdgcn_raw_buffer_load_b32(r, v, s, 16); }
__device__ __forceinline__ void stsc1(rsrc_t r, unsigned x, int v, int s) { __builtin_amdgcn_raw_buffer_store_b32(x, r, v, s, 16); }
__device__ __forceinline__ void stsc4(rsrc_t r, v4u x, int v, int s) { __builtin_amdgcn_raw_buffer_store_b128(x, r, v, s, 16); }
__device__ __forceinline__ float asf(unsigned x) { return __uint_as_float(x); }
__device__ __forceinline__ unsigned asu(float x) { return __float_as_uint(x); }
template <class V> __device__ __forceinline__ void pin(V& x) { asm volatile("" : "+v"(x)); }
__device__ __forceinline__ unsigned rep(float x) {
  f32x2 v = {x, x};
  return __builtin_bit_cast(unsigned, __builtin_convertvector(v, bf16x2));
}
__device__ __forceinline__ float rb(float x) { return asf(rep(x) & 0xFFFF0000u); }
__device__ __forceinline__ float bf(u16 x) { return asf((unsigned)x << 16); }
__device__ __forceinline__ unsigned tg(float x, unsigned T) { return (asu(x) & 0xFFFFFF00u) | T; }
__device__ __forceinline__ unsigned tgr(float x, unsigned T) { return ((asu(x) + 0x80u) & 0xFFFFFF00u) | T; }
__device__ __forceinline__ v4u tg4(f32x4 y, unsigned T) { return v4u{tg(y.x, T), tg(y.y, T), tg(y.z, T), tg(y.w, T)}; }
__device__ __forceinline__ unsigned or4(v4u d) { return (d.x | d.y) | (d.z | d.w); }
__device__ __forceinline__ bool allok(unsigned bad) { return __all((bad & 0xFFu) == 0u); }
template <int C> __device__ __forceinline__ float dpp(float x) {
  return asf((unsigned)__builtin_amdgcn_update_dpp(0, (int)asu(x), C, 0xF, 0xF, true));
}
__device__ __forceinline__ float xrow_sum(float x) {
  auto s = __builtin_amdgcn_permlane16_swap(asu(x), asu(x), false, false);
  x = asf(s[0]) + asf(s[1]);
  auto t = __builtin_amdgcn_permlane32_swap(asu(x), asu(x), false, false);
  return asf(t[0]) + asf(t[1]);
}
__device__ __forceinline__ float xrow_max(float x) {
  auto s = __builtin_amdgcn_permlane16_swap(asu(x), asu(x), false, false);
  x = __builtin_amdgcn_fmed3f(asf(s[0]), asf(s[1]), INFINITY);
  auto t = __builtin_amdgcn_permlane32_swap(asu(x), asu(x), false, false);
  return __builtin_amdgcn_fmed3f(asf(t[0]), asf(t[1]), INFINITY);
}
__device__ __forceinline__ float tr4(f32x4 v) {
  auto a = __builtin_amdgcn_permlane16_swap(asu(v.x), asu(v.y), false, false);
  auto b = __builtin_amdgcn_permlane16_swap(asu(v.z), asu(v.w), false, false);
  const float s01 = asf(a[0]) + asf(a[1]), s23 = asf(b[0]) + asf(b[1]);
  auto c = __builtin_amdgcn_permlane32_swap(asu(s01), asu(s23), false, false);
  return asf(c[0]) + asf(c[1]);
}
__device__ __forceinline__ float wave_sum2(float x, float y) {
  x += dpp<0xB1>(x); y += dpp<0xB1>(y);
  x += dpp<0x4E>(x); y += dpp<0x4E>(y);
  x += dpp<0x141>(x); y += dpp<0x141>(y);
  x += dpp<0x140>(x); y += dpp<0x140>(y);
  auto a = __builtin_amdgcn_permlane16_swap(asu(x), asu(y), false, false);
  const float s = asf(a[0]) + asf(a[1]);
  auto c = __builtin_amdgcn_permlane32_swap(asu(s), asu(s), false, false);
  return asf(c[0]) + asf(c[1]);
}
__device__ __forceinline__ f32x4 asf4(v4u d) { return __builtin_bit_cast(f32x4, d); }
__device__ __forceinline__ f32x4 mfma(v4u a, v4u b, f32x4 c) {
  return __builtin_amdgcn_mfma_f32_16x16x32_bf16(__builtin_bit_cast(bf16x8, a), __builtin_bit_cast(bf16x8, b), c, 0, 0, 0);
}
struct Mask { unsigned m0, m1, m2, m3; };
__device__ __forceinline__ v4u diag(unsigned hh, const Mask& k) { return v4u{hh & k.m0, hh & k.m1, hh & k.m2, hh & k.m3}; }
__device__ __forceinline__ void bar() {
  asm volatile("s_waitcnt lgkmcnt(0)" ::: "memory");
  __builtin_amdgcn_s_barrier();
  asm volatile("" ::: "memory");
}
__device__ __forceinline__ void rawbar() {
  __builtin_amdgcn_sched_barrier(0);
  __builtin_amdgcn_s_barrier();
  __builtin_amdgcn_sched_barrier(0);
}
__device__ __forceinline__ float silu(float v) { return v * __builtin_amdgcn_rcpf(1.f + __expf(-v)); }
__device__ __forceinline__ float fold1(float x) {
  auto t = __builtin_amdgcn_permlane32_swap(asu(x), asu(x), false, false);
  return asf(t[0]) + dpp<0x128>(asf(t[1]));
}
__device__ __forceinline__ f32x4 fold(f32x4 a) { return f32x4{fold1(a.x), fold1(a.y), fold1(a.z), fold1(a.w)}; }
__device__ __forceinline__ float fold2_1(float a, float b) {
  auto t = __builtin_amdgcn_permlane32_swap(asu(a), asu(b), false, false);
  return asf(t[0]) + dpp<0x128>(asf(t[1]));
}

#define STAMP(i) do {} while (0)
#define FINISH() do {} while (0)

struct Lane {
  Mask k;
  LAS unsigned* shw;
  LAS unsigned* shr;
  LAS float* put;
  bool putv;
};
__device__ __forceinline__ void shuf(LAS unsigned* wr, LAS unsigned* rd, float v, v4u& h0, v4u& h1) {
  *wr = rep(v);
  asm volatile("" ::: "memory");
  h0 = *(LAS v4u*)rd;
  h1 = *(LAS v4u*)(rd + 4);
}
__device__ __forceinline__ f32x4 gemv8(const v4u (&w)[8], v4u h0, v4u h1, const Mask& k) {
  f32x4 a = {0.f, 0.f, 0.f, 0.f}, b = {0.f, 0.f, 0.f, 0.f};
  const v4u a0 = diag(h0.x, k), a1 = diag(h0.y, k), a2 = diag(h0.z, k), a3 = diag(h0.w, k);
  a = mfma(a0, w[0], a); b = mfma(a1, w[1], b);
  const v4u a4 = diag(h1.x, k), a5 = diag(h1.y, k);
  a = mfma(a2, w[2], a); b = mfma(a3, w[3], b);
  const v4u a6 = diag(h1.z, k), a7 = diag(h1.w, k);
  a = mfma(a4, w[4], a); b = mfma(a5, w[5], b);
  a = mfma(a6, w[6], a); b = mfma(a7, w[7], b);
  return a + b;
}
__device__ __forceinline__ f32x2 ln_sums(LAS float* st) {
  const f32x4 a = *(LAS f32x4*)(st), b = *(LAS f32x4*)(st + 4), c = *(LAS f32x4*)(st + 8), d = *(LAS f32x4*)(st + 12);
  const f32x2 e = *(LAS f32x2*)(st + 16);
  const f32x4 t = (a + b) + (c + d);
  return f32x2{t.x + t.z + e.x, t.y + t.w + e.y};
}
template <int N> __device__ __forceinline__ float sumN(LAS float* red) {
  float a = 0.f, b = 0.f;
#pragma unroll
  for (int i = 0; i < N; i += 2) { a += red[i * 64]; b += red[i * 64 + 64]; }
  return a + b;
}
__device__ __forceinline__ void loads8(v4u (&w)[8], rsrc_t r, int stride, int lane, int base) {
  const int vo = (lane >> 3) * stride + (lane & 7) * 16;
#pragma unroll
  for (int s = 0; s < 8; ++s) w[s] = ldnt(r, vo, base + 8 * s * stride);
}
struct Tile18 { v4u w[18]; v2u c; };
__device__ __forceinline__ void tile18_load(Tile18& t, const u16* c, rsrc_t rwm, int ks, int cb, int lane) {
  t.c = *(const v2u*)(c + 144 * ks + 4 * (lane < 36 ? lane : 35));
  const int vo = (lane >> 3) * 6912 + (lane & 7) * 16;
#pragma unroll
  for (int s = 0; s < 18; ++s) t.w[s] = ldnt(rwm, vo, (144 * ks + 8 * s) * 6912 + cb);
}
__device__ __forceinline__ f32x4 tile18_mac(const Tile18& t, LAS unsigned* sh, int lane, const Mask& k) {
  if (lane < 36) {
    LAS unsigned* d = sh + (4 * (lane & 1)) * 20 + (lane >> 1);
    d[0] = rep(silu(asf(t.c.x << 16))); d[20] = rep(silu(asf(t.c.x & 0xFFFF0000u)));
    d[40] = rep(silu(asf(t.c.y << 16))); d[60] = rep(silu(asf(t.c.y & 0xFFFF0000u)));
  }
  asm volatile("" ::: "memory");
  LAS unsigned* s = sh + (lane >> 3) * 20;
  const v4u h0 = *(LAS v4u*)s, h1 = *(LAS v4u*)(s + 4), h2 = *(LAS v4u*)(s + 8), h3 = *(LAS v4u*)(s + 12);
  const v2u h4 = *(LAS v2u*)(s + 16);
  f32x4 a = {0.f, 0.f, 0.f, 0.f}, b = {0.f, 0.f, 0.f, 0.f};
  a = mfma(diag(h0.x, k), t.w[0], a); b = mfma(diag(h0.y, k), t.w[1], b); a = mfma(diag(h0.z, k), t.w[2], a); b = mfma(diag(h0.w, k), t.w[3], b);
  a = mfma(diag(h1.x, k), t.w[4], a); b = mfma(diag(h1.y, k), t.w[5], b); a = mfma(diag(h1.z, k), t.w[6], a); b = mfma(diag(h1.w, k), t.w[7], b);
  a = mfma(diag(h2.x, k), t.w[8], a); b = mfma(diag(h2.y, k), t.w[9], b); a = mfma(diag(h2.z, k), t.w[10], a); b = mfma(diag(h2.w, k), t.w[11], b);
  a = mfma(diag(h3.x, k), t.w[12], a); b = mfma(diag(h3.y, k), t.w[13], b); a = mfma(diag(h3.z, k), t.w[14], a); b = mfma(diag(h3.w, k), t.w[15], b);
  a = mfma(diag(h4.x, k), t.w[16], a); b = mfma(diag(h4.y, k), t.w[17], b);
  return a + b;
}
__device__ __forceinline__ void lds_arrive(LAS unsigned* c) {
  if ((threadIdx.x & 63) == 0) asm volatile("ds_add_u32 %0, %1" ::"v"(c), "v"(1u) : "memory");
  asm volatile("" ::: "memory");
}
__device__ __forceinline__ float vmax(float a, float b) { return __builtin_amdgcn_fmed3f(a, b, INFINITY); }
__device__ __forceinline__ float max12(LAS float* m) {
  const f32x4 b0 = *(LAS f32x4*)m, b1 = *(LAS f32x4*)(m + 4), b2 = *(LAS f32x4*)(m + 8);
  return vmax(vmax(vmax(vmax(b0.x, b0.y), vmax(b0.z, b0.w)), vmax(vmax(b1.x, b1.y), vmax(b1.z, b1.w))), vmax(vmax(b2.x, b2.y), vmax(b2.z, b2.w)));
}
__device__ __forceinline__ unsigned lds_inc(LAS unsigned* c) {
  asm volatile("" ::: "memory");
  unsigned old = 0;
  if ((threadIdx.x & 63) == 0) old = __hip_atomic_fetch_add(c, 1u, __ATOMIC_RELAXED, __HIP_MEMORY_SCOPE_WORKGROUP);
  old = (unsigned)__builtin_amdgcn_readfirstlane((int)old);
  asm volatile("" ::: "memory");
  return old;
}
__device__ __forceinline__ void lds_wait(LAS unsigned* c, unsigned need) {
  for (unsigned spin = 0; *(volatile LAS unsigned*)c < need && spin < (1u << 20); ++spin) {}
  asm volatile("" ::: "memory");
}
__device__ __forceinline__ void make_lane(Lane& ln, int lane, LAS unsigned* sh) {
  const unsigned mm = (lane & 1) ? 0xFFFF0000u : 0x0000FFFFu;
  const int d = (lane & 7) >> 1, c = lane & 15, g = lane >> 4;
  ln.k.m0 = d == 0 ? mm : 0u; ln.k.m1 = d == 1 ? mm : 0u; ln.k.m2 = d == 2 ? mm : 0u; ln.k.m3 = d == 3 ? mm : 0u;
  ln.shw = sh + 32 * (c & 1) + 8 * g + (c >> 1);
  ln.shr = sh + (lane >> 3) * 8;
  ln.put = (LAS float*)0 + (c >> 3) * 64 + 8 * (c & 7) + 4 * (g & 1);
  ln.putv = (c >> 3) == (g >> 1);
  pin(ln.k.m0); pin(ln.k.m1); pin(ln.k.m2); pin(ln.k.m3); pin(ln.shw); pin(ln.shr); pin(ln.put);
}
__device__ __forceinline__ void put_halves(const Lane& ln, int base_dw, f32x4 a) {
  if (ln.putv) *(LAS f32x4*)(ln.put + base_dw) = a;
}
__device__ __forceinline__ void modb_finish(const Tile18& t, LAS unsigned* sh, const Lane& ln, LAS float* Lf, int red_dw, LAS unsigned* cnt, int ks, int lane,
                                            rsrc_t ws, int tile, unsigned T) {
  put_halves(ln, red_dw + ks * 128, tile18_mac(t, sh, lane, ln.k));
  if (lds_inc(cnt) == 3u) stsc1(ws, tg(rb(sumN<8>(Lf + red_dw + lane)), T), lane * 4, WS_SB + tile * 256);
}

__global__ void __launch_bounds__(NTHR, 3) fused_decode(Params p) {
  __shared__ __attribute__((aligned(16))) unsigned smem[L_END];
  LAS unsigned* L = (LAS unsigned*)smem;
  LAS float* Lf = (LAS float*)smem;
  const int b = blockIdx.x, xcd = b & 7, slot = b >> 3;
  const int lane = threadIdx.x & 63, w = __builtin_amdgcn_readfirstlane(threadIdx.x >> 6);
  const int c4 = lane & 15, row = lane >> 4;
  const int col = 4 * c4 + row;
  STAMP(0);
#define LOAD_EPOCH() __builtin_amdgcn_sched_barrier(0); const unsigned Ev = __builtin_amdgcn_raw_buffer_load_b32(ws, 0, WS_EPOCH, 16); __builtin_amdgcn_sched_barrier(0)
#define GET_TAG() const unsigned E = (unsigned)__builtin_amdgcn_readfirstlane((int)Ev); const unsigned T = (E + 1u) & 0xFFu; (void)E
  LAS unsigned* sh = L + L_SH + w * SH_STRIDE;
  const int lbase = (int)(size_t)(LAS void*)smem;
  Lane ln;

  if (slot <= 9) {
  const int rgate = (int)((size_t)p.wg >> 7), xstar = (3 - rgate) & 7;
  const bool qonly = slot == 9 && xcd == xstar;
  if (slot < 9 || qonly) {
    if (w >= 10) return;
    const int f = xcd * 9 + slot;
    __builtin_amdgcn_sched_barrier(0);
    const rsrc_t ws = mk(p.ws, WS_END), rwm = mk(p.wm, D * 6 * D * 2);
    if (w == 9) {
      __builtin_amdgcn_s_setprio(3);
      if (lane == 0) L[L_CNT] = 0u;
      if (qonly) { bar(); return; }
      Tile18 t;
      tile18_load(t, p.c, rwm, f / 18, (f % 18) * 128, lane);
      LOAD_EPOCH();
      bar();
      make_lane(ln, lane, sh);
      GET_TAG();
      const f32x4 a = fold(tile18_mac(t, sh, lane, ln.k));
      if (c4 < 8 && row < 2) stsc4(ws, v4u{tgr(a.x, T), tgr(a.y, T), tgr(a.z, T), tgr(a.w, T)}, (8 * c4 + 4 * row) * 4, WS_S1 + f * 256);
      STAMP(1);
      FINISH();
      return;
    }
    const int j = w;
    const int s0 = (3 - rgate - xcd) & 7;
    const bool hasq = qonly || !(slot == s0 || (s0 == 0 && slot == 8));
    const int n = qonly ? xcd * 8 + 7 : xcd * 8 + slot - (slot > s0 ? 1 : 0);
    const u16 xr = p.x[64 * j + col], x0r = p.x[0];
    LOAD_EPOCH();
    rawbar();
    v4u wt[8], wg[8], wu[8], wd[8];
    const rsrc_t rg = mk(p.wg, D * FF * 2), ru = mk(p.wu, D * FF * 2), rd = mk(p.wd, FF * D * 2);
    loads8(wt, mk(p.wq, hasq ? D * 4096 * 2 : 0), 8192, lane, 64 * j * 8192 + n * 128);
    unsigned tg0 = __builtin_amdgcn_raw_buffer_load_b32(rg, (lane & 1) * 63 * FF * 2, 64 * j * FF * 2 + f * 128, 2);
    unsigned tg1 = __builtin_amdgcn_raw_buffer_load_b32(ru, (lane & 1) * 63 * FF * 2, 64 * j * FF * 2 + f * 128, 2);
    unsigned tg2 = __builtin_amdgcn_raw_buffer_load_b32(rd, (lane & 1) * 63 * 1152, 64 * f * 1152 + j * 128, 2);
    __builtin_amdgcn_sched_barrier(0);
    make_lane(ln, lane, sh);
    ln.put += lbase / 4;
    GET_TAG();
    const float x0 = bf(x0r);
    float xv = bf(xr);
    {
      const float d = xv - x0;
      const float s = wave_sum2(d, d * d);
      if ((lane & 47) == 0) Lf[L_ST + 2 * j + (lane >> 4)] = s;
    }
    lds_arrive(L + L_CNT);
    lds_wait(L + L_CNT, 9u);
    float mean1, lnv;
    {
      const f32x2 s = ln_sums(Lf + L_ST);
      const float dm = s.x * (1.f / D);
      mean1 = x0 + dm;
      const float var = fmaxf(s.y * (1.f / D) - dm * dm, 0.f);
      const float den = rb(__builtin_amdgcn_sqrtf(rb(rb(var) + 1e-5f)));
      lnv = rb(rb(xv - rb(mean1)) / den);
      pin(lnv); pin(mean1); pin(xv);
    }
    STAMP(1);
    if (hasq) {
      int o1 = (row * 18 + j) * 256 + c4 * 16;
      pin(o1);
      __builtin_amdgcn_s_waitcnt(0x0F70 | (PRE_WAIT + 3));
      v4u sa, ca;
      for (unsigned spin = 0;; ++spin) {
        asm volatile("" ::: "memory");
        sa = ldsc(ws, o1, WS_S1); ca = ldsc(ws, o1, WS_S1 + 9 * 256);
        __builtin_amdgcn_sched_barrier(0);
        sa ^= T; ca ^= T;
        if (allok(or4(sa) | or4(ca)) || spin > SPIN_MAX) break;
        __builtin_amdgcn_s_sleep(2);
      }
      const float h = rb(lnv * rb(1.f + rb(tr4(asf4(ca))))) + rb(tr4(asf4(sa)));
      STAMP(2);
      v4u h0, h1;
      shuf(ln.shw, ln.shr, h, h0, h1);
      put_halves(ln, L_REDA + j * 128, gemv8(wt, h0, h1, ln.k));
      bar();
      if (j == 0) stsc1(ws, tg(rb(sumN<18>(Lf + L_REDA + lane)), T), lane * 4, WS_SQ + n * 256);
      rawbar();
    }
    asm volatile("" ::"v"(tg0), "v"(tg1), "v"(tg2));
    if (qonly) { FINISH(); return; }
    STAMP(3);
    loads8(wg, rg, FF * 2, lane, 64 * j * FF * 2 + f * 128);
    loads8(wu, ru, FF * 2, lane, 64 * j * FF * 2 + f * 128);
    loads8(wd, rd, 1152, lane, 64 * f * 1152 + j * 128);
    __builtin_amdgcn_sched_barrier(0);
    int o4 = (j * 16 + row) * 256 + c4 * 16, ob = j * 256 + col * 4, o5 = (8 * c4 + 4 * row) * 4;
    LAS float* redg_w = Lf + L_REDG + j * 128 + (row >> 1) * 64 + 8 * c4 + 4 * (row & 1);
    LAS float* redg_r = Lf + L_REDG + lane;
    LAS unsigned* shw2 = L + L_Q + (lane & 7) * 8 + (lane >> 3);
    LAS unsigned* shr2 = L + L_Q + (lane >> 3) * 8;
    pin(o4); pin(ob); pin(o5); pin(redg_w); pin(redg_r); pin(shw2); pin(shr2);
    asm volatile("s_waitcnt vmcnt(0)" ::: "memory");
    STAMP(8);
    rawbar();
    STAMP(4);
    float x1, sb, cb;
    {
      unsigned a, s2, c2;
      v4u q0, q1, q2, q3;
      q0 = ldsc(ws, o4, WS_S4); q1 = ldsc(ws, o4, WS_S4 + 1024); q2 = ldsc(ws, o4, WS_S4 + 2048); q3 = ldsc(ws, o4, WS_S4 + 3072);
      a = ldsc1(ws, ob, WS_SB); s2 = ldsc1(ws, ob, WS_SB + 9 * 256); c2 = ldsc1(ws, ob, WS_SB + 18 * 256);
      for (unsigned spin = 0; spin <= SPIN_MAX; ++spin) {
        asm volatile("" ::: "memory");
        bool again = false;
        if (!allok(or4(q0 ^ T))) { q0 = ldsc(ws, o4, WS_S4); again = true; }
        if (!allok(or4(q1 ^ T))) { q1 = ldsc(ws, o4, WS_S4 + 1024); again = true; }
        if (!allok(or4(q2 ^ T))) { q2 = ldsc(ws, o4, WS_S4 + 2048); again = true; }
        if (!allok(or4(q3 ^ T))) { q3 = ldsc(ws, o4, WS_S4 + 3072); again = true; }
        if (!allok((a ^ T) | (s2 ^ T) | (c2 ^ T))) {
          a = ldsc1(ws, ob, WS_SB); s2 = ldsc1(ws, ob, WS_SB + 9 * 256); c2 = ldsc1(ws, ob, WS_SB + 18 * 256);
          again = true;
        }
        if (!again) break;
      }
      q0 ^= T; q1 ^= T; q2 ^= T; q3 ^= T; a ^= T; s2 ^= T; c2 ^= T;
      x1 = rb(xv + rb(asf(a) * rb(tr4((asf4(q0) + asf4(q1)) + (asf4(q2) + asf4(q3))))));
      sb = asf(s2); cb = rb(1.f + asf(c2));
    }
    STAMP(5);
    {
      const float d = x1 - mean1;
      const float s = wave_sum2(d, d * d);
      if ((lane & 47) == 0) Lf[L_ST + 32 + 2 * j + (lane >> 4)] = s;
    }
    bar();
    {
      const f32x2 s = ln_sums(Lf + L_ST + 32);
      const float dm = s.x * (1.f / D);
      const float var = fmaxf(s.y * (1.f / D) - dm * dm, 0.f);
      const float den = rb(__builtin_amdgcn_sqrtf(rb(rb(var) + 1e-5f)));
      const float h2 = rb(rb(rb(x1 - rb(mean1 + dm)) * __builtin_amdgcn_rcpf(den)) * cb) + sb;
      v4u h0, h1;
      shuf(ln.shw, ln.shr, h2, h0, h1);
      const f32x4 g = gemv8(wg, h0, h1, ln.k), u = gemv8(wu, h0, h1, ln.k);
      const f32x4 t = {fold2_1(g.x, u.x), fold2_1(g.y, u.y), fold2_1(g.z, u.z), fold2_1(g.w, u.w)};
      if (c4 < 8) *(LAS f32x4*)redg_w = t;
    }
    bar();
    STAMP(6);
    if (j == 1) {
      float g = 0.f, u = 0.f, g2 = 0.f, u2 = 0.f;
#pragma unroll
      for (int i = 0; i < 8; i += 2) {
        g += redg_r[i * 128]; u += redg_r[i * 128 + 64];
        g2 += redg_r[i * 128 + 128]; u2 += redg_r[i * 128 + 192];
      }
      g = rb((g + redg_r[1024]) + g2); u = rb((u + redg_r[1088]) + u2);
      const float z = 1.5957691216057308f * (g + 0.044715f * g * g * g);
      *shw2 = rep(rb(g * __builtin_amdgcn_rcpf(1.f + __expf(-z))) * u);
    }
    bar();
    {
      const v4u h0 = *(LAS v4u*)shr2, h1 = *(LAS v4u*)(shr2 + 4);
      const f32x4 y = fold(gemv8(wd, h0, h1, ln.k));
      if (c4 < 8 && row < 2) stsc4(ws, tg4(y, T), o5, WS_S5 + (j * 72 + f) * 256);
    }
    STAMP(7);
    FINISH();
    return;
  }

  {
    int tile = 16 + (w >> 2) * 8 + xcd;
    if (w >= 8 && xcd >= 4) {
      const int r = xcd - 4 - ((xstar >= 4 && xcd > xstar) ? 1 : 0);
      if (r >= (xstar < 4 ? 3 : 2)) return;
      tile = 16 + r * 8 + xstar;
    }
    if (threadIdx.x < 3) L[L_CNT + threadIdx.x] = 0u;
    const rsrc_t ws = mk(p.ws, WS_END), rwm = mk(p.wm, D * 6 * D * 2);
    Tile18 t;
    tile18_load(t, p.c, rwm, w & 3, 2304 + tile * 128, lane);
    LOAD_EPOCH();
    __builtin_amdgcn_sched_barrier(0);
    bar();
    make_lane(ln, lane, sh);
    GET_TAG();
    modb_finish(t, sh, ln, Lf, L_REDA + (w >> 2) * 512, L + L_CNT + (w >> 2), w & 3, lane, ws, tile, T);
    STAMP(7);
    FINISH();
    return;
  }

  }
  const int g1 = (slot + 11) >> 5, g = 2 * xcd + g1, k = slot - 10 - 11 * g1;
  const bool light = k == 10;
  const int s3 = WS_S3 + (g * 11 + k) * S3_STRIDE;
  __builtin_amdgcn_sched_barrier(0);
  const rsrc_t ws = mk(p.ws, WS_END), rwm = mk(p.wm, D * 6 * D * 2);
  unsigned E = 0, T = 0;
  v4u wt[8];
  const rsrc_t rwo = mk(p.wo, (k < 9 && w < 4) ? 4096 * D * 2 : 0);
  if (light && w >= 8) {
    Tile18 t;
    tile18_load(t, p.c, rwm, w - 8, 2304 + g * 128, lane);
    LOAD_EPOCH();
    if (w == 8 && lane < 32) Lf[L_MW + lane] = -INFINITY;
    if (w == 9 && lane < 32) Lf[L_LW + lane] = 0.f;
    if (w == 10 && lane < 3) L[L_CNT + lane] = 0u;
    bar();
    make_lane(ln, lane, sh);
    E = (unsigned)__builtin_amdgcn_readfirstlane((int)Ev); T = (E + 1u) & 0xFFu;
    modb_finish(t, sh, ln, Lf, L_REDA, L + L_CNT + 1, w - 8, lane, ws, g, T);
    STAMP(1);
    rawbar();
    rawbar();
  } else {
    const size_t kvoff = ((size_t)g * SK + (SK - WIN) + k * 384 + w * 32) * 128;
    const rsrc_t rk = mk(p.kc + kvoff, 8192), rv = mk(p.vc + kvoff, 8192);
    v4u kk[8], vv[8];
    if (WO_POS == 0) loads8(wt, rwo, 1152, lane, (256 * g + 64 * (w & 3)) * 1152 + k * 128);
    const int kvo = (lane & 7) * 256 + ((lane >> 3) & 1) * 64 + row * 16, vvo = row * 2048 + c4 * 16;
#pragma unroll
    for (int t = 0; t < 4; ++t) {
      kk[2 * t] = ldnt(rk, kvo, t * 2048);
      kk[2 * t + 1] = ldnt(rk, kvo + 128, t * 2048);
    }
    const unsigned tv0 = __builtin_amdgcn_raw_buffer_load_b32(rv, 0, 0, 2);
    const unsigned tv1 = __builtin_amdgcn_raw_buffer_load_b32(mk(p.out, (light && w == 0) ? D * 2 : 0), 0, 0, 0);
    const unsigned tv2 = __builtin_amdgcn_raw_buffer_load_b32(rwo, (lane & 1) * 63 * 1152, (256 * g + 64 * (w & 3)) * 1152 + k * 128, 2);
    LOAD_EPOCH();
    if (!light) {
      if (w == 8 && lane < 32) Lf[L_MW + lane] = -INFINITY;
      if (w == 9 && lane < 32) Lf[L_LW + lane] = 0.f;
      if (w == 10 && lane < 3) L[L_CNT + lane] = 0u;
    }
    __builtin_amdgcn_sched_barrier(0);
    bar();
    if (WO_POS == 1) loads8(wt, rwo, 1152, lane, (256 * g + 64 * (w & 3)) * 1152 + k * 128);
#pragma unroll
    for (int s = 0; s < 8; ++s) vv[s] = ldnt(rv, vvo + s * 256, 0);
    __builtin_amdgcn_sched_barrier(0);
    make_lane(ln, lane, sh);
    ln.put += lbase / 4;
    E = (unsigned)__builtin_amdgcn_readfirstlane((int)Ev); T = (E + 1u) & 0xFFu;
    LAS unsigned* qrd = L + L_Q + (lane & 1) * 64 + ((lane >> 1) & 1) * 16 + row * 4;
    LAS float* mwr = Lf + L_MW + ((lane >> 3) & 1) * 16;
    LAS float* owr = Lf + L_O + w * 256 + (row >> 1) * 128 + 8 * c4 + 4 * (row & 1);
    pin(qrd); pin(mwr); pin(owr);
    asm volatile("" ::"v"(tv0), "v"(tv1), "v"(tv2));
    STAMP(1);
    if (w == 0) {
      const int oq = lane * 16, sq = WS_SQ + g * 1024;
      v4u q;
      for (unsigned spin = 0;; ++spin) {
        asm volatile("" ::: "memory");
        q = ldsc(ws, oq, sq) ^ T;
        if (allok(or4(q)) || spin > SPIN_MAX) break;
        __builtin_amdgcn_s_sleep(2);
      }
      *(LAS v2u*)(L + L_Q + lane * 2) = v2u{(q.x >> 16) | q.y, (q.z >> 16) | q.w};
    }
    bar();
    STAMP(2);
    float sc = 0.f;
    {
      const v4u q0 = *(LAS v4u*)qrd, q1 = *(LAS v4u*)(qrd + 32);
#pragma unroll
      for (int t = 0; t < 4; ++t) {
        f32x4 d = {0.f, 0.f, 0.f, 0.f};
        d = mfma(q0, kk[2 * t], d);
        d = mfma(q1, kk[2 * t + 1], d);
        const float h0 = d.x + dpp<0x128>(d.z), h1 = d.w + dpp<0x128>(d.y);
        const float v = c4 < 8 ? h0 : h1;
        sc = row == t ? v : sc;
      }
      sc = rb(rb(sc) * 0.08838834764831845f);
      float m = sc;
      m = vmax(m, dpp<0xB1>(m)); m = vmax(m, dpp<0x4E>(m)); m = vmax(m, dpp<0x141>(m));
      m = xrow_max(m);
      if ((lane & 55) == 0) mwr[w] = m;
    }
    bar();
    STAMP(3);
    {
      const float M = max12(mwr);
      const float pe = __expf(sc - M);
      v4u h0, h1;
      shuf(sh + lane, ln.shr, pe, h0, h1);
      float l = pe;
      l += dpp<0xB1>(l); l += dpp<0x4E>(l); l += dpp<0x141>(l);
      l = xrow_sum(l);
      if ((lane & 55) == 0) mwr[32 + w] = l;
      lds_arrive(L + L_CNT + 2);
      v4u A[8];
      A[0] = diag(h0.x, ln.k); A[1] = diag(h0.y, ln.k); A[2] = diag(h0.z, ln.k); A[3] = diag(h0.w, ln.k);
      A[4] = diag(h1.x, ln.k); A[5] = diag(h1.y, ln.k); A[6] = diag(h1.z, ln.k); A[7] = diag(h1.w, ln.k);
      __builtin_amdgcn_sched_barrier(0);
      f32x4 o0 = {0.f, 0.f, 0.f, 0.f}, o1 = {0.f, 0.f, 0.f, 0.f};
      o0 = mfma(A[0], vv[0], o0); o1 = mfma(A[1], vv[1], o1); o0 = mfma(A[2], vv[2], o0); o1 = mfma(A[3], vv[3], o1);
      o0 = mfma(A[4], vv[4], o0); o1 = mfma(A[5], vv[5], o1); o0 = mfma(A[6], vv[6], o0); o1 = mfma(A[7], vv[7], o1);
      *(LAS f32x4*)owr = o0 + o1;
      lds_arrive(L + L_CNT);
    }
    STAMP(4);
    if (w >= 7) __builtin_amdgcn_s_setprio(3);
    if (w == 7) {
      lds_wait(L + L_CNT + 2, light ? 8u : 12u);
      LAS float* lw = Lf + L_LW + (lane & 1) * 16;
      LAS float* mw = Lf + L_MW + (lane & 1) * 16;
      const f32x4 a0 = *(LAS f32x4*)lw, a1 = *(LAS f32x4*)(lw + 4), a2 = *(LAS f32x4*)(lw + 8);
      const float l = ((a0.x + a0.y) + (a0.z + a0.w)) + ((a1.x + a1.y) + (a1.z + a1.w)) + ((a2.x + a2.y) + (a2.z + a2.w));
      const float M = max12(mw);
      if (lane < 2) __builtin_amdgcn_raw_buffer_store_b64(v2u{tg(M, T), tg(l, T)}, ws, lane * 8, s3 + 1024, 16);
    }
  }
  if (w >= 8) {
    __builtin_amdgcn_s_setprio(3);
    const int wp = w - 8;
    LAS float* o = Lf + L_O + row * 256 + wp * 64 + c4 * 4;
    int so = wp * 256 + col * 4;
    pin(o); pin(so);
    lds_wait(L + L_CNT, light ? 8u : 12u);
    f32x4 v = *(LAS f32x4*)o + *(LAS f32x4*)(o + 1024);
    if (!light) v += *(LAS f32x4*)(o + 2048);
    stsc1(ws, tg(tr4(v), T), so, s3);
    STAMP(5);
    if (!(light && g < 9 && w == 8)) { FINISH(); return; }
  }
  if (k < 9) {
    if (w >= 4) { FINISH(); return; }
    if (WO_POS == 2) loads8(wt, rwo, 1152, lane, (256 * g + 64 * w) * 1152 + k * 128);
    __builtin_amdgcn_sched_barrier(0);
    const int k2 = row + 8 > 10 ? 10 : row + 8;
    const int gb = WS_S3 + g * 11 * S3_STRIDE, hd = (w >> 1) * 8;
    int oa = row * S3_STRIDE + w * 256 + c4 * 16, oc = k2 * S3_STRIDE + w * 256 + c4 * 16;
    int ma_ = row * S3_STRIDE + 1024 + hd, mc_ = k2 * S3_STRIDE + 1024 + hd;
    pin(oa); pin(oc); pin(ma_); pin(mc_);
    v2u m0, m1, m2;
    v4u o0, o1, o2;
    for (unsigned spin = 0;; ++spin) {
      asm volatile("" ::: "memory");
      m0 = ldsc2(ws, ma_, gb); m1 = ldsc2(ws, ma_, gb + 4 * S3_STRIDE); m2 = ldsc2(ws, mc_, gb);
      o0 = ldsc(ws, oa, gb); o1 = ldsc(ws, oa, gb + 4 * S3_STRIDE); o2 = ldsc(ws, oc, gb);
      __builtin_amdgcn_sched_barrier(0);
      m0 ^= T; m1 ^= T; m2 ^= T; o0 ^= T; o1 ^= T; o2 ^= T;
      if (allok((m0.x | m0.y) | (m1.x | m1.y) | (m2.x | m2.y) | or4(o0) | or4(o1) | or4(o2)) || spin > SPIN_MAX) break;
      __builtin_amdgcn_s_sleep(POLL_SLEEP);
    }
    const float ma = asf(m0.x), mb = asf(m1.x), mc = row == 3 ? -INFINITY : asf(m2.x);
    const float MM = xrow_max(vmax(vmax(ma, mb), mc));
    float fa = __expf(ma - MM), fb = __expf(mb - MM), fc = __expf(mc - MM);
    const float inv = __builtin_amdgcn_rcpf(xrow_sum(fa * asf(m0.y) + fb * asf(m1.y) + fc * asf(m2.y)));
    STAMP(6);
    v4u h0, h1;
    shuf(ln.shw, ln.shr, tr4(asf4(o0) * fa + asf4(o1) * fb + asf4(o2) * fc) * inv, h0, h1);
    put_halves(ln, L_REDA + w * 128, gemv8(wt, h0, h1, ln.k));
    bar();
    if (w == 0) stsc1(ws, tg(sumN<8>(Lf + L_REDA + lane), T), lane * 4, WS_S4 + (k * 16 + g) * 256);
    STAMP(7);
    FINISH();
    return;
  }
  if (!(light && g < 9) || w > 8) { FINISH(); return; }
  {
    const int j = g;
    float x1 = 0.f, gbv = 0.f;
    if (w == 0) {
      const float xv = bf(p.x[64 * j + col]);
      unsigned a2, b2;
      v4u q0, q1, q2, q3;
      for (unsigned spin = 0;; ++spin) {
        asm volatile("" ::: "memory");
        const int vo = (j * 16 + row) * 256 + c4 * 16;
        a2 = ldsc1(ws, col * 4, WS_SB + j * 256); b2 = ldsc1(ws, col * 4, WS_SB + (27 + j) * 256);
        q0 = ldsc(ws, vo, WS_S4); q1 = ldsc(ws, vo, WS_S4 + 1024); q2 = ldsc(ws, vo, WS_S4 + 2048); q3 = ldsc(ws, vo, WS_S4 + 3072);
        __builtin_amdgcn_sched_barrier(0);
        q0 ^= T; q1 ^= T; q2 ^= T; q3 ^= T; a2 ^= T; b2 ^= T;
        if (allok(a2 | b2 | or4(q0) | or4(q1) | or4(q2) | or4(q3)) || spin > SPIN_MAX) break;
        __builtin_amdgcn_s_sleep(8);
      }
      x1 = rb(xv + rb(asf(a2) * rb(tr4((asf4(q0) + asf4(q1)) + (asf4(q2) + asf4(q3))))));
      gbv = asf(b2);
      pin(x1); pin(gbv);
    }
    STAMP(6);
    {
      v4u y0, y1;
      int vo = (8 * w + row) * 256 + c4 * 16;
      LAS float* rw = Lf + L_REDG + w * 64 + col;
      pin(vo); pin(rw);
      for (unsigned spin = 0;; ++spin) {
        asm volatile("" ::: "memory");
        y0 = ldsc(ws, vo, WS_S5 + j * 72 * 256); y1 = ldsc(ws, vo, WS_S5 + j * 72 * 256 + 1024);
        __builtin_amdgcn_sched_barrier(0);
        y0 ^= T; y1 ^= T;
        if (allok(or4(y0) | or4(y1)) || spin > SPIN_MAX) break;
        __builtin_amdgcn_s_sleep(1);
      }
      *rw = tr4(asf4(y0) + asf4(y1));
    }
    bar();
    if (w == 0) {
      LAS float* r = Lf + L_REDG + col;
      const float y = ((r[0] + r[64]) + (r[128] + r[192])) + ((r[256] + r[320]) + (r[384] + r[448])) + r[512];
      f32x2 o = {x1 + rb(gbv * rb(y)), 0.f};
      p.out[64 * j + col] = (u16)(__builtin_bit_cast(unsigned, __builtin_convertvector(o, bf16x2)) & 0xFFFFu);
      if (j == 0 && lane == 0) __builtin_amdgcn_raw_buffer_store_b32(E + 1u, ws, 0, WS_EPOCH, 16);
    }
    STAMP(7);
    FINISH();
  }
}

}

extern "C" void kernel_launch(void* const* d_in, const int* in_sizes, int n_in, void* d_out, int out_size, void* d_ws, size_t ws_size,
                              hipStream_t stream) {
  Params p;
  p.x = (const u16*)d_in[0]; p.kc = (const u16*)d_in[1]; p.vc = (const u16*)d_in[2]; p.wq = (const u16*)d_in[3]; p.wo = (const u16*)d_in[4];
  p.wg = (const u16*)d_in[6]; p.wu = (const u16*)d_in[7]; p.wd = (const u16*)d_in[8]; p.c = (const u16*)d_in[9]; p.wm = (const u16*)d_in[10];
  p.out = (u16*)d_out; p.ws = (unsigned char*)d_ws;
  fused_decode<<<256, NTHR, 0, stream>>>(p);
}
```

```cpp
#include <hip/hip_runtime.h>
#include <hip/hip_bf16.h>
#include <stdint.h>

namespace {

typedef unsigned short u16;
typedef unsigned v4u __attribute__((ext_vector_type(4)));
typedef unsigned v2u __attribute__((ext_vector_type(2)));
typedef float f32x4 __attribute__((ext_vector_type(4)));
typedef float f32x2 __attribute__((ext_vector_type(2)));
typedef __bf16 bf16x8 __attribute__((ext_vector_type(8)));
typedef __bf16 bf16x2 __attribute__((ext_vector_type(2)));
typedef __amdgpu_buffer_rsrc_t rsrc_t;
#define LAS __attribute__((address_space(3)))

constexpr int D = 576, SK = 65536, WIN = 4096, FF = 4608;
constexpr int NTHR = 768;
constexpr int WS_EPOCH = 0;
constexpr int WS_S1 = 4096;
constexpr int WS_SQ = WS_S1 + 72 * 256;
constexpr int WS_SB = WS_SQ + 64 * 256;
constexpr int WS_S4 = WS_SB + 36 * 256;
constexpr int WS_S5 = WS_S4 + 144 * 256;
constexpr int WS_S3 = WS_S5 + 648 * 256;
constexpr int S3_STRIDE = 1280;
constexpr int WS_END = WS_S3 + 176 * S3_STRIDE;
constexpr unsigned SPIN_MAX = 1u << 19;

struct Params {
  const u16 *x, *kc, *vc, *wq, *wo, *wg, *wu, *wd, *c, *wm;
  u16* out;
  unsigned char* ws;
};

constexpr int SH_STRIDE = 160;
constexpr int L_SH = 0;
constexpr int L_REDA = L_SH + 12 * SH_STRIDE;
constexpr int L_REDG = L_REDA + 1536;
constexpr int L_ST = L_REDG + 1152;
constexpr int L_Q = L_ST + 64;
constexpr int L_MW = L_Q + 128;
constexpr int L_LW = L_MW + 32;
constexpr int L_CNT = L_LW + 32;
constexpr int L_O = L_CNT + 16;
constexpr int L_END = L_O + 12 * 256;

__device__ __forceinline__ rsrc_t mk(const void* p, unsigned bytes) {
  return __builtin_amdgcn_make_buffer_rsrc((void*)p, 0, (int)bytes, 0x00020000);
}
__device__ __forceinline__ v4u ldnt(rsrc_t r, int v, int s) { return __builtin_amdgcn_raw_buffer_load_b128(r, v, s, 2); }
__device__ __forceinline__ v4u ldsc(rsrc_t r, int v, int s) { return __builtin_amdgcn_raw_buffer_load_b128(r, v, s, 16); }
__device__ __forceinline__ unsigned ldsc1(rsrc_t r, int v, int s) { return __builtin_amdgcn_raw_buffer_load_b32(r, v, s, 16); }
__device__ __forceinline__ void stsc4(rsrc_t r, v4u x, int v, int s) { __builtin_amdgcn_raw_buffer_store_b128(x, r, v, s, 16); }
__device__ __forceinline__ float asf(unsigned x) { return __uint_as_float(x); }
__device__ __forceinline__ unsigned asu(float x) { return __float_as_uint(x); }
template <class V> __device__ __forceinline__ void pin(V& x) { asm volatile("" : "+v"(x)); }
__device__ __forceinline__ unsigned rep(float x) {
  f32x2 v = {x, x};
  return __builtin_bit_cast(unsigned, __builtin_convertvector(v, bf16x2));
}
__device__ __forceinline__ float rb(float x) { return asf(rep(x) & 0xFFFF0000u); }
__device__ __forceinline__ float bf(u16 x) { return asf((unsigned)x << 16); }
__device__ __forceinline__ unsigned tg(float x, unsigned T) { return (asu(x) & 0xFFFFFF00u) | T; }
__device__ __forceinline__ unsigned tgr(float x, unsigned T) { return ((asu(x) + 0x80u) & 0xFFFFFF00u) | T; }
__device__ __forceinline__ v4u tg4(f32x4 y, v4u T) { return v4u{tg(y.x, T.x), tg(y.y, T.y), tg(y.z, T.z), tg(y.w, T.w)}; }
__device__ __forceinline__ v4u make_tag(unsigned e, unsigned xw, unsigned cw) {
  const unsigned t = (e + 1u) * 0x01010101u + (xw ^ (cw * 0x9E3779B1u)) * 0x85EBCA6Bu;
  return v4u{t & 0xFFu, (t >> 8) & 0xFFu, (t >> 16) & 0xFFu, t >> 24};
}
template <int C> __device__ __forceinline__ float dpp(float x);
__device__ __forceinline__ void pub64(rsrc_t ws, float v, v4u T, int lane, int soff);
__device__ __forceinline__ unsigned or4(v4u d) { return (d.x | d.y) | (d.z | d.w); }
__device__ __forceinline__ bool allok(unsigned bad) { return __all((bad & 0xFFu) == 0u); }
template <int C> __device__ __forceinline__ float dpp(float x) {
  return asf((unsigned)__builtin_amdgcn_update_dpp(0, (int)asu(x), C, 0xF, 0xF, true));
}
__device__ __forceinline__ void pub64(rsrc_t ws, float v, v4u T, int lane, int soff) {
  const v4u d = {tg(v, T.x), tg(dpp<0x55>(v), T.y), tg(dpp<0xAA>(v), T.z), tg(dpp<0xFF>(v), T.w)};
  if ((lane & 3) == 0) __builtin_amdgcn_raw_buffer_store_b128(d, ws, lane * 4, soff, 16);
}
__device__ __forceinline__ float xrow_sum(float x) {
  auto s = __builtin_amdgcn_permlane16_swap(asu(x), asu(x), false, false);
  x = asf(s[0]) + asf(s[1]);
  auto t = __builtin_amdgcn_permlane32_swap(asu(x), asu(x), false, false);
  return asf(t[0]) + asf(t[1]);
}
__device__ __forceinline__ float xrow_max(float x) {
  auto s = __builtin_amdgcn_permlane16_swap(asu(x), asu(x), false, false);
  x = __builtin_amdgcn_fmed3f(asf(s[0]), asf(s[1]), INFINITY);
  auto t = __builtin_amdgcn_permlane32_swap(asu(x), asu(x), false, false);
  return __builtin_amdgcn_fmed3f(asf(t[0]), asf(t[1]), INFINITY);
}
__device__ __forceinline__ float tr4(f32x4 v) {
  auto a = __builtin_amdgcn_permlane16_swap(asu(v.x), asu(v.y), false, false);
  auto b = __builtin_amdgcn_permlane16_swap(asu(v.z), asu(v.w), false, false);
  const float s01 = asf(a[0]) + asf(a[1]), s23 = asf(b[0]) + asf(b[1]);
  auto c = __builtin_amdgcn_permlane32_swap(asu(s01), asu(s23), false, false);
  return asf(c[0]) + asf(c[1]);
}
__device__ __forceinline__ float wave_sum2(float x, float y) {
  x += dpp<0xB1>(x); y += dpp<0xB1>(y);
  x += dpp<0x4E>(x); y += dpp<0x4E>(y);
  x += dpp<0x141>(x); y += dpp<0x141>(y);
  x += dpp<0x140>(x); y += dpp<0x140>(y);
  auto a = __builtin_amdgcn_permlane16_swap(asu(x), asu(y), false, false);
  const float s = asf(a[0]) + asf(a[1]);
  auto c = __builtin_amdgcn_permlane32_swap(asu(s), asu(s), false, false);
  return asf(c[0]) + asf(c[1]);
}
__device__ __forceinline__ f32x4 asf4(v4u d) { return __builtin_bit_cast(f32x4, d); }
__device__ __forceinline__ f32x4 mfma(v4u a, v4u b, f32x4 c) {
  return __builtin_amdgcn_mfma_f32_16x16x32_bf16(__builtin_bit_cast(bf16x8, a), __builtin_bit_cast(bf16x8, b), c, 0, 0, 0);
}
struct Mask { unsigned m0, m1, m2, m3; };
__device__ __forceinline__ v4u diag(unsigned hh, const Mask& k) { return v4u{hh & k.m0, hh & k.m1, hh & k.m2, hh & k.m3}; }
__device__ __forceinline__ void bar() {
  asm volatile("s_waitcnt lgkmcnt(0)" ::: "memory");
  __builtin_amdgcn_s_barrier();
  asm volatile("" ::: "memory");
}
__device__ __forceinline__ void rawbar() {
  __builtin_amdgcn_sched_barrier(0);
  __builtin_amdgcn_s_barrier();
  __builtin_amdgcn_sched_barrier(0);
}
__device__ __forceinline__ float silu(float v) { return v * __builtin_amdgcn_rcpf(1.f + __expf(-v)); }
__device__ __forceinline__ float fold1(float x) {
  auto t = __builtin_amdgcn_permlane32_swap(asu(x), asu(x), false, false);
  return asf(t[0]) + dpp<0x128>(asf(t[1]));
}
__device__ __forceinline__ f32x4 fold(f32x4 a) { return f32x4{fold1(a.x), fold1(a.y), fold1(a.z), fold1(a.w)}; }
__device__ __forceinline__ float fold2_1(float a, float b) {
  auto t = __builtin_amdgcn_permlane32_swap(asu(a), asu(b), false, false);
  return asf(t[0]) + dpp<0x128>(asf(t[1]));
}


struct Lane {
  Mask k;
  LAS unsigned* shw;
  LAS unsigned* shr;
  LAS float* put;
  bool putv;
};
__device__ __forceinline__ void shuf(LAS unsigned* wr, LAS unsigned* rd, float v, v4u& h0, v4u& h1) {
  *wr = rep(v);
  asm volatile("" ::: "memory");
  h0 = *(LAS v4u*)rd;
  h1 = *(LAS v4u*)(rd + 4);
}
__device__ __forceinline__ f32x4 gemv8(const v4u (&w)[8], v4u h0, v4u h1, const Mask& k) {
  f32x4 a = {0.f, 0.f, 0.f, 0.f}, b = {0.f, 0.f, 0.f, 0.f};
  const v4u a0 = diag(h0.x, k), a1 = diag(h0.y, k), a2 = diag(h0.z, k), a3 = diag(h0.w, k);
  a = mfma(a0, w[0], a); b = mfma(a1, w[1], b);
  const v4u a4 = diag(h1.x, k), a5 = diag(h1.y, k);
  a = mfma(a2, w[2], a); b = mfma(a3, w[3], b);
  const v4u a6 = diag(h1.z, k), a7 = diag(h1.w, k);
  a = mfma(a4, w[4], a); b = mfma(a5, w[5], b);
  a = mfma(a6, w[6], a); b = mfma(a7, w[7], b);
  return a + b;
}
__device__ __forceinline__ f32x2 ln_sums(LAS float* st) {
  const f32x4 a = *(LAS f32x4*)(st), b = *(LAS f32x4*)(st + 4), c = *(LAS f32x4*)(st + 8), d = *(LAS f32x4*)(st + 12);
  const f32x2 e = *(LAS f32x2*)(st + 16);
  const f32x4 t = (a + b) + (c + d);
  return f32x2{t.x + t.z + e.x, t.y + t.w + e.y};
}
template <int N> __device__ __forceinline__ float sumN(LAS float* red) {
  float a = 0.f, b = 0.f;
#pragma unroll
  for (int i = 0; i < N; i += 2) { a += red[i * 64]; b += red[i * 64 + 64]; }
  return a + b;
}
__device__ __forceinline__ void loads8(v4u (&w)[8], rsrc_t r, int stride, int lane, int base) {
  const int vo = (lane >> 3) * stride + (lane & 7) * 16;
#pragma unroll
  for (int s = 0; s < 8; ++s) w[s] = ldnt(r, vo, base + 8 * s * stride);
}
struct Tile18 { v4u w[18]; v2u c; };
__device__ __forceinline__ void tile18_load(Tile18& t, const u16* c, rsrc_t rwm, int ks, int cb, int lane) {
  t.c = *(const v2u*)(c + 144 * ks + 4 * (lane < 36 ? lane : 35));
  const int vo = (lane >> 3) * 6912 + (lane & 7) * 16;
#pragma unroll
  for (int s = 0; s < 18; ++s) t.w[s] = ldnt(rwm, vo, (144 * ks + 8 * s) * 6912 + cb);
}
__device__ __forceinline__ f32x4 tile18_mac(const Tile18& t, LAS unsigned* sh, int lane, const Mask& k) {
  if (lane < 36) {
    LAS unsigned* d = sh + (4 * (lane & 1)) * 20 + (lane >> 1);
    d[0] = rep(silu(asf(t.c.x << 16))); d[20] = rep(silu(asf(t.c.x & 0xFFFF0000u)));
    d[40] = rep(silu(asf(t.c.y << 16))); d[60] = rep(silu(asf(t.c.y & 0xFFFF0000u)));
  }
  asm volatile("" ::: "memory");
  LAS unsigned* s = sh + (lane >> 3) * 20;
  const v4u h0 = *(LAS v4u*)s, h1 = *(LAS v4u*)(s + 4), h2 = *(LAS v4u*)(s + 8), h3 = *(LAS v4u*)(s + 12);
  const v2u h4 = *(LAS v2u*)(s + 16);
  f32x4 a = {0.f, 0.f, 0.f, 0.f}, b = {0.f, 0.f, 0.f, 0.f};
  a = mfma(diag(h0.x, k), t.w[0], a); b = mfma(diag(h0.y, k), t.w[1], b); a = mfma(diag(h0.z, k), t.w[2], a); b = mfma(diag(h0.w, k), t.w[3], b);
  a = mfma(diag(h1.x, k), t.w[4], a); b = mfma(diag(h1.y, k), t.w[5], b); a = mfma(diag(h1.z, k), t.w[6], a); b = mfma(diag(h1.w, k), t.w[7], b);
  a = mfma(diag(h2.x, k), t.w[8], a); b = mfma(diag(h2.y, k), t.w[9], b); a = mfma(diag(h2.z, k), t.w[10], a); b = mfma(diag(h2.w, k), t.w[11], b);
  a = mfma(diag(h3.x, k), t.w[12], a); b = mfma(diag(h3.y, k), t.w[13], b); a = mfma(diag(h3.z, k), t.w[14], a); b = mfma(diag(h3.w, k), t.w[15], b);
  a = mfma(diag(h4.x, k), t.w[16], a); b = mfma(diag(h4.y, k), t.w[17], b);
  return a + b;
}
__device__ __forceinline__ void lds_arrive(LAS unsigned* c) {
  if ((threadIdx.x & 63) == 0) asm volatile("ds_add_u32 %0, %1" ::"v"(c), "v"(1u) : "memory");
  asm volatile("" ::: "memory");
}
__device__ __forceinline__ float vmax(float a, float b) { return __builtin_amdgcn_fmed3f(a, b, INFINITY); }
__device__ __forceinline__ float max12(LAS float* m) {
  const f32x4 b0 = *(LAS f32x4*)m, b1 = *(LAS f32x4*)(m + 4), b2 = *(LAS f32x4*)(m + 8);
  return vmax(vmax(vmax(vmax(b0.x, b0.y), vmax(b0.z, b0.w)), vmax(vmax(b1.x, b1.y), vmax(b1.z, b1.w))), vmax(vmax(b2.x, b2.y), vmax(b2.z, b2.w)));
}
__device__ __forceinline__ unsigned lds_inc(LAS unsigned* c) {
  asm volatile("" ::: "memory");
  unsigned old = 0;
  if ((threadIdx.x & 63) == 0) old = __hip_atomic_fetch_add(c, 1u, __ATOMIC_RELAXED, __HIP_MEMORY_SCOPE_WORKGROUP);
  old = (unsigned)__builtin_amdgcn_readfirstlane((int)old);
  asm volatile("" ::: "memory");
  return old;
}
__device__ __forceinline__ void lds_wait(LAS unsigned* c, unsigned need) {
  for (unsigned spin = 0; *(volatile LAS unsigned*)c < need && spin < (1u << 28); ++spin) {}
  asm volatile("" ::: "memory");
}
__device__ __forceinline__ void make_lane(Lane& ln, int lane, LAS unsigned* sh) {
  const unsigned mm = (lane & 1) ? 0xFFFF0000u : 0x0000FFFFu;
  const int d = (lane & 7) >> 1, c = lane & 15, g = lane >> 4;
  ln.k.m0 = d == 0 ? mm : 0u; ln.k.m1 = d == 1 ? mm : 0u; ln.k.m2 = d == 2 ? mm : 0u; ln.k.m3 = d == 3 ? mm : 0u;
  ln.shw = sh + 32 * (c & 1) + 8 * g + (c >> 1);
  ln.shr = sh + (lane >> 3) * 8;
  ln.put = (LAS float*)0 + (c >> 3) * 64 + 8 * (c & 7) + 4 * (g & 1);
  ln.putv = (c >> 3) == (g >> 1);
  pin(ln.k.m0); pin(ln.k.m1); pin(ln.k.m2); pin(ln.k.m3); pin(ln.shw); pin(ln.shr); pin(ln.put);
}
__device__ __forceinline__ void put_halves(const Lane& ln, int base_dw, f32x4 a) {
  if (ln.putv) *(LAS f32x4*)(ln.put + base_dw) = a;
}
__device__ __forceinline__ void modb_finish(const Tile18& t, LAS unsigned* sh, const Lane& ln, LAS float* Lf, int red_dw, LAS unsigned* cnt, int ks, int lane,
                                            rsrc_t ws, int tile, v4u T) {
  put_halves(ln, red_dw + ks * 128, tile18_mac(t, sh, lane, ln.k));
  if (lds_inc(cnt) == 3u) pub64(ws, rb(sumN<8>(Lf + red_dw + lane)), T, lane, WS_SB + tile * 256);
}

__global__ void __launch_bounds__(NTHR, 3) fused_decode(Params p) {
  __shared__ __attribute__((aligned(16))) unsigned smem[L_END];
  LAS unsigned* L = (LAS unsigned*)smem;
  LAS float* Lf = (LAS float*)smem;
  const int b = blockIdx.x, xcd = b & 7, slot = b >> 3;
  const int lane = threadIdx.x & 63, w = __builtin_amdgcn_readfirstlane(threadIdx.x >> 6);
  const int c4 = lane & 15, row = lane >> 4;
  const int col = 4 * c4 + row;
#define LOAD_EPOCH() __builtin_amdgcn_sched_barrier(0); const unsigned Ev = __builtin_amdgcn_raw_buffer_load_b32(ws, 0, WS_EPOCH, 16); \
  const unsigned Xv = __builtin_amdgcn_raw_buffer_load_b32(mk(p.x, 4), 0, 0, 0), Cv = __builtin_amdgcn_raw_buffer_load_b32(mk(p.c, 4), 0, 0, 0); __builtin_amdgcn_sched_barrier(0)
#define TAG_NOW() (E = (unsigned)__builtin_amdgcn_readfirstlane((int)Ev), T = make_tag(E, (unsigned)__builtin_amdgcn_readfirstlane((int)Xv), (unsigned)__builtin_amdgcn_readfirstlane((int)Cv)))
#define GET_TAG() unsigned E; v4u T; TAG_NOW(); (void)E
  LAS unsigned* sh = L + L_SH + w * SH_STRIDE;
  const int lbase = (int)(size_t)(LAS void*)smem;
  Lane ln;

  if (slot <= 9) {
  const int rgate = (int)((size_t)p.wg >> 7), xstar = (3 - rgate) & 7;
  const bool qonly = slot == 9 && xcd == xstar;
  if (slot < 9 || qonly) {
    if (w >= 10) return;
    const int f = xcd * 9 + slot;
    __builtin_amdgcn_sched_barrier(0);
    const rsrc_t ws = mk(p.ws, WS_END), rwm = mk(p.wm, D * 6 * D * 2);
    if (w == 9) {
      __builtin_amdgcn_s_setprio(3);
      if (lane == 0) L[L_CNT] = 0u;
      if (qonly) { bar(); return; }
      Tile18 t;
      tile18_load(t, p.c, rwm, f / 18, (f % 18) * 128, lane);
      LOAD_EPOCH();
      bar();
      make_lane(ln, lane, sh);
      GET_TAG();
      const f32x4 a = fold(tile18_mac(t, sh, lane, ln.k));
      if (c4 < 8 && row < 2) stsc4(ws, v4u{tgr(a.x, T.x), tgr(a.y, T.y), tgr(a.z, T.z), tgr(a.w, T.w)}, (8 * c4 + 4 * row) * 4, WS_S1 + f * 256);
      return;
    }
    const int j = w;
    const int s0 = (3 - rgate - xcd) & 7;
    const bool hasq = qonly || !(slot == s0 || (s0 == 0 && slot == 8));
    const int n = qonly ? xcd * 8 + 7 : xcd * 8 + slot - (slot > s0 ? 1 : 0);
    const u16 xr = p.x[64 * j + col], x0r = p.x[0];
    LOAD_EPOCH();
    rawbar();
    v4u wt[8], wg[8], wu[8], wd[8];
    const rsrc_t rg = mk(p.wg, D * FF * 2), ru = mk(p.wu, D * FF * 2), rd = mk(p.wd, FF * D * 2);
    loads8(wt, mk(p.wq, hasq ? D * 4096 * 2 : 0), 8192, lane, 64 * j * 8192 + n * 128);
    unsigned tg0 = __builtin_amdgcn_raw_buffer_load_b32(rg, (lane & 1) * 63 * FF * 2, 64 * j * FF * 2 + f * 128, 2);
    unsigned tg1 = __builtin_amdgcn_raw_buffer_load_b32(ru, (lane & 1) * 63 * FF * 2, 64 * j * FF * 2 + f * 128, 2);
    unsigned tg2 = __builtin_amdgcn_raw_buffer_load_b32(rd, (lane & 1) * 63 * 1152, 64 * f * 1152 + j * 128, 2);
    __builtin_amdgcn_sched_barrier(0);
    make_lane(ln, lane, sh);
    ln.put += lbase / 4;
    GET_TAG();
    const float x0 = bf(x0r);
    float xv = bf(xr);
    {
      const float d = xv - x0;
      const float s = wave_sum2(d, d * d);
      if ((lane & 47) == 0) Lf[L_ST + 2 * j + (lane >> 4)] = s;
    }
    lds_arrive(L + L_CNT);
    lds_wait(L + L_CNT, 9u);
    float mean1, lnv;
    {
      const f32x2 s = ln_sums(Lf + L_ST);
      const float dm = s.x * (1.f / D);
      mean1 = x0 + dm;
      const float var = fmaxf(s.y * (1.f / D) - dm * dm, 0.f);
      const float den = rb(__builtin_amdgcn_sqrtf(rb(rb(var) + 1e-5f)));
      lnv = rb(rb(xv - rb(mean1)) / den);
      pin(lnv); pin(mean1); pin(xv);
    }
    if (hasq) {
      int o1 = (row * 18 + j) * 256 + c4 * 16;
      pin(o1);
      __builtin_amdgcn_s_waitcnt(0x0F70 | 5);
      v4u sa, ca;
      for (unsigned spin = 0;; ++spin) {
        asm volatile("" ::: "memory");
        sa = ldsc(ws, o1, WS_S1); ca = ldsc(ws, o1, WS_S1 + 9 * 256);
        __builtin_amdgcn_sched_barrier(0);
        sa ^= T; ca ^= T;
        if (allok(or4(sa) | or4(ca)) || spin > SPIN_MAX) break;
        __builtin_amdgcn_s_sleep(2);
      }
      const float h = rb(lnv * rb(1.f + rb(tr4(asf4(ca))))) + rb(tr4(asf4(sa)));
      v4u h0, h1;
      shuf(ln.shw, ln.shr, h, h0, h1);
      put_halves(ln, L_REDA + j * 128, gemv8(wt, h0, h1, ln.k));
      bar();
      if (j == 0) pub64(ws, rb(sumN<18>(Lf + L_REDA + lane)), T, lane, WS_SQ + n * 256);
      rawbar();
    }
    asm volatile("" ::"v"(tg0), "v"(tg1), "v"(tg2));
    if (qonly) return;
    loads8(wg, rg, FF * 2, lane, 64 * j * FF * 2 + f * 128);
    loads8(wu, ru, FF * 2, lane, 64 * j * FF * 2 + f * 128);
    loads8(wd, rd, 1152, lane, 64 * f * 1152 + j * 128);
    __builtin_amdgcn_sched_barrier(0);
    int o4 = (j * 16 + row) * 256 + c4 * 16, ob = j * 256 + col * 4, o5 = (8 * c4 + 4 * row) * 4;
    LAS float* redg_w = Lf + L_REDG + j * 128 + (row >> 1) * 64 + 8 * c4 + 4 * (row & 1);
    LAS float* redg_r = Lf + L_REDG + lane;
    LAS unsigned* shw2 = L + L_Q + (lane & 7) * 8 + (lane >> 3);
    LAS unsigned* shr2 = L + L_Q + (lane >> 3) * 8;
    const unsigned Tl = row == 0 ? T.x : row == 1 ? T.y : row == 2 ? T.z : T.w;
    pin(o4); pin(ob); pin(o5); pin(redg_w); pin(redg_r); pin(shw2); pin(shr2);
    asm volatile("s_waitcnt vmcnt(0)" ::: "memory");
    rawbar();
    float x1, sb, cb;
    {
      unsigned a, s2, c2;
      v4u q0, q1, q2, q3;
      q0 = ldsc(ws, o4, WS_S4); q1 = ldsc(ws, o4, WS_S4 + 1024); q2 = ldsc(ws, o4, WS_S4 + 2048); q3 = ldsc(ws, o4, WS_S4 + 3072);
      a = ldsc1(ws, ob, WS_SB); s2 = ldsc1(ws, ob, WS_SB + 9 * 256); c2 = ldsc1(ws, ob, WS_SB + 18 * 256);
      for (unsigned spin = 0; spin <= SPIN_MAX; ++spin) {
        asm volatile("" ::: "memory");
        bool again = false;
        if (!allok(or4(q0 ^ T))) { q0 = ldsc(ws, o4, WS_S4); again = true; }
        if (!allok(or4(q1 ^ T))) { q1 = ldsc(ws, o4, WS_S4 + 1024); again = true; }
        if (!allok(or4(q2 ^ T))) { q2 = ldsc(ws, o4, WS_S4 + 2048); again = true; }
        if (!allok(or4(q3 ^ T))) { q3 = ldsc(ws, o4, WS_S4 + 3072); again = true; }
        if (!allok((a ^ Tl) | (s2 ^ Tl) | (c2 ^ Tl))) {
          a = ldsc1(ws, ob, WS_SB); s2 = ldsc1(ws, ob, WS_SB + 9 * 256); c2 = ldsc1(ws, ob, WS_SB + 18 * 256);
          again = true;
        }
        if (!again) break;
      }
      q0 ^= T; q1 ^= T; q2 ^= T; q3 ^= T; a ^= Tl; s2 ^= Tl; c2 ^= Tl;
      x1 = rb(xv + rb(asf(a) * rb(tr4((asf4(q0) + asf4(q1)) + (asf4(q2) + asf4(q3))))));
      sb = asf(s2); cb = rb(1.f + asf(c2));
    }
    {
      const float d = x1 - mean1;
      const float s = wave_sum2(d, d * d);
      if ((lane & 47) == 0) Lf[L_ST + 32 + 2 * j + (lane >> 4)] = s;
    }
    bar();
    {
      const f32x2 s = ln_sums(Lf + L_ST + 32);
      const float dm = s.x * (1.f / D);
      const float var = fmaxf(s.y * (1.f / D) - dm * dm, 0.f);
      const float den = rb(__builtin_amdgcn_sqrtf(rb(rb(var) + 1e-5f)));
      const float h2 = rb(rb(rb(x1 - rb(mean1 + dm)) * __builtin_amdgcn_rcpf(den)) * cb) + sb;
      v4u h0, h1;
      shuf(ln.shw, ln.shr, h2, h0, h1);
      const f32x4 g = gemv8(wg, h0, h1, ln.k), u = gemv8(wu, h0, h1, ln.k);
      const f32x4 t = {fold2_1(g.x, u.x), fold2_1(g.y, u.y), fold2_1(g.z, u.z), fold2_1(g.w, u.w)};
      if (c4 < 8) *(LAS f32x4*)redg_w = t;
    }
    bar();
    if (j == 1) {
      float g = 0.f, u = 0.f, g2 = 0.f, u2 = 0.f;
#pragma unroll
      for (int i = 0; i < 8; i += 2) {
        g += redg_r[i * 128]; u += redg_r[i * 128 + 64];
        g2 += redg_r[i * 128 + 128]; u2 += redg_r[i * 128 + 192];
      }
      g = rb((g + redg_r[1024]) + g2); u = rb((u + redg_r[1088]) + u2);
      const float z = 1.5957691216057308f * (g + 0.044715f * g * g * g);
      *shw2 = rep(rb(g * __builtin_amdgcn_rcpf(1.f + __expf(-z))) * u);
    }
    bar();
    {
      const v4u h0 = *(LAS v4u*)shr2, h1 = *(LAS v4u*)(shr2 + 4);
      const f32x4 y = fold(gemv8(wd, h0, h1, ln.k));
      if (c4 < 8 && row < 2) stsc4(ws, tg4(y, T), o5, WS_S5 + (j * 72 + f) * 256);
    }
    return;
  }

  {
    int tile = 16 + (w >> 2) * 8 + xcd;
    if (w >= 8 && xcd >= 4) {
      const int r = xcd - 4 - ((xstar >= 4 && xcd > xstar) ? 1 : 0);
      if (r >= (xstar < 4 ? 3 : 2)) return;
      tile = 16 + r * 8 + xstar;
    }
    if (threadIdx.x < 3) L[L_CNT + threadIdx.x] = 0u;
    const rsrc_t ws = mk(p.ws, WS_END), rwm = mk(p.wm, D * 6 * D * 2);
    Tile18 t;
    tile18_load(t, p.c, rwm, w & 3, 2304 + tile * 128, lane);
    LOAD_EPOCH();
    __builtin_amdgcn_sched_barrier(0);
    bar();
    make_lane(ln, lane, sh);
    GET_TAG();
    modb_finish(t, sh, ln, Lf, L_REDA + (w >> 2) * 512, L + L_CNT + (w >> 2), w & 3, lane, ws, tile, T);
    return;
  }

  }
  const int g1 = (slot + 11) >> 5, g = 2 * xcd + g1, k = slot - 10 - 11 * g1;
  const bool light = k == 10;
  const int s3 = WS_S3 + (g * 11 + k) * S3_STRIDE;
  __builtin_amdgcn_sched_barrier(0);
  const rsrc_t ws = mk(p.ws, WS_END), rwm = mk(p.wm, D * 6 * D * 2);
  unsigned E = 0;
  v4u T = {0u, 0u, 0u, 0u};
  v4u wt[8];
  const rsrc_t rwo = mk(p.wo, (k < 9 && w < 4) ? 4096 * D * 2 : 0);
  if (light && w >= 8) {
    Tile18 t;
    tile18_load(t, p.c, rwm, w - 8, 2304 + g * 128, lane);
    LOAD_EPOCH();
    if (w == 8 && lane < 32) Lf[L_MW + lane] = -INFINITY;
    if (w == 9 && lane < 32) Lf[L_LW + lane] = 0.f;
    if (w == 10 && lane < 3) L[L_CNT + lane] = 0u;
    bar();
    make_lane(ln, lane, sh);
    TAG_NOW();
    modb_finish(t, sh, ln, Lf, L_REDA, L + L_CNT + 1, w - 8, lane, ws, g, T);
    rawbar();
    rawbar();
  } else {
    const size_t kvoff = ((size_t)g * SK + (SK - WIN) + k * 384 + w * 32) * 128;
    const rsrc_t rk = mk(p.kc + kvoff, 8192), rv = mk(p.vc + kvoff, 8192);
    v4u kk[8], vv[8];
    const int kvo = (lane & 7) * 256 + ((lane >> 3) & 1) * 64 + row * 16, vvo = row * 2048 + c4 * 16;
#pragma unroll
    for (int t = 0; t < 4; ++t) {
      kk[2 * t] = ldnt(rk, kvo, t * 2048);
      kk[2 * t + 1] = ldnt(rk, kvo + 128, t * 2048);
    }
    const unsigned tv0 = __builtin_amdgcn_raw_buffer_load_b32(rv, 0, 0, 2);
    const unsigned tv1 = __builtin_amdgcn_raw_buffer_load_b32(mk(p.out, (light && w == 0) ? D * 2 : 0), 0, 0, 0);
    const unsigned tv2 = __builtin_amdgcn_raw_buffer_load_b32(rwo, (lane & 1) * 63 * 1152, (256 * g + 64 * (w & 3)) * 1152 + k * 128, 2);
    LOAD_EPOCH();
    if (!light) {
      if (w == 8 && lane < 32) Lf[L_MW + lane] = -INFINITY;
      if (w == 9 && lane < 32) Lf[L_LW + lane] = 0.f;
      if (w == 10 && lane < 3) L[L_CNT + lane] = 0u;
    }
    __builtin_amdgcn_sched_barrier(0);
    bar();
    loads8(wt, rwo, 1152, lane, (256 * g + 64 * (w & 3)) * 1152 + k * 128);
#pragma unroll
    for (int s = 0; s < 8; ++s) vv[s] = ldnt(rv, vvo + s * 256, 0);
    __builtin_amdgcn_sched_barrier(0);
    make_lane(ln, lane, sh);
    ln.put += lbase / 4;
    TAG_NOW();
    LAS unsigned* qrd = L + L_Q + (lane & 1) * 64 + ((lane >> 1) & 1) * 16 + row * 4;
    LAS float* mwr = Lf + L_MW + ((lane >> 3) & 1) * 16;
    LAS float* owr = Lf + L_O + w * 256 + (row >> 1) * 128 + 8 * c4 + 4 * (row & 1);
    pin(qrd); pin(mwr); pin(owr);
    asm volatile("" ::"v"(tv0), "v"(tv1), "v"(tv2));
    if (w == 0) {
      const int oq = lane * 16, sq = WS_SQ + g * 1024;
      v4u q;
      for (unsigned spin = 0;; ++spin) {
        asm volatile("" ::: "memory");
        q = ldsc(ws, oq, sq) ^ T;
        if (allok(or4(q)) || spin > SPIN_MAX) break;
        __builtin_amdgcn_s_sleep(2);
      }
      *(LAS v2u*)(L + L_Q + lane * 2) = v2u{(q.x >> 16) | q.y, (q.z >> 16) | q.w};
    }
    bar();
    float sc = 0.f;
    {
      const v4u q0 = *(LAS v4u*)qrd, q1 = *(LAS v4u*)(qrd + 32);
#pragma unroll
      for (int t = 0; t < 4; ++t) {
        f32x4 d = {0.f, 0.f, 0.f, 0.f};
        d = mfma(q0, kk[2 * t], d);
        d = mfma(q1, kk[2 * t + 1], d);
        const float h0 = d.x + dpp<0x128>(d.z), h1 = d.w + dpp<0x128>(d.y);
        const float v = c4 < 8 ? h0 : h1;
        sc = row == t ? v : sc;
      }
      sc = rb(rb(sc) * 0.08838834764831845f);
      float m = sc;
      m = vmax(m, dpp<0xB1>(m)); m = vmax(m, dpp<0x4E>(m)); m = vmax(m, dpp<0x141>(m));
      m = xrow_max(m);
      if ((lane & 55) == 0) mwr[w] = m;
    }
    bar();
    {
      const float M = max12(mwr);
      const float pe = __expf(sc - M);
      v4u h0, h1;
      shuf(sh + lane, ln.shr, pe, h0, h1);
      float l = pe;
      l += dpp<0xB1>(l); l += dpp<0x4E>(l); l += dpp<0x141>(l);
      l = xrow_sum(l);
      if ((lane & 55) == 0) mwr[32 + w] = l;
      lds_arrive(L + L_CNT + 2);
      v4u A[8];
      A[0] = diag(h0.x, ln.k); A[1] = diag(h0.y, ln.k); A[2] = diag(h0.z, ln.k); A[3] = diag(h0.w, ln.k);
      A[4] = diag(h1.x, ln.k); A[5] = diag(h1.y, ln.k); A[6] = diag(h1.z, ln.k); A[7] = diag(h1.w, ln.k);
      __builtin_amdgcn_sched_barrier(0);
      f32x4 o0 = {0.f, 0.f, 0.f, 0.f}, o1 = {0.f, 0.f, 0.f, 0.f};
      o0 = mfma(A[0], vv[0], o0); o1 = mfma(A[1], vv[1], o1); o0 = mfma(A[2], vv[2], o0); o1 = mfma(A[3], vv[3], o1);
      o0 = mfma(A[4], vv[4], o0); o1 = mfma(A[5], vv[5], o1); o0 = mfma(A[6], vv[6], o0); o1 = mfma(A[7], vv[7], o1);
      *(LAS f32x4*)owr = o0 + o1;
      lds_arrive(L + L_CNT);
    }
    if (w >= 7) __builtin_amdgcn_s_setprio(3);
    if (w == 7) {
      lds_wait(L + L_CNT + 2, light ? 8u : 12u);
      LAS float* lw = Lf + L_LW + (lane & 1) * 16;
      LAS float* mw = Lf + L_MW + (lane & 1) * 16;
      const f32x4 a0 = *(LAS f32x4*)lw, a1 = *(LAS f32x4*)(lw + 4), a2 = *(LAS f32x4*)(lw + 8);
      const float l = ((a0.x + a0.y) + (a0.z + a0.w)) + ((a1.x + a1.y) + (a1.z + a1.w)) + ((a2.x + a2.y) + (a2.z + a2.w));
      const float M = max12(mw);
      const v4u ml = {tg(M, T.x), tg(l, T.y), tg(dpp<0x55>(M), T.z), tg(dpp<0x55>(l), T.w)};
      if (lane == 0) stsc4(ws, ml, 0, s3 + 1024);
    }
  }
  if (w >= 8) {
    __builtin_amdgcn_s_setprio(3);
    const int wp = w - 8;
    LAS float* o = Lf + L_O + row * 256 + wp * 64 + c4 * 4;
    int so = wp * 256 + c4 * 16;
    pin(o); pin(so);
    lds_wait(L + L_CNT, light ? 8u : 12u);
    f32x4 v = *(LAS f32x4*)o + *(LAS f32x4*)(o + 1024);
    if (!light) v += *(LAS f32x4*)(o + 2048);
    const f32x4 vs = {xrow_sum(v.x), xrow_sum(v.y), xrow_sum(v.z), xrow_sum(v.w)};
    if (lane < 16) stsc4(ws, tg4(vs, T), so, s3);
    if (!(light && g < 9 && w == 8)) return;
  }
  if (k < 9) {
    if (w >= 4) return;
    const int k2 = row + 8 > 10 ? 10 : row + 8;
    const int gb = WS_S3 + g * 11 * S3_STRIDE;
    const bool hd = w >= 2;
    int oa = row * S3_STRIDE + w * 256 + c4 * 16, oc = k2 * S3_STRIDE + w * 256 + c4 * 16;
    int ma_ = row * S3_STRIDE + 1024, mc_ = k2 * S3_STRIDE + 1024;
    pin(oa); pin(oc); pin(ma_); pin(mc_);
    v4u m0, m1, m2;
    v4u o0, o1, o2;
    for (unsigned spin = 0;; ++spin) {
      asm volatile("" ::: "memory");
      m0 = ldsc(ws, ma_, gb); m1 = ldsc(ws, ma_, gb + 4 * S3_STRIDE); m2 = ldsc(ws, mc_, gb);
      o0 = ldsc(ws, oa, gb); o1 = ldsc(ws, oa, gb + 4 * S3_STRIDE); o2 = ldsc(ws, oc, gb);
      __builtin_amdgcn_sched_barrier(0);
      m0 ^= T; m1 ^= T; m2 ^= T; o0 ^= T; o1 ^= T; o2 ^= T;
      if (allok(or4(m0) | or4(m1) | or4(m2) | or4(o0) | or4(o1) | or4(o2)) || spin > SPIN_MAX) break;
      __builtin_amdgcn_s_sleep(1);
    }
    const float ma = asf(hd ? m0.z : m0.x), mb = asf(hd ? m1.z : m1.x), mc = row == 3 ? -INFINITY : asf(hd ? m2.z : m2.x);
    const float MM = xrow_max(vmax(vmax(ma, mb), mc));
    float fa = __expf(ma - MM), fb = __expf(mb - MM), fc = __expf(mc - MM);
    const float inv = __builtin_amdgcn_rcpf(xrow_sum(fa * asf(hd ? m0.w : m0.y) + fb * asf(hd ? m1.w : m1.y) + fc * asf(hd ? m2.w : m2.y)));
    v4u h0, h1;
    shuf(ln.shw, ln.shr, tr4(asf4(o0) * fa + asf4(o1) * fb + asf4(o2) * fc) * inv, h0, h1);
    put_halves(ln, L_REDA + w * 128, gemv8(wt, h0, h1, ln.k));
    bar();
    if (w == 0) pub64(ws, sumN<8>(Lf + L_REDA + lane), T, lane, WS_S4 + (k * 16 + g) * 256);
    return;
  }
  if (!(light && g < 9) || w > 8) return;
  {
    const int j = g;
    float x1 = 0.f, gbv = 0.f;
    if (w == 0) {
      const float xv = bf(p.x[64 * j + col]);
      const unsigned Tl = row == 0 ? T.x : row == 1 ? T.y : row == 2 ? T.z : T.w;
      unsigned a2, b2;
      v4u q0, q1, q2, q3;
      for (unsigned spin = 0;; ++spin) {
        asm volatile("" ::: "memory");
        const int vo = (j * 16 + row) * 256 + c4 * 16;
        a2 = ldsc1(ws, col * 4, WS_SB + j * 256); b2 = ldsc1(ws, col * 4, WS_SB + (27 + j) * 256);
        q0 = ldsc(ws, vo, WS_S4); q1 = ldsc(ws, vo, WS_S4 + 1024); q2 = ldsc(ws, vo, WS_S4 + 2048); q3 = ldsc(ws, vo, WS_S4 + 3072);
        __builtin_amdgcn_sched_barrier(0);
        q0 ^= T; q1 ^= T; q2 ^= T; q3 ^= T; a2 ^= Tl; b2 ^= Tl;
        if (allok(a2 | b2 | or4(q0) | or4(q1) | or4(q2) | or4(q3)) || spin > SPIN_MAX) break;
        __builtin_amdgcn_s_sleep(8);
      }
      x1 = rb(xv + rb(asf(a2) * rb(tr4((asf4(q0) + asf4(q1)) + (asf4(q2) + asf4(q3))))));
      gbv = asf(b2);
      pin(x1); pin(gbv);
    }
    {
      v4u y0, y1;
      int vo = (8 * w + row) * 256 + c4 * 16;
      LAS float* rw = Lf + L_REDG + w * 64 + col;
      pin(vo); pin(rw);
      for (unsigned spin = 0;; ++spin) {
        asm volatile("" ::: "memory");
        y0 = ldsc(ws, vo, WS_S5 + j * 72 * 256); y1 = ldsc(ws, vo, WS_S5 + j * 72 * 256 + 1024);
        __builtin_amdgcn_sched_barrier(0);
        y0 ^= T; y1 ^= T;
        if (allok(or4(y0) | or4(y1)) || spin > SPIN_MAX) break;
        __builtin_amdgcn_s_sleep(1);
      }
      *rw = tr4(asf4(y0) + asf4(y1));
    }
    bar();
    if (w == 0) {
      LAS float* r = Lf + L_REDG + col;
      const float y = ((r[0] + r[64]) + (r[128] + r[192])) + ((r[256] + r[320]) + (r[384] + r[448])) + r[512];
      f32x2 o = {x1 + rb(gbv * rb(y)), 0.f};
      p.out[64 * j + col] = (u16)(__builtin_bit_cast(unsigned, __builtin_convertvector(o, bf16x2)) & 0xFFFFu);
      if (j == 0 && lane == 0) __builtin_amdgcn_raw_buffer_store_b32(E + 1u, ws, 0, WS_EPOCH, 16);
    }
  }
}

}

extern "C" void kernel_launch(void* const* d_in, const int* in_sizes, int n_in, void* d_out, int out_size, void* d_ws, size_t ws_size,
                              hipStream_t stream) {
  Params p;
  p.x = (const u16*)d_in[0]; p.kc = (const u16*)d_in[1]; p.vc = (const u16*)d_in[2]; p.wq = (const u16*)d_in[3]; p.wo = (const u16*)d_in[4];
  p.wg = (const u16*)d_in[6]; p.wu = (const u16*)d_in[7]; p.wd = (const u16*)d_in[8]; p.c = (const u16*)d_in[9]; p.wm = (const u16*)d_in[10];
  p.out = (u16*)d_out; p.ws = (unsigned char*)d_ws;
  fused_decode<<<256, NTHR, 0, stream>>>(p);
}
```

```cpp
#include <hip/hip_runtime.h>
#include <hip/hip_bf16.h>
#include <stdint.h>

namespace {

typedef unsigned short u16;
typedef unsigned v4u __attribute__((ext_vector_type(4)));
typedef unsigned v2u __attribute__((ext_vector_type(2)));
typedef float f32x4 __attribute__((ext_vector_type(4)));
typedef float f32x2 __attribute__((ext_vector_type(2)));
typedef __bf16 bf16x8 __attribute__((ext_vector_type(8)));
typedef __bf16 bf16x2 __attribute__((ext_vector_type(2)));
typedef __amdgpu_buffer_rsrc_t rsrc_t;
#define LAS __attribute__((address_space(3)))

constexpr int D = 576, SK = 65536, WIN = 4096, FF = 4608;
constexpr int NTHR = 768;
constexpr int WS_EPOCH = 0;
constexpr int WS_S1 = 4096;
constexpr int WS_SQ = WS_S1 + 72 * 256;
constexpr int WS_SB = WS_SQ + 64 * 256;
constexpr int WS_S4 = WS_SB + 36 * 256;
constexpr int WS_S5 = WS_S4 + 144 * 256;
constexpr int WS_S3 = WS_S5 + 648 * 256;
constexpr int S3_STRIDE = 1280;
constexpr int WS_END = WS_S3 + 176 * S3_STRIDE;
constexpr unsigned SPIN_MAX = 1u << 19;

struct Params {
  const u16 *x, *kc, *vc, *wq, *wo, *wg, *wu, *wd, *c, *wm;
  u16* out;
  unsigned char* ws;
};

constexpr int SH_STRIDE = 160;
constexpr int L_SH = 0;
constexpr int L_REDA = L_SH + 12 * SH_STRIDE;
constexpr int L_REDG = L_REDA + 1536;
constexpr int L_ST = L_REDG + 1152;
constexpr int L_Q = L_ST + 64;
constexpr int L_MW = L_Q + 128;
constexpr int L_LW = L_MW + 32;
constexpr int L_CNT = L_LW + 32;
constexpr int L_O = L_CNT + 16;
constexpr int L_END = L_O + 12 * 256;

__device__ __forceinline__ rsrc_t mk(const void* p, unsigned bytes) {
  return __builtin_amdgcn_make_buffer_rsrc((void*)p, 0, (int)bytes, 0x00020000);
}
__device__ __forceinline__ v4u ldnt(rsrc_t r, int v, int s) { return __builtin_amdgcn_raw_buffer_load_b128(r, v, s, 2); }
__device__ __forceinline__ v4u ldsc(rsrc_t r, int v, int s) { return __builtin_amdgcn_raw_buffer_load_b128(r, v, s, 16); }
__device__ __forceinline__ unsigned ldsc1(rsrc_t r, int v, int s) { return __builtin_amdgcn_raw_buffer_load_b32(r, v, s, 16); }
__device__ __forceinline__ void stsc4(rsrc_t r, v4u x, int v, int s) { __builtin_amdgcn_raw_buffer_store_b128(x, r, v, s, 16); }
__device__ __forceinline__ float asf(unsigned x) { return __uint_as_float(x); }
__device__ __forceinline__ unsigned asu(float x) { return __float_as_uint(x); }
template <class V> __device__ __forceinline__ void pin(V& x) { asm volatile("" : "+v"(x)); }
__device__ __forceinline__ unsigned rep(float x) {
  f32x2 v = {x, x};
  return __builtin_bit_cast(unsigned, __builtin_convertvector(v, bf16x2));
}
__device__ __forceinline__ float rb(float x) { return asf(rep(x) & 0xFFFF0000u); }
__device__ __forceinline__ float bf(u16 x) { return asf((unsigned)x << 16); }
__device__ __forceinline__ unsigned tg(float x, unsigned T) { return (asu(x) & 0xFFFFFF00u) | T; }
__device__ __forceinline__ unsigned tgr(float x, unsigned T) { return ((asu(x) + 0x80u) & 0xFFFFFF00u) | T; }
__device__ __forceinline__ v4u tg4(f32x4 y, v4u T) { return v4u{tg(y.x, T.x), tg(y.y, T.y), tg(y.z, T.z), tg(y.w, T.w)}; }
__device__ __forceinline__ v4u make_tag(unsigned e, unsigned xw, unsigned cw) {
  const unsigned t = (e + 1u) * 0x01010101u + (xw ^ (cw * 0x9E3779B1u)) * 0x85EBCA6Bu;
  return v4u{t & 0xFFu, (t >> 8) & 0xFFu, (t >> 16) & 0xFFu, t >> 24};
}
template <int C> __device__ __forceinline__ float dpp(float x);
__device__ __forceinline__ void pub64(rsrc_t ws, float v, v4u T, int lane, int soff);
__device__ __forceinline__ unsigned or4(v4u d) { return (d.x | d.y) | (d.z | d.w); }
__device__ __forceinline__ bool allok(unsigned bad) { return __all((bad & 0xFFu) == 0u); }
template <int C> __device__ __forceinline__ float dpp(float x) {
  return asf((unsigned)__builtin_amdgcn_update_dpp(0, (int)asu(x), C, 0xF, 0xF, true));
}
__device__ __forceinline__ void pub64(rsrc_t ws, float v, v4u T, int lane, int soff) {
  const v4u d = {tg(v, T.x), tg(dpp<0x55>(v), T.y), tg(dpp<0xAA>(v), T.z), tg(dpp<0xFF>(v), T.w)};
  if ((lane & 3) == 0) __builtin_amdgcn_raw_buffer_store_b128(d, ws, lane * 4, soff, 16);
}
__device__ __forceinline__ float xrow_sum(float x) {
  auto s = __builtin_amdgcn_permlane16_swap(asu(x), asu(x), false, false);
  x = asf(s[0]) + asf(s[1]);
  auto t = __builtin_amdgcn_permlane32_swap(asu(x), asu(x), false, false);
  return asf(t[0]) + asf(t[1]);
}
__device__ __forceinline__ float xrow_max(float x) {
  auto s = __builtin_amdgcn_permlane16_swap(asu(x), asu(x), false, false);
  x = __builtin_amdgcn_fmed3f(asf(s[0]), asf(s[1]), INFINITY);
  auto t = __builtin_amdgcn_permlane32_swap(asu(x), asu(x), false, false);
  return __builtin_amdgcn_fmed3f(asf(t[0]), asf(t[1]), INFINITY);
}
__device__ __forceinline__ float tr4(f32x4 v) {
  auto a = __builtin_amdgcn_permlane16_swap(asu(v.x), asu(v.y), false, false);
  auto b = __builtin_amdgcn_permlane16_swap(asu(v.z), asu(v.w), false, false);
  const float s01 = asf(a[0]) + asf(a[1]), s23 = asf(b[0]) + asf(b[1]);
  auto c = __builtin_amdgcn_permlane32_swap(asu(s01), asu(s23), false, false);
  return asf(c[0]) + asf(c[1]);
}
__device__ __forceinline__ float wave_sum2(float x, float y) {
  x += dpp<0xB1>(x); y += dpp<0xB1>(y);
  x += dpp<0x4E>(x); y += dpp<0x4E>(y);
  x += dpp<0x141>(x); y += dpp<0x141>(y);
  x += dpp<0x140>(x); y += dpp<0x140>(y);
  auto a = __builtin_amdgcn_permlane16_swap(asu(x), asu(y), false, false);
  const float s = asf(a[0]) + asf(a[1]);
  auto c = __builtin_amdgcn_permlane32_swap(asu(s), asu(s), false, false);
  return asf(c[0]) + asf(c[1]);
}
__device__ __forceinline__ f32x4 asf4(v4u d) { return __builtin_bit_cast(f32x4, d); }
__device__ __forceinline__ f32x4 mfma(v4u a, v4u b, f32x4 c) {
  return __builtin_amdgcn_mfma_f32_16x16x32_bf16(__builtin_bit_cast(bf16x8, a), __builtin_bit_cast(bf16x8, b), c, 0, 0, 0);
}
struct Mask { unsigned m0, m1, m2, m3; };
__device__ __forceinline__ v4u diag(unsigned hh, const Mask& k) { return v4u{hh & k.m0, hh & k.m1, hh & k.m2, hh & k.m3}; }
__device__ __forceinline__ void bar() {
  asm volatile("s_waitcnt lgkmcnt(0)" ::: "memory");
  __builtin_amdgcn_s_barrier();
  asm volatile("" ::: "memory");
}
__device__ __forceinline__ void rawbar() {
  __builtin_amdgcn_sched_barrier(0);
  __builtin_amdgcn_s_barrier();
  __builtin_amdgcn_sched_barrier(0);
}
__device__ __forceinline__ float silu(float v) { return v * __builtin_amdgcn_rcpf(1.f + __expf(-v)); }
__device__ __forceinline__ float fold1(float x) {
  auto t = __builtin_amdgcn_permlane32_swap(asu(x), asu(x), false, false);
  return asf(t[0]) + dpp<0x128>(asf(t[1]));
}
__device__ __forceinline__ f32x4 fold(f32x4 a) { return f32x4{fold1(a.x), fold1(a.y), fold1(a.z), fold1(a.w)}; }
__device__ __forceinline__ float fold2_1(float a, float b) {
  auto t = __builtin_amdgcn_permlane32_swap(asu(a), asu(b), false, false);
  return asf(t[0]) + dpp<0x128>(asf(t[1]));
}


struct Lane {
  Mask k;
  LAS unsigned* shw;
  LAS unsigned* shr;
  LAS float* put;
  bool putv;
};
__device__ __forceinline__ void shuf(LAS unsigned* wr, LAS unsigned* rd, float v, v4u& h0, v4u& h1) {
  *wr = rep(v);
  asm volatile("" ::: "memory");
  h0 = *(LAS v4u*)rd;
  h1 = *(LAS v4u*)(rd + 4);
}
__device__ __forceinline__ f32x4 gemv8(const v4u (&w)[8], v4u h0, v4u h1, const Mask& k) {
  f32x4 a = {0.f, 0.f, 0.f, 0.f}, b = {0.f, 0.f, 0.f, 0.f};
  const v4u a0 = diag(h0.x, k), a1 = diag(h0.y, k), a2 = diag(h0.z, k), a3 = diag(h0.w, k);
  a = mfma(a0, w[0], a); b = mfma(a1, w[1], b);
  const v4u a4 = diag(h1.x, k), a5 = diag(h1.y, k);
  a = mfma(a2, w[2], a); b = mfma(a3, w[3], b);
  const v4u a6 = diag(h1.z, k), a7 = diag(h1.w, k);
  a = mfma(a4, w[4], a); b = mfma(a5, w[5], b);
  a = mfma(a6, w[6], a); b = mfma(a7, w[7], b);
  return a + b;
}
__device__ __forceinline__ f32x2 ln_sums(LAS float* st) {
  const f32x4 a = *(LAS f32x4*)(st), b = *(LAS f32x4*)(st + 4), c = *(LAS f32x4*)(st + 8), d = *(LAS f32x4*)(st + 12);
  const f32x2 e = *(LAS f32x2*)(st + 16);
  const f32x4 t = (a + b) + (c + d);
  return f32x2{t.x + t.z + e.x, t.y + t.w + e.y};
}
template <int N> __device__ __forceinline__ float sumN(LAS float* red) {
  float a = 0.f, b = 0.f;
#pragma unroll
  for (int i = 0; i < N; i += 2) { a += red[i * 64]; b += red[i * 64 + 64]; }
  return a + b;
}
__device__ __forceinline__ void loads8(v4u (&w)[8], rsrc_t r, int stride, int lane, int base) {
  const int vo = (lane >> 3) * stride + (lane & 7) * 16;
#pragma unroll
  for (int s = 0; s < 8; ++s) w[s] = ldnt(r, vo, base + 8 * s * stride);
}
struct Tile18 { v4u w[18]; v2u c; };
__device__ __forceinline__ void tile18_load(Tile18& t, const u16* c, rsrc_t rwm, int ks, int cb, int lane) {
  t.c = *(const v2u*)(c + 144 * ks + 4 * (lane < 36 ? lane : 35));
  const int vo = (lane >> 3) * 6912 + (lane & 7) * 16;
#pragma unroll
  for (int s = 0; s < 18; ++s) t.w[s] = ldnt(rwm, vo, (144 * ks + 8 * s) * 6912 + cb);
}
__device__ __forceinline__ f32x4 tile18_mac(const Tile18& t, LAS unsigned* sh, int lane, const Mask& k) {
  if (lane < 36) {
    LAS unsigned* d = sh + (4 * (lane & 1)) * 20 + (lane >> 1);
    d[0] = rep(silu(asf(t.c.x << 16))); d[20] = rep(silu(asf(t.c.x & 0xFFFF0000u)));
    d[40] = rep(silu(asf(t.c.y << 16))); d[60] = rep(silu(asf(t.c.y & 0xFFFF0000u)));
  }
  asm volatile("" ::: "memory");
  LAS unsigned* s = sh + (lane >> 3) * 20;
  const v4u h0 = *(LAS v4u*)s, h1 = *(LAS v4u*)(s + 4), h2 = *(LAS v4u*)(s + 8), h3 = *(LAS v4u*)(s + 12);
  const v2u h4 = *(LAS v2u*)(s + 16);
  f32x4 a = {0.f, 0.f, 0.f, 0.f}, b = {0.f, 0.f, 0.f, 0.f};
  a = mfma(diag(h0.x, k), t.w[0], a); b = mfma(diag(h0.y, k), t.w[1], b); a = mfma(diag(h0.z, k), t.w[2], a); b = mfma(diag(h0.w, k), t.w[3], b);
  a = mfma(diag(h1.x, k), t.w[4], a); b = mfma(diag(h1.y, k), t.w[5], b); a = mfma(diag(h1.z, k), t.w[6], a); b = mfma(diag(h1.w, k), t.w[7], b);
  a = mfma(diag(h2.x, k), t.w[8], a); b = mfma(diag(h2.y, k), t.w[9], b); a = mfma(diag(h2.z, k), t.w[10], a); b = mfma(diag(h2.w, k), t.w[11], b);
  a = mfma(diag(h3.x, k), t.w[12], a); b = mfma(diag(h3.y, k), t.w[13], b); a = mfma(diag(h3.z, k), t.w[14], a); b = mfma(diag(h3.w, k), t.w[15], b);
  a = mfma(diag(h4.x, k), t.w[16], a); b = mfma(diag(h4.y, k), t.w[17], b);
  return a + b;
}
__device__ __forceinline__ void lds_arrive(LAS unsigned* c) {
  if ((threadIdx.x & 63) == 0) asm volatile("ds_add_u32 %0, %1" ::"v"(c), "v"(1u) : "memory");
  asm volatile("" ::: "memory");
}
__device__ __forceinline__ float vmax(float a, float b) { return __builtin_amdgcn_fmed3f(a, b, INFINITY); }
__device__ __forceinline__ float max12(LAS float* m) {
  const f32x4 b0 = *(LAS f32x4*)m, b1 = *(LAS f32x4*)(m + 4), b2 = *(LAS f32x4*)(m + 8);
  return vmax(vmax(vmax(vmax(b0.x, b0.y), vmax(b0.z, b0.w)), vmax(vmax(b1.x, b1.y), vmax(b1.z, b1.w))), vmax(vmax(b2.x, b2.y), vmax(b2.z, b2.w)));
}
__device__ __forceinline__ unsigned lds_inc(LAS unsigned* c) {
  asm volatile("" ::: "memory");
  unsigned old = 0;
  if ((threadIdx.x & 63) == 0) old = __hip_atomic_fetch_add(c, 1u, __ATOMIC_RELAXED, __HIP_MEMORY_SCOPE_WORKGROUP);
  old = (unsigned)__builtin_amdgcn_readfirstlane((int)old);
  asm volatile("" ::: "memory");
  return old;
}
__device__ __forceinline__ void lds_wait(LAS unsigned* c, unsigned need) {
  for (unsigned spin = 0; *(volatile LAS unsigned*)c < need && spin < (1u << 28); ++spin) {}
  asm volatile("" ::: "memory");
}
__device__ __forceinline__ void make_lane(Lane& ln, int lane, LAS unsigned* sh) {
  const unsigned mm = (lane & 1) ? 0xFFFF0000u : 0x0000FFFFu;
  const int d = (lane & 7) >> 1, c = lane & 15, g = lane >> 4;
  ln.k.m0 = d == 0 ? mm : 0u; ln.k.m1 = d == 1 ? mm : 0u; ln.k.m2 = d == 2 ? mm : 0u; ln.k.m3 = d == 3 ? mm : 0u;
  ln.shw = sh + 32 * (c & 1) + 8 * g + (c >> 1);
  ln.shr = sh + (lane >> 3) * 8;
  ln.put = (LAS float*)0 + (c >> 3) * 64 + 8 * (c & 7) + 4 * (g & 1);
  ln.putv = (c >> 3) == (g >> 1);
  pin(ln.k.m0); pin(ln.k.m1); pin(ln.k.m2); pin(ln.k.m3); pin(ln.shw); pin(ln.shr); pin(ln.put);
}
__device__ __forceinline__ void put_halves(const Lane& ln, int base_dw, f32x4 a) {
  if (ln.putv) *(LAS f32x4*)(ln.put + base_dw) = a;
}
__device__ __forceinline__ void modb_finish(const Tile18& t, LAS unsigned* sh, const Lane& ln, LAS float* Lf, int red_dw, LAS unsigned* cnt, int ks, int lane,
                                            rsrc_t ws, int tile, v4u T) {
  put_halves(ln, red_dw + ks * 128, tile18_mac(t, sh, lane, ln.k));
  if (lds_inc(cnt) == 3u) pub64(ws, rb(sumN<8>(Lf + red_dw + lane)), T, lane, WS_SB + tile * 256);
}

__global__ void __launch_bounds__(NTHR, 3) fused_decode(Params p) {
  __shared__ __attribute__((aligned(16))) unsigned smem[L_END];
  LAS unsigned* L = (LAS unsigned*)smem;
  LAS float* Lf = (LAS float*)smem;
  const int b = blockIdx.x, xcd = b & 7, slot = b >> 3;
  const int lane = threadIdx.x & 63, w = __builtin_amdgcn_readfirstlane(threadIdx.x >> 6);
  const int c4 = lane & 15, row = lane >> 4;
  const int col = 4 * c4 + row;
#define LOAD_EPOCH() __builtin_amdgcn_sched_barrier(0); const unsigned Ev = __builtin_amdgcn_raw_buffer_load_b32(ws, 0, WS_EPOCH, 16); \
  const unsigned Xv = __builtin_amdgcn_raw_buffer_load_b32(mk(p.x, 4), 0, 0, 0), Cv = __builtin_amdgcn_raw_buffer_load_b32(mk(p.c, 4), 0, 0, 0); __builtin_amdgcn_sched_barrier(0)
#define TAG_NOW() (E = (unsigned)__builtin_amdgcn_readfirstlane((int)Ev), T = make_tag(E, (unsigned)__builtin_amdgcn_readfirstlane((int)Xv), (unsigned)__builtin_amdgcn_readfirstlane((int)Cv)))
#define GET_TAG() unsigned E; v4u T; TAG_NOW(); (void)E
  LAS unsigned* sh = L + L_SH + w * SH_STRIDE;
  const int lbase = (int)(size_t)(LAS void*)smem;
  Lane ln;

  if (slot <= 9) {
  const int rgate = (int)((size_t)p.wg >> 7), xstar = (3 - rgate) & 7;
  const bool qonly = slot == 9 && xcd == xstar;
  if (slot < 9 || qonly) {
    if (w >= 10) return;
    const int f = xcd * 9 + slot;
    __builtin_amdgcn_sched_barrier(0);
    const rsrc_t ws = mk(p.ws, WS_END), rwm = mk(p.wm, D * 6 * D * 2);
    if (w == 9) {
      __builtin_amdgcn_s_setprio(3);
      if (lane == 0) L[L_CNT] = 0u;
      if (qonly) { bar(); return; }
      Tile18 t;
      tile18_load(t, p.c, rwm, f / 18, (f % 18) * 128, lane);
      LOAD_EPOCH();
      bar();
      make_lane(ln, lane, sh);
      GET_TAG();
      const f32x4 a = fold(tile18_mac(t, sh, lane, ln.k));
      if (c4 < 8 && row < 2) stsc4(ws, v4u{tgr(a.x, T.x), tgr(a.y, T.y), tgr(a.z, T.z), tgr(a.w, T.w)}, (8 * c4 + 4 * row) * 4, WS_S1 + f * 256);
      return;
    }
    const int j = w;
    const int s0 = (3 - rgate - xcd) & 7;
    const bool hasq = qonly || !(slot == s0 || (s0 == 0 && slot == 8));
    const int n = qonly ? xcd * 8 + 7 : xcd * 8 + slot - (slot > s0 ? 1 : 0);
    const u16 xr = p.x[64 * j + col], x0r = p.x[0];
    LOAD_EPOCH();
    rawbar();
    v4u wt[8], wg[8], wu[8], wd[8];
    const rsrc_t rg = mk(p.wg, D * FF * 2), ru = mk(p.wu, D * FF * 2), rd = mk(p.wd, FF * D * 2);
    loads8(wt, mk(p.wq, hasq ? D * 4096 * 2 : 0), 8192, lane, 64 * j * 8192 + n * 128);
    unsigned tg0 = __builtin_amdgcn_raw_buffer_load_b32(rg, 0, 64 * j * FF * 2 + f * 128, 2);
    unsigned tg1 = __builtin_amdgcn_raw_buffer_load_b32(ru, 0, 64 * j * FF * 2 + f * 128, 2);
    unsigned tg2 = __builtin_amdgcn_raw_buffer_load_b32(rd, 0, 64 * f * 1152 + j * 128, 2);
    __builtin_amdgcn_sched_barrier(0);
    make_lane(ln, lane, sh);
    ln.put += lbase / 4;
    GET_TAG();
    const float x0 = bf(x0r);
    float xv = bf(xr);
    {
      const float d = xv - x0;
      const float s = wave_sum2(d, d * d);
      if ((lane & 47) == 0) Lf[L_ST + 2 * j + (lane >> 4)] = s;
    }
    lds_arrive(L + L_CNT);
    lds_wait(L + L_CNT, 9u);
    float mean1, lnv;
    {
      const f32x2 s = ln_sums(Lf + L_ST);
      const float dm = s.x * (1.f / D);
      mean1 = x0 + dm;
      const float var = fmaxf(s.y * (1.f / D) - dm * dm, 0.f);
      const float den = rb(__builtin_amdgcn_sqrtf(rb(rb(var) + 1e-5f)));
      lnv = rb(rb(xv - rb(mean1)) / den);
      pin(lnv); pin(mean1); pin(xv);
    }
    if (hasq) {
      int o1 = (row * 18 + j) * 256 + c4 * 16;
      pin(o1);
      __builtin_amdgcn_s_waitcnt(0x0F70 | 5);
      v4u sa, ca;
      for (unsigned spin = 0;; ++spin) {
        asm volatile("" ::: "memory");
        sa = ldsc(ws, o1, WS_S1); ca = ldsc(ws, o1, WS_S1 + 9 * 256);
        __builtin_amdgcn_sched_barrier(0);
        sa ^= T; ca ^= T;
        if (allok(or4(sa) | or4(ca)) || spin > SPIN_MAX) break;
        __builtin_amdgcn_s_sleep(2);
      }
      const float h = rb(lnv * rb(1.f + rb(tr4(asf4(ca))))) + rb(tr4(asf4(sa)));
      v4u h0, h1;
      shuf(ln.shw, ln.shr, h, h0, h1);
      put_halves(ln, L_REDA + j * 128, gemv8(wt, h0, h1, ln.k));
      bar();
      if (j == 0) pub64(ws, rb(sumN<18>(Lf + L_REDA + lane)), T, lane, WS_SQ + n * 256);
      rawbar();
    }
    asm volatile("" ::"v"(tg0), "v"(tg1), "v"(tg2));
    if (qonly) return;
    loads8(wg, rg, FF * 2, lane, 64 * j * FF * 2 + f * 128);
    loads8(wu, ru, FF * 2, lane, 64 * j * FF * 2 + f * 128);
    loads8(wd, rd, 1152, lane, 64 * f * 1152 + j * 128);
    __builtin_amdgcn_sched_barrier(0);
    int o4 = (j * 16 + row) * 256 + c4 * 16, ob = j * 256 + col * 4, o5 = (8 * c4 + 4 * row) * 4;
    LAS float* redg_w = Lf + L_REDG + j * 128 + (row >> 1) * 64 + 8 * c4 + 4 * (row & 1);
    LAS float* redg_r = Lf + L_REDG + lane;
    LAS unsigned* shw2 = L + L_Q + (lane & 7) * 8 + (lane >> 3);
    LAS unsigned* shr2 = L + L_Q + (lane >> 3) * 8;
    const unsigned Tl = row == 0 ? T.x : row == 1 ? T.y : row == 2 ? T.z : T.w;
    pin(o4); pin(ob); pin(o5); pin(redg_w); pin(redg_r); pin(shw2); pin(shr2);
    asm volatile("s_waitcnt vmcnt(0)" ::: "memory");
    rawbar();
    float x1, sb, cb;
    {
      unsigned a, s2, c2;
      v4u q0, q1, q2, q3;
      q0 = ldsc(ws, o4, WS_S4); q1 = ldsc(ws, o4, WS_S4 + 1024); q2 = ldsc(ws, o4, WS_S4 + 2048); q3 = ldsc(ws, o4, WS_S4 + 3072);
      a = ldsc1(ws, ob, WS_SB); s2 = ldsc1(ws, ob, WS_SB + 9 * 256); c2 = ldsc1(ws, ob, WS_SB + 18 * 256);
      for (unsigned spin = 0; spin <= SPIN_MAX; ++spin) {
        asm volatile("" ::: "memory");
        bool again = false;
        if (!allok(or4(q0 ^ T))) { q0 = ldsc(ws, o4, WS_S4); again = true; }
        if (!allok(or4(q1 ^ T))) { q1 = ldsc(ws, o4, WS_S4 + 1024); again = true; }
        if (!allok(or4(q2 ^ T))) { q2 = ldsc(ws, o4, WS_S4 + 2048); again = true; }
        if (!allok(or4(q3 ^ T))) { q3 = ldsc(ws, o4, WS_S4 + 3072); again = true; }
        if (!allok((a ^ Tl) | (s2 ^ Tl) | (c2 ^ Tl))) {
          a = ldsc1(ws, ob, WS_SB); s2 = ldsc1(ws, ob, WS_SB + 9 * 256); c2 = ldsc1(ws, ob, WS_SB + 18 * 256);
          again = true;
        }
        if (!again) break;
      }
      q0 ^= T; q1 ^= T; q2 ^= T; q3 ^= T; a ^= Tl; s2 ^= Tl; c2 ^= Tl;
      x1 = rb(xv + rb(asf(a) * rb(tr4((asf4(q0) + asf4(q1)) + (asf4(q2) + asf4(q3))))));
      sb = asf(s2); cb = rb(1.f + asf(c2));
    }
    {
      const float d = x1 - mean1;
      const float s = wave_sum2(d, d * d);
      if ((lane & 47) == 0) Lf[L_ST + 32 + 2 * j + (lane >> 4)] = s;
    }
    bar();
    {
      const f32x2 s = ln_sums(Lf + L_ST + 32);
      const float dm = s.x * (1.f / D);
      const float var = fmaxf(s.y * (1.f / D) - dm * dm, 0.f);
      const float den = rb(__builtin_amdgcn_sqrtf(rb(rb(var) + 1e-5f)));
      const float h2 = rb(rb(rb(x1 - rb(mean1 + dm)) * __builtin_amdgcn_rcpf(den)) * cb) + sb;
      v4u h0, h1;
      shuf(ln.shw, ln.shr, h2, h0, h1);
      const f32x4 g = gemv8(wg, h0, h1, ln.k), u = gemv8(wu, h0, h1, ln.k);
      const f32x4 t = {fold2_1(g.x, u.x), fold2_1(g.y, u.y), fold2_1(g.z, u.z), fold2_1(g.w, u.w)};
      if (c4 < 8) *(LAS f32x4*)redg_w = t;
    }
    bar();
    if (j == 1) {
      float g = 0.f, u = 0.f, g2 = 0.f, u2 = 0.f;
#pragma unroll
      for (int i = 0; i < 8; i += 2) {
        g += redg_r[i * 128]; u += redg_r[i * 128 + 64];
        g2 += redg_r[i * 128 + 128]; u2 += redg_r[i * 128 + 192];
      }
      g = rb((g + redg_r[1024]) + g2); u = rb((u + redg_r[1088]) + u2);
      const float z = 1.5957691216057308f * (g + 0.044715f * g * g * g);
      *shw2 = rep(rb(g * __builtin_amdgcn_rcpf(1.f + __expf(-z))) * u);
    }
    bar();
    {
      const v4u h0 = *(LAS v4u*)shr2, h1 = *(LAS v4u*)(shr2 + 4);
      const f32x4 y = fold(gemv8(wd, h0, h1, ln.k));
      if (c4 < 8 && row < 2) stsc4(ws, tg4(y, T), o5, WS_S5 + (j * 72 + f) * 256);
    }
    return;
  }

  {
    int tile = 16 + (w >> 2) * 8 + xcd;
    if (w >= 8 && xcd >= 4) {
      const int r = xcd - 4 - ((xstar >= 4 && xcd > xstar) ? 1 : 0);
      if (r >= (xstar < 4 ? 3 : 2)) return;
      tile = 16 + r * 8 + xstar;
    }
    if (threadIdx.x < 3) L[L_CNT + threadIdx.x] = 0u;
    const rsrc_t ws = mk(p.ws, WS_END), rwm = mk(p.wm, D * 6 * D * 2);
    Tile18 t;
    tile18_load(t, p.c, rwm, w & 3, 2304 + tile * 128, lane);
    LOAD_EPOCH();
    __builtin_amdgcn_sched_barrier(0);
    bar();
    make_lane(ln, lane, sh);
    GET_TAG();
    modb_finish(t, sh, ln, Lf, L_REDA + (w >> 2) * 512, L + L_CNT + (w >> 2), w & 3, lane, ws, tile, T);
    return;
  }

  }
  const int g1 = (slot + 11) >> 5, g = 2 * xcd + g1, k = slot - 10 - 11 * g1;
  const bool light = k == 10;
  const int s3 = WS_S3 + (g * 11 + k) * S3_STRIDE;
  __builtin_amdgcn_sched_barrier(0);
  const rsrc_t ws = mk(p.ws, WS_END), rwm = mk(p.wm, D * 6 * D * 2);
  unsigned E = 0;
  v4u T = {0u, 0u, 0u, 0u};
  v4u wt[8];
  const rsrc_t rwo = mk(p.wo, (k < 9 && w < 4) ? 4096 * D * 2 : 0);
  if (light && w >= 8) {
    Tile18 t;
    tile18_load(t, p.c, rwm, w - 8, 2304 + g * 128, lane);
    LOAD_EPOCH();
    if (w == 8 && lane < 32) Lf[L_MW + lane] = -INFINITY;
    if (w == 9 && lane < 32) Lf[L_LW + lane] = 0.f;
    if (w == 10 && lane < 3) L[L_CNT + lane] = 0u;
    bar();
    make_lane(ln, lane, sh);
    TAG_NOW();
    modb_finish(t, sh, ln, Lf, L_REDA, L + L_CNT + 1, w - 8, lane, ws, g, T);
    rawbar();
    rawbar();
  } else {
    const size_t kvoff = ((size_t)g * SK + (SK - WIN) + k * 384 + w * 32) * 128;
    const rsrc_t rk = mk(p.kc + kvoff, 8192), rv = mk(p.vc + kvoff, 8192);
    v4u kk[8], vv[8];
    const int kvo = (lane & 7) * 256 + ((lane >> 3) & 1) * 64 + row * 16, vvo = row * 2048 + c4 * 16;
#pragma unroll
    for (int t = 0; t < 4; ++t) {
      kk[2 * t] = ldnt(rk, kvo, t * 2048);
      kk[2 * t + 1] = ldnt(rk, kvo + 128, t * 2048);
    }
    const unsigned tv0 = __builtin_amdgcn_raw_buffer_load_b32(rv, 0, 0, 2);
    const unsigned tv1 = __builtin_amdgcn_raw_buffer_load_b32(mk(p.out, (light && w == 0) ? D * 2 : 0), 0, 0, 0);
    const unsigned tv2 = __builtin_amdgcn_raw_buffer_load_b32(rwo, 0, (256 * g + 64 * (w & 3)) * 1152 + k * 128, 2);
    LOAD_EPOCH();
    if (!light) {
      if (w == 8 && lane < 32) Lf[L_MW + lane] = -INFINITY;
      if (w == 9 && lane < 32) Lf[L_LW + lane] = 0.f;
      if (w == 10 && lane < 3) L[L_CNT + lane] = 0u;
    }
    __builtin_amdgcn_sched_barrier(0);
    bar();
    loads8(wt, rwo, 1152, lane, (256 * g + 64 * (w & 3)) * 1152 + k * 128);
#pragma unroll
    for (int s = 0; s < 8; ++s) vv[s] = ldnt(rv, vvo + s * 256, 0);
    __builtin_amdgcn_sched_barrier(0);
    make_lane(ln, lane, sh);
    ln.put += lbase / 4;
    TAG_NOW();
    LAS unsigned* qrd = L + L_Q + (lane & 1) * 64 + ((lane >> 1) & 1) * 16 + row * 4;
    LAS float* mwr = Lf + L_MW + ((lane >> 3) & 1) * 16;
    LAS float* owr = Lf + L_O + w * 256 + (row >> 1) * 128 + 8 * c4 + 4 * (row & 1);
    pin(qrd); pin(mwr); pin(owr);
    asm volatile("" ::"v"(tv0), "v"(tv1), "v"(tv2));
    if (w == 0) {
      const int oq = lane * 16, sq = WS_SQ + g * 1024;
      v4u q;
      for (unsigned spin = 0;; ++spin) {
        asm volatile("" ::: "memory");
        q = ldsc(ws, oq, sq) ^ T;
        if (allok(or4(q)) || spin > SPIN_MAX) break;
        __builtin_amdgcn_s_sleep(2);
      }
      *(LAS v2u*)(L + L_Q + lane * 2) = v2u{(q.x >> 16) | q.y, (q.z >> 16) | q.w};
    }
    bar();
    float sc = 0.f;
    {
      const v4u q0 = *(LAS v4u*)qrd, q1 = *(LAS v4u*)(qrd + 32);
#pragma unroll
      for (int t = 0; t < 4; ++t) {
        f32x4 d = {0.f, 0.f, 0.f, 0.f};
        d = mfma(q0, kk[2 * t], d);
        d = mfma(q1, kk[2 * t + 1], d);
        const float h0 = d.x + dpp<0x128>(d.z), h1 = d.w + dpp<0x128>(d.y);
        const float v = c4 < 8 ? h0 : h1;
        sc = row == t ? v : sc;
      }
      sc = rb(rb(sc) * 0.08838834764831845f);
      float m = sc;
      m = vmax(m, dpp<0xB1>(m)); m = vmax(m, dpp<0x4E>(m)); m = vmax(m, dpp<0x141>(m));
      m = xrow_max(m);
      if ((lane & 55) == 0) mwr[w] = m;
    }
    bar();
    {
      const float M = max12(mwr);
      const float pe = __expf(sc - M);
      v4u h0, h1;
      shuf(sh + lane, ln.shr, pe, h0, h1);
      float l = pe;
      l += dpp<0xB1>(l); l += dpp<0x4E>(l); l += dpp<0x141>(l);
      l = xrow_sum(l);
      if ((lane & 55) == 0) mwr[32 + w] = l;
      lds_arrive(L + L_CNT + 2);
      v4u A[8];
      A[0] = diag(h0.x, ln.k); A[1] = diag(h0.y, ln.k); A[2] = diag(h0.z, ln.k); A[3] = diag(h0.w, ln.k);
      A[4] = diag(h1.x, ln.k); A[5] = diag(h1.y, ln.k); A[6] = diag(h1.z, ln.k); A[7] = diag(h1.w, ln.k);
      __builtin_amdgcn_sched_barrier(0);
      f32x4 o0 = {0.f, 0.f, 0.f, 0.f}, o1 = {0.f, 0.f, 0.f, 0.f};
      o0 = mfma(A[0], vv[0], o0); o1 = mfma(A[1], vv[1], o1); o0 = mfma(A[2], vv[2], o0); o1 = mfma(A[3], vv[3], o1);
      o0 = mfma(A[4], vv[4], o0); o1 = mfma(A[5], vv[5], o1); o0 = mfma(A[6], vv[6], o0); o1 = mfma(A[7], vv[7], o1);
      *(LAS f32x4*)owr = o0 + o1;
      lds_arrive(L + L_CNT);
    }
    if (w >= 7) __builtin_amdgcn_s_setprio(3);
    if (w == 7) {
      lds_wait(L + L_CNT + 2, light ? 8u : 12u);
      LAS float* lw = Lf + L_LW + (lane & 1) * 16;
      LAS float* mw = Lf + L_MW + (lane & 1) * 16;
      const f32x4 a0 = *(LAS f32x4*)lw, a1 = *(LAS f32x4*)(lw + 4), a2 = *(LAS f32x4*)(lw + 8);
      const float l = ((a0.x + a0.y) + (a0.z + a0.w)) + ((a1.x + a1.y) + (a1.z + a1.w)) + ((a2.x + a2.y) + (a2.z + a2.w));
      const float M = max12(mw);
      const v4u ml = {tg(M, T.x), tg(l, T.y), tg(dpp<0x55>(M), T.z), tg(dpp<0x55>(l), T.w)};
      if (lane == 0) stsc4(ws, ml, 0, s3 + 1024);
    }
  }
  if (w >= 8) {
    __builtin_amdgcn_s_setprio(3);
    const int wp = w - 8;
    LAS float* o = Lf + L_O + row * 256 + wp * 64 + c4 * 4;
    int so = wp * 256 + c4 * 16;
    pin(o); pin(so);
    lds_wait(L + L_CNT, light ? 8u : 12u);
    f32x4 v = *(LAS f32x4*)o + *(LAS f32x4*)(o + 1024);
    if (!light) v += *(LAS f32x4*)(o + 2048);
    const f32x4 vs = {xrow_sum(v.x), xrow_sum(v.y), xrow_sum(v.z), xrow_sum(v.w)};
    if (lane < 16) stsc4(ws, tg4(vs, T), so, s3);
    if (!(light && g < 9 && w == 8)) return;
  }
  if (k < 9) {
    if (w >= 4) return;
    const int k2 = row + 8 > 10 ? 10 : row + 8;
    const int gb = WS_S3 + g * 11 * S3_STRIDE;
    const bool hd = w >= 2;
    int oa = row * S3_STRIDE + w * 256 + c4 * 16, oc = k2 * S3_STRIDE + w * 256 + c4 * 16;
    int ma_ = row * S3_STRIDE + 1024, mc_ = k2 * S3_STRIDE + 1024;
    pin(oa); pin(oc); pin(ma_); pin(mc_);
    v4u m0, m1, m2;
    v4u o0, o1, o2;
    for (unsigned spin = 0;; ++spin) {
      asm volatile("" ::: "memory");
      m0 = ldsc(ws, ma_, gb); m1 = ldsc(ws, ma_, gb + 4 * S3_STRIDE); m2 = ldsc(ws, mc_, gb);
      o0 = ldsc(ws, oa, gb); o1 = ldsc(ws, oa, gb + 4 * S3_STRIDE); o2 = ldsc(ws, oc, gb);
      __builtin_amdgcn_sched_barrier(0);
      m0 ^= T; m1 ^= T; m2 ^= T; o0 ^= T; o1 ^= T; o2 ^= T;
      if (allok(or4(m0) | or4(m1) | or4(m2) | or4(o0) | or4(o1) | or4(o2)) || spin > SPIN_MAX) break;
      __builtin_amdgcn_s_sleep(1);
    }
    const float ma = asf(hd ? m0.z : m0.x), mb = asf(hd ? m1.z : m1.x), mc = row == 3 ? -INFINITY : asf(hd ? m2.z : m2.x);
    const float MM = xrow_max(vmax(vmax(ma, mb), mc));
    float fa = __expf(ma - MM), fb = __expf(mb - MM), fc = __expf(mc - MM);
    const float inv = __builtin_amdgcn_rcpf(xrow_sum(fa * asf(hd ? m0.w : m0.y) + fb * asf(hd ? m1.w : m1.y) + fc * asf(hd ? m2.w : m2.y)));
    v4u h0, h1;
    shuf(ln.shw, ln.shr, tr4(asf4(o0) * fa + asf4(o1) * fb + asf4(o2) * fc) * inv, h0, h1);
    put_halves(ln, L_REDA + w * 128, gemv8(wt, h0, h1, ln.k));
    bar();
    if (w == 0) pub64(ws, sumN<8>(Lf + L_REDA + lane), T, lane, WS_S4 + (k * 16 + g) * 256);
    return;
  }
  if (!(light && g < 9) || w > 8) return;
  {
    const int j = g;
    float x1 = 0.f, gbv = 0.f;
    if (w == 0) {
      const float xv = bf(p.x[64 * j + col]);
      const unsigned Tl = row == 0 ? T.x : row == 1 ? T.y : row == 2 ? T.z : T.w;
      unsigned a2, b2;
      v4u q0, q1, q2, q3;
      for (unsigned spin = 0;; ++spin) {
        asm volatile("" ::: "memory");
        const int vo = (j * 16 + row) * 256 + c4 * 16;
        a2 = ldsc1(ws, col * 4, WS_SB + j * 256); b2 = ldsc1(ws, col * 4, WS_SB + (27 + j) * 256);
        q0 = ldsc(ws, vo, WS_S4); q1 = ldsc(ws, vo, WS_S4 + 1024); q2 = ldsc(ws, vo, WS_S4 + 2048); q3 = ldsc(ws, vo, WS_S4 + 3072);
        __builtin_amdgcn_sched_barrier(0);
        q0 ^= T; q1 ^= T; q2 ^= T; q3 ^= T; a2 ^= Tl; b2 ^= Tl;
        if (allok(a2 | b2 | or4(q0) | or4(q1) | or4(q2) | or4(q3)) || spin > SPIN_MAX) break;
        __builtin_amdgcn_s_sleep(8);
      }
      x1 = rb(xv + rb(asf(a2) * rb(tr4((asf4(q0) + asf4(q1)) + (asf4(q2) + asf4(q3))))));
      gbv = asf(b2);
      pin(x1); pin(gbv);
    }
    {
      v4u y0, y1;
      int vo = (8 * w + row) * 256 + c4 * 16;
      LAS float* rw = Lf + L_REDG + w * 64 + col;
      pin(vo); pin(rw);
      for (unsigned spin = 0;; ++spin) {
        asm volatile("" ::: "memory");
        y0 = ldsc(ws, vo, WS_S5 + j * 72 * 256); y1 = ldsc(ws, vo, WS_S5 + j * 72 * 256 + 1024);
        __builtin_amdgcn_sched_barrier(0);
        y0 ^= T; y1 ^= T;
        if (allok(or4(y0) | or4(y1)) || spin > SPIN_MAX) break;
        __builtin_amdgcn_s_sleep(1);
      }
      *rw = tr4(asf4(y0) + asf4(y1));
    }
    bar();
    if (w == 0) {
      LAS float* r = Lf + L_REDG + col;
      const float y = ((r[0] + r[64]) + (r[128] + r[192])) + ((r[256] + r[320]) + (r[384] + r[448])) + r[512];
      f32x2 o = {x1 + rb(gbv * rb(y)), 0.f};
      p.out[64 * j + col] = (u16)(__builtin_bit_cast(unsigned, __builtin_convertvector(o, bf16x2)) & 0xFFFFu);
      if (j == 0 && lane == 0) __builtin_amdgcn_raw_buffer_store_b32(E + 1u, ws, 0, WS_EPOCH, 16);
    }
  }
}

}

extern "C" void kernel_launch(void* const* d_in, const int* in_sizes, int n_in, void* d_out, int out_size, void* d_ws, size_t ws_size,
                              hipStream_t stream) {
  Params p;
  p.x = (const u16*)d_in[0]; p.kc = (const u16*)d_in[1]; p.vc = (const u16*)d_in[2]; p.wq = (const u16*)d_in[3]; p.wo = (const u16*)d_in[4];
  p.wg = (const u16*)d_in[6]; p.wu = (const u16*)d_in[7]; p.wd = (const u16*)d_in[8]; p.c = (const u16*)d_in[9]; p.wm = (const u16*)d_in[10];
  p.out = (u16*)d_out; p.ws = (unsigned char*)d_ws;
  fused_decode<<<256, NTHR, 0, stream>>>(p);
}
```

```cpp
#include <hip/hip_runtime.h>
#include <hip/hip_bf16.h>
#include <stdint.h>

namespace {

typedef unsigned short u16;
typedef unsigned v4u __attribute__((ext_vector_type(4)));
typedef unsigned v2u __attribute__((ext_vector_type(2)));
typedef float f32x4 __attribute__((ext_vector_type(4)));
typedef float f32x2 __attribute__((ext_vector_type(2)));
typedef __bf16 bf16x8 __attribute__((ext_vector_type(8)));
typedef __bf16 bf16x2 __attribute__((ext_vector_type(2)));
typedef __amdgpu_buffer_rsrc_t rsrc_t;
#define LAS __attribute__((address_space(3)))

constexpr int D = 576, SK = 65536, WIN = 4096, FF = 4608;
constexpr int NTHR = 768;
constexpr int WS_EPOCH = 0;
constexpr int WS_S1 = 4096;
constexpr int WS_SQ = WS_S1 + 72 * 256;
constexpr int WS_SB = WS_SQ + 64 * 256;
constexpr int WS_S4 = WS_SB + 36 * 256;
constexpr int WS_S5 = WS_S4 + 144 * 256;
constexpr int WS_S3 = WS_S5 + 648 * 256;
constexpr int S3_STRIDE = 1280;
constexpr int WS_END = WS_S3 + 176 * S3_STRIDE;
constexpr unsigned SPIN_MAX = 1u << 19;

struct Params {
  const u16 *x, *kc, *vc, *wq, *wo, *wg, *wu, *wd, *c, *wm;
  u16* out;
  unsigned char* ws;
};

constexpr int SH_STRIDE = 160;
constexpr int L_SH = 0;
constexpr int L_REDA = L_SH + 12 * SH_STRIDE;
constexpr int L_REDG = L_REDA + 1536;
constexpr int L_ST = L_REDG + 1152;
constexpr int L_Q = L_ST + 64;
constexpr int L_MW = L_Q + 128;
constexpr int L_LW = L_MW + 32;
constexpr int L_CNT = L_LW + 32;
constexpr int L_O = L_CNT + 16;
constexpr int L_END = L_O + 12 * 256;

__device__ __forceinline__ rsrc_t mk(const void* p, unsigned bytes) {
  return __builtin_amdgcn_make_buffer_rsrc((void*)p, 0, (int)bytes, 0x00020000);
}
__device__ __forceinline__ v4u ldnt(rsrc_t r, int v, int s) { return __builtin_amdgcn_raw_buffer_load_b128(r, v, s, 2); }
__device__ __forceinline__ v4u ldsc(rsrc_t r, int v, int s) { return __builtin_amdgcn_raw_buffer_load_b128(r, v, s, 16); }
__device__ __forceinline__ unsigned ldsc1(rsrc_t r, int v, int s) { return __builtin_amdgcn_raw_buffer_load_b32(r, v, s, 16); }
__device__ __forceinline__ void stsc4(rsrc_t r, v4u x, int v, int s) { __builtin_amdgcn_raw_buffer_store_b128(x, r, v, s, 16); }
__device__ __forceinline__ float asf(unsigned x) { return __uint_as_float(x); }
__device__ __forceinline__ unsigned asu(float x) { return __float_as_uint(x); }
template <class V> __device__ __forceinline__ void pin(V& x) { asm volatile("" : "+v"(x)); }
__device__ __forceinline__ unsigned rep(float x) {
  f32x2 v = {x, x};
  return __builtin_bit_cast(unsigned, __builtin_convertvector(v, bf16x2));
}
__device__ __forceinline__ float rb(float x) { return asf(rep(x) & 0xFFFF0000u); }
__device__ __forceinline__ float bf(u16 x) { return asf((unsigned)x << 16); }
__device__ __forceinline__ unsigned tg(float x, unsigned T) { return (asu(x) & 0xFFFFFF00u) | T; }
__device__ __forceinline__ unsigned tgr(float x, unsigned T) { return ((asu(x) + 0x80u) & 0xFFFFFF00u) | T; }
__device__ __forceinline__ v4u tg4(f32x4 y, v4u T) { return v4u{tg(y.x, T.x), tg(y.y, T.y), tg(y.z, T.z), tg(y.w, T.w)}; }
__device__ __forceinline__ v4u make_tag(unsigned e, unsigned xw, unsigned cw) {
  const unsigned t = (e + 1u) * 0x01010101u + (xw ^ (cw * 0x9E3779B1u)) * 0x85EBCA6Bu;
  return v4u{t & 0xFFu, (t >> 8) & 0xFFu, (t >> 16) & 0xFFu, t >> 24};
}
template <int C> __device__ __forceinline__ float dpp(float x);
__device__ __forceinline__ void pub64(rsrc_t ws, float v, v4u T, int lane, int soff);
__device__ __forceinline__ unsigned or4(v4u d) { return (d.x | d.y) | (d.z | d.w); }
__device__ __forceinline__ bool allok(unsigned bad) { return __all((bad & 0xFFu) == 0u); }
template <int C> __device__ __forceinline__ float dpp(float x) {
  return asf((unsigned)__builtin_amdgcn_update_dpp(0, (int)asu(x), C, 0xF, 0xF, true));
}
__device__ __forceinline__ void pub64(rsrc_t ws, float v, v4u T, int lane, int soff) {
  const v4u d = {tg(v, T.x), tg(dpp<0x55>(v), T.y), tg(dpp<0xAA>(v), T.z), tg(dpp<0xFF>(v), T.w)};
  if ((lane & 3) == 0) __builtin_amdgcn_raw_buffer_store_b128(d, ws, lane * 4, soff, 16);
}
__device__ __forceinline__ float xrow_sum(float x) {
  auto s = __builtin_amdgcn_permlane16_swap(asu(x), asu(x), false, false);
  x = asf(s[0]) + asf(s[1]);
  auto t = __builtin_amdgcn_permlane32_swap(asu(x), asu(x), false, false);
  return asf(t[0]) + asf(t[1]);
}
__device__ __forceinline__ float xrow_max(float x) {
  auto s = __builtin_amdgcn_permlane16_swap(asu(x), asu(x), false, false);
  x = __builtin_amdgcn_fmed3f(asf(s[0]), asf(s[1]), INFINITY);
  auto t = __builtin_amdgcn_permlane32_swap(asu(x), asu(x), false, false);
  return __builtin_amdgcn_fmed3f(asf(t[0]), asf(t[1]), INFINITY);
}
__device__ __forceinline__ float tr4(f32x4 v) {
  auto a = __builtin_amdgcn_permlane16_swap(asu(v.x), asu(v.y), false, false);
  auto b = __builtin_amdgcn_permlane16_swap(asu(v.z), asu(v.w), false, false);
  const float s01 = asf(a[0]) + asf(a[1]), s23 = asf(b[0]) + asf(b[1]);
  auto c = __builtin_amdgcn_permlane32_swap(asu(s01), asu(s23), false, false);
  return asf(c[0]) + asf(c[1]);
}
__device__ __forceinline__ float wave_sum2(float x, float y) {
  x += dpp<0xB1>(x); y += dpp<0xB1>(y);
  x += dpp<0x4E>(x); y += dpp<0x4E>(y);
  x += dpp<0x141>(x); y += dpp<0x141>(y);
  x += dpp<0x140>(x); y += dpp<0x140>(y);
  auto a = __builtin_amdgcn_permlane16_swap(asu(x), asu(y), false, false);
  const float s = asf(a[0]) + asf(a[1]);
  auto c = __builtin_amdgcn_permlane32_swap(asu(s), asu(s), false, false);
  return asf(c[0]) + asf(c[1]);
}
__device__ __forceinline__ f32x4 asf4(v4u d) { return __builtin_bit_cast(f32x4, d); }
__device__ __forceinline__ f32x4 mfma(v4u a, v4u b, f32x4 c) {
  return __builtin_amdgcn_mfma_f32_16x16x32_bf16(__builtin_bit_cast(bf16x8, a), __builtin_bit_cast(bf16x8, b), c, 0, 0, 0);
}
struct Mask { unsigned m0, m1, m2, m3; };
__device__ __forceinline__ v4u diag(unsigned hh, const Mask& k) { return v4u{hh & k.m0, hh & k.m1, hh & k.m2, hh & k.m3}; }
__device__ __forceinline__ void bar() {
  asm volatile("s_waitcnt lgkmcnt(0)" ::: "memory");
  __builtin_amdgcn_s_barrier();
  asm volatile("" ::: "memory");
}
__device__ __forceinline__ void rawbar() {
  __builtin_amdgcn_sched_barrier(0);
  __builtin_amdgcn_s_barrier();
  __builtin_amdgcn_sched_barrier(0);
}
__device__ __forceinline__ float silu(float v) { return v * __builtin_amdgcn_rcpf(1.f + __expf(-v)); }
__device__ __forceinline__ float fold1(float x) {
  auto t = __builtin_amdgcn_permlane32_swap(asu(x), asu(x), false, false);
  return asf(t[0]) + dpp<0x128>(asf(t[1]));
}
__device__ __forceinline__ f32x4 fold(f32x4 a) { return f32x4{fold1(a.x), fold1(a.y), fold1(a.z), fold1(a.w)}; }
__device__ __forceinline__ float fold2_1(float a, float b) {
  auto t = __builtin_amdgcn_permlane32_swap(asu(a), asu(b), false, false);
  return asf(t[0]) + dpp<0x128>(asf(t[1]));
}


struct Lane {
  Mask k;
  LAS unsigned* shw;
  LAS unsigned* shr;
  LAS float* put;
  bool putv;
};
__device__ __forceinline__ void shuf(LAS unsigned* wr, LAS unsigned* rd, float v, v4u& h0, v4u& h1) {
  *wr = rep(v);
  asm volatile("" ::: "memory");
  h0 = *(LAS v4u*)rd;
  h1 = *(LAS v4u*)(rd + 4);
}
__device__ __forceinline__ f32x4 gemv8(const v4u (&w)[8], v4u h0, v4u h1, const Mask& k) {
  f32x4 a = {0.f, 0.f, 0.f, 0.f}, b = {0.f, 0.f, 0.f, 0.f};
  const v4u a0 = diag(h0.x, k), a1 = diag(h0.y, k), a2 = diag(h0.z, k), a3 = diag(h0.w, k);
  a = mfma(a0, w[0], a); b = mfma(a1, w[1], b);
  const v4u a4 = diag(h1.x, k), a5 = diag(h1.y, k);
  a = mfma(a2, w[2], a); b = mfma(a3, w[3], b);
  const v4u a6 = diag(h1.z, k), a7 = diag(h1.w, k);
  a = mfma(a4, w[4], a); b = mfma(a5, w[5], b);
  a = mfma(a6, w[6], a); b = mfma(a7, w[7], b);
  return a + b;
}
__device__ __forceinline__ f32x2 ln_sums(LAS float* st) {
  const f32x4 a = *(LAS f32x4*)(st), b = *(LAS f32x4*)(st + 4), c = *(LAS f32x4*)(st + 8), d = *(LAS f32x4*)(st + 12);
  const f32x2 e = *(LAS f32x2*)(st + 16);
  const f32x4 t = (a + b) + (c + d);
  return f32x2{t.x + t.z + e.x, t.y + t.w + e.y};
}
template <int N> __device__ __forceinline__ float sumN(LAS float* red) {
  float a = 0.f, b = 0.f;
#pragma unroll
  for (int i = 0; i < N; i += 2) { a += red[i * 64]; b += red[i * 64 + 64]; }
  return a + b;
}
__device__ __forceinline__ void loads8(v4u (&w)[8], rsrc_t r, int stride, int lane, int base) {
  const int vo = (lane >> 3) * stride + (lane & 7) * 16;
#pragma unroll
  for (int s = 0; s < 8; ++s) w[s] = ldnt(r, vo, base + 8 * s * stride);
}
struct Tile18 { v4u w[18]; v2u c; };
__device__ __forceinline__ void tile18_load(Tile18& t, const u16* c, rsrc_t rwm, int ks, int cb, int lane) {
  t.c = *(const v2u*)(c + 144 * ks + 4 * (lane < 36 ? lane : 35));
  const int vo = (lane >> 3) * 6912 + (lane & 7) * 16;
#pragma unroll
  for (int s = 0; s < 18; ++s) t.w[s] = ldnt(rwm, vo, (144 * ks + 8 * s) * 6912 + cb);
}
__device__ __forceinline__ f32x4 tile18_mac(const Tile18& t, LAS unsigned* sh, int lane, const Mask& k) {
  if (lane < 36) {
    LAS unsigned* d = sh + (4 * (lane & 1)) * 20 + (lane >> 1);
    d[0] = rep(silu(asf(t.c.x << 16))); d[20] = rep(silu(asf(t.c.x & 0xFFFF0000u)));
    d[40] = rep(silu(asf(t.c.y << 16))); d[60] = rep(silu(asf(t.c.y & 0xFFFF0000u)));
  }
  asm volatile("" ::: "memory");
  LAS unsigned* s = sh + (lane >> 3) * 20;
  const v4u h0 = *(LAS v4u*)s, h1 = *(LAS v4u*)(s + 4), h2 = *(LAS v4u*)(s + 8), h3 = *(LAS v4u*)(s + 12);
  const v2u h4 = *(LAS v2u*)(s + 16);
  f32x4 a = {0.f, 0.f, 0.f, 0.f}, b = {0.f, 0.f, 0.f, 0.f};
  a = mfma(diag(h0.x, k), t.w[0], a); b = mfma(diag(h0.y, k), t.w[1], b); a = mfma(diag(h0.z, k), t.w[2], a); b = mfma(diag(h0.w, k), t.w[3], b);
  a = mfma(diag(h1.x, k), t.w[4], a); b = mfma(diag(h1.y, k), t.w[5], b); a = mfma(diag(h1.z, k), t.w[6], a); b = mfma(diag(h1.w, k), t.w[7], b);
  a = mfma(diag(h2.x, k), t.w[8], a); b = mfma(diag(h2.y, k), t.w[9], b); a = mfma(diag(h2.z, k), t.w[10], a); b = mfma(diag(h2.w, k), t.w[11], b);
  a = mfma(diag(h3.x, k), t.w[12], a); b = mfma(diag(h3.y, k), t.w[13], b); a = mfma(diag(h3.z, k), t.w[14], a); b = mfma(diag(h3.w, k), t.w[15], b);
  a = mfma(diag(h4.x, k), t.w[16], a); b = mfma(diag(h4.y, k), t.w[17], b);
  return a + b;
}
__device__ __forceinline__ void lds_arrive(LAS unsigned* c) {
  if ((threadIdx.x & 63) == 0) asm volatile("ds_add_u32 %0, %1" ::"v"(c), "v"(1u) : "memory");
  asm volatile("" ::: "memory");
}
__device__ __forceinline__ float vmax(float a, float b) { return __builtin_amdgcn_fmed3f(a, b, INFINITY); }
__device__ __forceinline__ float max12(LAS float* m) {
  const f32x4 b0 = *(LAS f32x4*)m, b1 = *(LAS f32x4*)(m + 4), b2 = *(LAS f32x4*)(m + 8);
  return vmax(vmax(vmax(vmax(b0.x, b0.y), vmax(b0.z, b0.w)), vmax(vmax(b1.x, b1.y), vmax(b1.z, b1.w))), vmax(vmax(b2.x, b2.y), vmax(b2.z, b2.w)));
}
__device__ __forceinline__ unsigned lds_inc(LAS unsigned* c) {
  asm volatile("" ::: "memory");
  unsigned old = 0;
  if ((threadIdx.x & 63) == 0) old = __hip_atomic_fetch_add(c, 1u, __ATOMIC_RELAXED, __HIP_MEMORY_SCOPE_WORKGROUP);
  old = (unsigned)__builtin_amdgcn_readfirstlane((int)old);
  asm volatile("" ::: "memory");
  return old;
}
__device__ __forceinline__ void lds_wait(LAS unsigned* c, unsigned need) {
  for (unsigned spin = 0; *(volatile LAS unsigned*)c < need && spin < (1u << 28); ++spin) {}
  asm volatile("" ::: "memory");
}
__device__ __forceinline__ void make_lane(Lane& ln, int lane, LAS unsigned* sh, LAS float* Lf) {
  const unsigned mm = (lane & 1) ? 0xFFFF0000u : 0x0000FFFFu;
  const int d = (lane & 7) >> 1, c = lane & 15, g = lane >> 4;
  ln.k.m0 = d == 0 ? mm : 0u; ln.k.m1 = d == 1 ? mm : 0u; ln.k.m2 = d == 2 ? mm : 0u; ln.k.m3 = d == 3 ? mm : 0u;
  ln.shw = sh + 32 * (c & 1) + 8 * g + (c >> 1);
  ln.shr = sh + (lane >> 3) * 8;
  ln.put = Lf + (c >> 3) * 64 + 8 * (c & 7) + 4 * (g & 1);
  ln.putv = (c >> 3) == (g >> 1);
  pin(ln.k.m0); pin(ln.k.m1); pin(ln.k.m2); pin(ln.k.m3); pin(ln.shw); pin(ln.shr); pin(ln.put);
}
__device__ __forceinline__ void put_halves(const Lane& ln, int base_dw, f32x4 a) {
  if (ln.putv) *(LAS f32x4*)(ln.put + base_dw) = a;
}
__device__ __forceinline__ void modb_finish(const Tile18& t, LAS unsigned* sh, const Lane& ln, LAS float* Lf, int red_dw, LAS unsigned* cnt, int ks, int lane,
                                            rsrc_t ws, int tile, v4u T) {
  put_halves(ln, red_dw + ks * 128, tile18_mac(t, sh, lane, ln.k));
  if (lds_inc(cnt) == 3u) pub64(ws, rb(sumN<8>(Lf + red_dw + lane)), T, lane, WS_SB + tile * 256);
}

__global__ void __launch_bounds__(NTHR, 3) fused_decode(Params p) {
  __shared__ __attribute__((aligned(16))) unsigned smem[L_END];
  LAS unsigned* L = (LAS unsigned*)smem;
  LAS float* Lf = (LAS float*)smem;
  const int b = blockIdx.x, xcd = b & 7, slot = b >> 3;
  const int lane = threadIdx.x & 63, w = __builtin_amdgcn_readfirstlane(threadIdx.x >> 6);
  const int c4 = lane & 15, row = lane >> 4;
  const int col = 4 * c4 + row;
#define LOAD_EPOCH() __builtin_amdgcn_sched_barrier(0); const unsigned Ev = __builtin_amdgcn_raw_buffer_load_b32(ws, 0, WS_EPOCH, 16); \
  const unsigned Xv = __builtin_amdgcn_raw_buffer_load_b32(mk(p.x, 4), 0, 0, 0), Cv = __builtin_amdgcn_raw_buffer_load_b32(mk(p.c, 4), 0, 0, 0); __builtin_amdgcn_sched_barrier(0)
#define TAG_NOW() (E = (unsigned)__builtin_amdgcn_readfirstlane((int)Ev), T = make_tag(E, (unsigned)__builtin_amdgcn_readfirstlane((int)Xv), (unsigned)__builtin_amdgcn_readfirstlane((int)Cv)))
#define GET_TAG() unsigned E; v4u T; TAG_NOW(); (void)E
  LAS unsigned* sh = L + L_SH + w * SH_STRIDE;
  Lane ln;

  if (slot <= 9) {
  const int rgate = (int)((size_t)p.wg >> 7), xstar = (3 - rgate) & 7;
  const bool qonly = slot == 9 && xcd == xstar;
  if (slot < 9 || qonly) {
    if (w >= 10) return;
    const int f = xcd * 9 + slot;
    __builtin_amdgcn_sched_barrier(0);
    const rsrc_t ws = mk(p.ws, WS_END), rwm = mk(p.wm, D * 6 * D * 2);
    if (w == 9) {
      __builtin_amdgcn_s_setprio(3);
      if (lane == 0) L[L_CNT] = 0u;
      if (qonly) { bar(); return; }
      Tile18 t;
      tile18_load(t, p.c, rwm, f / 18, (f % 18) * 128, lane);
      LOAD_EPOCH();
      bar();
      make_lane(ln, lane, sh, Lf);
      GET_TAG();
      const f32x4 a = fold(tile18_mac(t, sh, lane, ln.k));
      if (c4 < 8 && row < 2) stsc4(ws, v4u{tgr(a.x, T.x), tgr(a.y, T.y), tgr(a.z, T.z), tgr(a.w, T.w)}, (8 * c4 + 4 * row) * 4, WS_S1 + f * 256);
      return;
    }
    const int j = w;
    const int s0 = (3 - rgate - xcd) & 7;
    const bool hasq = qonly || !(slot == s0 || (s0 == 0 && slot == 8));
    const int n = qonly ? xcd * 8 + 7 : xcd * 8 + slot - (slot > s0 ? 1 : 0);
    const u16 xr = p.x[64 * j + col], x0r = p.x[0];
    LOAD_EPOCH();
    rawbar();
    v4u wt[8], wg[8], wu[8], wd[8];
    const rsrc_t rg = mk(p.wg, D * FF * 2), ru = mk(p.wu, D * FF * 2), rd = mk(p.wd, FF * D * 2);
    loads8(wt, mk(p.wq, hasq ? D * 4096 * 2 : 0), 8192, lane, 64 * j * 8192 + n * 128);
    unsigned tg0 = __builtin_amdgcn_raw_buffer_load_b32(rg, 0, 64 * j * FF * 2 + f * 128, 2);
    unsigned tg1 = __builtin_amdgcn_raw_buffer_load_b32(ru, 0, 64 * j * FF * 2 + f * 128, 2);
    unsigned tg2 = __builtin_amdgcn_raw_buffer_load_b32(rd, 0, 64 * f * 1152 + j * 128, 2);
    __builtin_amdgcn_sched_barrier(0);
    make_lane(ln, lane, sh, Lf);
    GET_TAG();
    const float x0 = bf(x0r);
    float xv = bf(xr);
    {
      const float d = xv - x0;
      const float s = wave_sum2(d, d * d);
      if ((lane & 47) == 0) Lf[L_ST + 2 * j + (lane >> 4)] = s;
    }
    lds_arrive(L + L_CNT);
    lds_wait(L + L_CNT, 9u);
    float mean1, lnv;
    {
      const f32x2 s = ln_sums(Lf + L_ST);
      const float dm = s.x * (1.f / D);
      mean1 = x0 + dm;
      const float var = fmaxf(s.y * (1.f / D) - dm * dm, 0.f);
      const float den = rb(__builtin_amdgcn_sqrtf(rb(rb(var) + 1e-5f)));
      lnv = rb(rb(xv - rb(mean1)) / den);
      pin(lnv); pin(mean1); pin(xv);
    }
    if (hasq) {
      int o1 = (row * 18 + j) * 256 + c4 * 16;
      pin(o1);
      __builtin_amdgcn_s_waitcnt(0x0F70 | 5);
      v4u sa, ca;
      for (unsigned spin = 0;; ++spin) {
        asm volatile("" ::: "memory");
        sa = ldsc(ws, o1, WS_S1); ca = ldsc(ws, o1, WS_S1 + 9 * 256);
        __builtin_amdgcn_sched_barrier(0);
        sa ^= T; ca ^= T;
        if (allok(or4(sa) | or4(ca)) || spin > SPIN_MAX) break;
        __builtin_amdgcn_s_sleep(2);
      }
      const float h = rb(lnv * rb(1.f + rb(tr4(asf4(ca))))) + rb(tr4(asf4(sa)));
      v4u h0, h1;
      shuf(ln.shw, ln.shr, h, h0, h1);
      put_halves(ln, L_REDA + j * 128, gemv8(wt, h0, h1, ln.k));
      bar();
      if (j == 0) pub64(ws, rb(sumN<18>(Lf + L_REDA + lane)), T, lane, WS_SQ + n * 256);
      rawbar();
    }
    asm volatile("" ::"v"(tg0), "v"(tg1), "v"(tg2));
    if (qonly) return;
    loads8(wg, rg, FF * 2, lane, 64 * j * FF * 2 + f * 128);
    loads8(wu, ru, FF * 2, lane, 64 * j * FF * 2 + f * 128);
    loads8(wd, rd, 1152, lane, 64 * f * 1152 + j * 128);
    __builtin_amdgcn_sched_barrier(0);
    int o4 = (j * 16 + row) * 256 + c4 * 16, ob = j * 256 + col * 4, o5 = (8 * c4 + 4 * row) * 4;
    LAS float* redg_w = Lf + L_REDG + j * 128 + (row >> 1) * 64 + 8 * c4 + 4 * (row & 1);
    LAS float* redg_r = Lf + L_REDG + lane;
    LAS unsigned* shw2 = L + L_Q + (lane & 7) * 8 + (lane >> 3);
    LAS unsigned* shr2 = L + L_Q + (lane >> 3) * 8;
    const unsigned Tl = row == 0 ? T.x : row == 1 ? T.y : row == 2 ? T.z : T.w;
    pin(o4); pin(ob); pin(o5); pin(redg_w); pin(redg_r); pin(shw2); pin(shr2);
    asm volatile("s_waitcnt vmcnt(0)" ::: "memory");
    rawbar();
    float x1, sb, cb;
    {
      unsigned a, s2, c2;
      v4u q0, q1, q2, q3;
      q0 = ldsc(ws, o4, WS_S4); q1 = ldsc(ws, o4, WS_S4 + 1024); q2 = ldsc(ws, o4, WS_S4 + 2048); q3 = ldsc(ws, o4, WS_S4 + 3072);
      a = ldsc1(ws, ob, WS_SB); s2 = ldsc1(ws, ob, WS_SB + 9 * 256); c2 = ldsc1(ws, ob, WS_SB + 18 * 256);
      for (unsigned spin = 0; spin <= SPIN_MAX; ++spin) {
        asm volatile("" ::: "memory");
        bool again = false;
        if (!allok(or4(q0 ^ T))) { q0 = ldsc(ws, o4, WS_S4); again = true; }
        if (!allok(or4(q1 ^ T))) { q1 = ldsc(ws, o4, WS_S4 + 1024); again = true; }
        if (!allok(or4(q2 ^ T))) { q2 = ldsc(ws, o4, WS_S4 + 2048); again = true; }
        if (!allok(or4(q3 ^ T))) { q3 = ldsc(ws, o4, WS_S4 + 3072); again = true; }
        if (!allok((a ^ Tl) | (s2 ^ Tl) | (c2 ^ Tl))) {
          a = ldsc1(ws, ob, WS_SB); s2 = ldsc1(ws, ob, WS_SB + 9 * 256); c2 = ldsc1(ws, ob, WS_SB + 18 * 256);
          again = true;
        }
        if (!again) break;
      }
      q0 ^= T; q1 ^= T; q2 ^= T; q3 ^= T; a ^= Tl; s2 ^= Tl; c2 ^= Tl;
      x1 = rb(xv + rb(asf(a) * rb(tr4((asf4(q0) + asf4(q1)) + (asf4(q2) + asf4(q3))))));
      sb = asf(s2); cb = rb(1.f + asf(c2));
    }
    {
      const float d = x1 - mean1;
      const float s = wave_sum2(d, d * d);
      if ((lane & 47) == 0) Lf[L_ST + 32 + 2 * j + (lane >> 4)] = s;
    }
    bar();
    {
      const f32x2 s = ln_sums(Lf + L_ST + 32);
      const float dm = s.x * (1.f / D);
      const float var = fmaxf(s.y * (1.f / D) - dm * dm, 0.f);
      const float den = rb(__builtin_amdgcn_sqrtf(rb(rb(var) + 1e-5f)));
      const float h2 = rb(rb(rb(x1 - rb(mean1 + dm)) * __builtin_amdgcn_rcpf(den)) * cb) + sb;
      v4u h0, h1;
      shuf(ln.shw, ln.shr, h2, h0, h1);
      const f32x4 g = gemv8(wg, h0, h1, ln.k), u = gemv8(wu, h0, h1, ln.k);
      const f32x4 t = {fold2_1(g.x, u.x), fold2_1(g.y, u.y), fold2_1(g.z, u.z), fold2_1(g.w, u.w)};
      if (c4 < 8) *(LAS f32x4*)redg_w = t;
    }
    bar();
    if (j == 1) {
      float g = 0.f, u = 0.f, g2 = 0.f, u2 = 0.f;
#pragma unroll
      for (int i = 0; i < 8; i += 2) {
        g += redg_r[i * 128]; u += redg_r[i * 128 + 64];
        g2 += redg_r[i * 128 + 128]; u2 += redg_r[i * 128 + 192];
      }
      g = rb((g + redg_r[1024]) + g2); u = rb((u + redg_r[1088]) + u2);
      const float z = 1.5957691216057308f * (g + 0.044715f * g * g * g);
      *shw2 = rep(rb(g * __builtin_amdgcn_rcpf(1.f + __expf(-z))) * u);
    }
    bar();
    {
      const v4u h0 = *(LAS v4u*)shr2, h1 = *(LAS v4u*)(shr2 + 4);
      const f32x4 y = fold(gemv8(wd, h0, h1, ln.k));
      if (c4 < 8 && row < 2) stsc4(ws, tg4(y, T), o5, WS_S5 + (j * 72 + f) * 256);
    }
    return;
  }

  {
    int tile = 16 + (w >> 2) * 8 + xcd;
    if (w >= 8 && xcd >= 4) {
      const int r = xcd - 4 - ((xstar >= 4 && xcd > xstar) ? 1 : 0);
      if (r >= (xstar < 4 ? 3 : 2)) return;
      tile = 16 + r * 8 + xstar;
    }
    if (threadIdx.x < 3) L[L_CNT + threadIdx.x] = 0u;
    const rsrc_t ws = mk(p.ws, WS_END), rwm = mk(p.wm, D * 6 * D * 2);
    Tile18 t;
    tile18_load(t, p.c, rwm, w & 3, 2304 + tile * 128, lane);
    LOAD_EPOCH();
    __builtin_amdgcn_sched_barrier(0);
    bar();
    make_lane(ln, lane, sh, Lf);
    GET_TAG();
    modb_finish(t, sh, ln, Lf, L_REDA + (w >> 2) * 512, L + L_CNT + (w >> 2), w & 3, lane, ws, tile, T);
    return;
  }

  }
  const int g1 = (slot + 11) >> 5, g = 2 * xcd + g1, k = slot - 10 - 11 * g1;
  const bool light = k == 10;
  const int s3 = WS_S3 + (g * 11 + k) * S3_STRIDE;
  __builtin_amdgcn_sched_barrier(0);
  const rsrc_t ws = mk(p.ws, WS_END), rwm = mk(p.wm, D * 6 * D * 2);
  unsigned E = 0;
  v4u T = {0u, 0u, 0u, 0u};
  v4u wt[8];
  const rsrc_t rwo = mk(p.wo, (k < 9 && w < 4) ? 4096 * D * 2 : 0);
  if (light && w >= 8) {
    Tile18 t;
    tile18_load(t, p.c, rwm, w - 8, 2304 + g * 128, lane);
    LOAD_EPOCH();
    if (w == 8 && lane < 32) Lf[L_MW + lane] = -INFINITY;
    if (w == 9 && lane < 32) Lf[L_LW + lane] = 0.f;
    if (w == 10 && lane < 3) L[L_CNT + lane] = 0u;
    bar();
    make_lane(ln, lane, sh, Lf);
    TAG_NOW();
    modb_finish(t, sh, ln, Lf, L_REDA, L + L_CNT + 1, w - 8, lane, ws, g, T);
    rawbar();
    rawbar();
  } else {
    const size_t kvoff = ((size_t)g * SK + (SK - WIN) + k * 384 + w * 32) * 128;
    const rsrc_t rk = mk(p.kc + kvoff, 8192), rv = mk(p.vc + kvoff, 8192);
    v4u kk[8], vv[8];
    const int kvo = (lane & 7) * 256 + ((lane >> 3) & 1) * 64 + row * 16, vvo = row * 2048 + c4 * 16;
#pragma unroll
    for (int t = 0; t < 4; ++t) {
      kk[2 * t] = ldnt(rk, kvo, t * 2048);
      kk[2 * t + 1] = ldnt(rk, kvo + 128, t * 2048);
    }
    const unsigned tv0 = __builtin_amdgcn_raw_buffer_load_b32(rv, 0, 0, 2);
    const unsigned tv1 = __builtin_amdgcn_raw_buffer_load_b32(mk(p.out, (light && w == 0) ? D * 2 : 0), 0, 0, 0);
    const unsigned tv2 = __builtin_amdgcn_raw_buffer_load_b32(rwo, 0, (256 * g + 64 * (w & 3)) * 1152 + k * 128, 2);
    LOAD_EPOCH();
    if (!light) {
      if (w == 8 && lane < 32) Lf[L_MW + lane] = -INFINITY;
      if (w == 9 && lane < 32) Lf[L_LW + lane] = 0.f;
      if (w == 10 && lane < 3) L[L_CNT + lane] = 0u;
    }
    __builtin_amdgcn_sched_barrier(0);
    bar();
    loads8(wt, rwo, 1152, lane, (256 * g + 64 * (w & 3)) * 1152 + k * 128);
#pragma unroll
    for (int s = 0; s < 8; ++s) vv[s] = ldnt(rv, vvo + s * 256, 0);
    __builtin_amdgcn_sched_barrier(0);
    make_lane(ln, lane, sh, Lf);
    TAG_NOW();
    LAS unsigned* qrd = L + L_Q + (lane & 1) * 64 + ((lane >> 1) & 1) * 16 + row * 4;
    LAS float* mwr = Lf + L_MW + ((lane >> 3) & 1) * 16;
    LAS float* owr = Lf + L_O + w * 256 + (row >> 1) * 128 + 8 * c4 + 4 * (row & 1);
    pin(qrd); pin(mwr); pin(owr);
    asm volatile("" ::"v"(tv0), "v"(tv1), "v"(tv2));
    if (w == 0) {
      const int oq = lane * 16, sq = WS_SQ + g * 1024;
      v4u q;
      for (unsigned spin = 0;; ++spin) {
        asm volatile("" ::: "memory");
        q = ldsc(ws, oq, sq) ^ T;
        if (allok(or4(q)) || spin > SPIN_MAX) break;
        __builtin_amdgcn_s_sleep(2);
      }
      *(LAS v2u*)(L + L_Q + lane * 2) = v2u{(q.x >> 16) | q.y, (q.z >> 16) | q.w};
    }
    bar();
    float sc = 0.f;
    {
      const v4u q0 = *(LAS v4u*)qrd, q1 = *(LAS v4u*)(qrd + 32);
#pragma unroll
      for (int t = 0; t < 4; ++t) {
        f32x4 d = {0.f, 0.f, 0.f, 0.f};
        d = mfma(q0, kk[2 * t], d);
        d = mfma(q1, kk[2 * t + 1], d);
        const float h0 = d.x + dpp<0x128>(d.z), h1 = d.w + dpp<0x128>(d.y);
        const float v = c4 < 8 ? h0 : h1;
        sc = row == t ? v : sc;
      }
      sc = rb(rb(sc) * 0.08838834764831845f);
      float m = sc;
      m = vmax(m, dpp<0xB1>(m)); m = vmax(m, dpp<0x4E>(m)); m = vmax(m, dpp<0x141>(m));
      m = xrow_max(m);
      if ((lane & 55) == 0) mwr[w] = m;
    }
    bar();
    {
      const float M = max12(mwr);
      const float pe = __expf(sc - M);
      v4u h0, h1;
      shuf(sh + lane, ln.shr, pe, h0, h1);
      float l = pe;
      l += dpp<0xB1>(l); l += dpp<0x4E>(l); l += dpp<0x141>(l);
      l = xrow_sum(l);
      if ((lane & 55) == 0) mwr[32 + w] = l;
      lds_arrive(L + L_CNT + 2);
      v4u A[8];
      A[0] = diag(h0.x, ln.k); A[1] = diag(h0.y, ln.k); A[2] = diag(h0.z, ln.k); A[3] = diag(h0.w, ln.k);
      A[4] = diag(h1.x, ln.k); A[5] = diag(h1.y, ln.k); A[6] = diag(h1.z, ln.k); A[7] = diag(h1.w, ln.k);
      __builtin_amdgcn_sched_barrier(0);
      f32x4 o0 = {0.f, 0.f, 0.f, 0.f}, o1 = {0.f, 0.f, 0.f, 0.f};
      o0 = mfma(A[0], vv[0], o0); o1 = mfma(A[1], vv[1], o1); o0 = mfma(A[2], vv[2], o0); o1 = mfma(A[3], vv[3], o1);
      o0 = mfma(A[4], vv[4], o0); o1 = mfma(A[5], vv[5], o1); o0 = mfma(A[6], vv[6], o0); o1 = mfma(A[7], vv[7], o1);
      *(LAS f32x4*)owr = o0 + o1;
      lds_arrive(L + L_CNT);
    }
    if (w >= 7) __builtin_amdgcn_s_setprio(3);
    if (w == 7) {
      lds_wait(L + L_CNT + 2, light ? 8u : 12u);
      LAS float* lw = Lf + L_LW + (lane & 1) * 16;
      LAS float* mw = Lf + L_MW + (lane & 1) * 16;
      const f32x4 a0 = *(LAS f32x4*)lw, a1 = *(LAS f32x4*)(lw + 4), a2 = *(LAS f32x4*)(lw + 8);
      const float l = ((a0.x + a0.y) + (a0.z + a0.w)) + ((a1.x + a1.y) + (a1.z + a1.w)) + ((a2.x + a2.y) + (a2.z + a2.w));
      const float M = max12(mw);
      const v4u ml = {tg(M, T.x), tg(l, T.y), tg(dpp<0x55>(M), T.z), tg(dpp<0x55>(l), T.w)};
      if (lane == 0) stsc4(ws, ml, 0, s3 + 1024);
    }
  }
  if (w >= 8) {
    __builtin_amdgcn_s_setprio(3);
    const int wp = w - 8;
    LAS float* o = Lf + L_O + row * 256 + wp * 64 + c4 * 4;
    int so = wp * 256 + c4 * 16;
    pin(o); pin(so);
    lds_wait(L + L_CNT, light ? 8u : 12u);
    f32x4 v = *(LAS f32x4*)o + *(LAS f32x4*)(o + 1024);
    if (!light) v += *(LAS f32x4*)(o + 2048);
    const f32x4 vs = {xrow_sum(v.x), xrow_sum(v.y), xrow_sum(v.z), xrow_sum(v.w)};
    if (lane < 16) stsc4(ws, tg4(vs, T), so, s3);
    if (!(light && g < 9 && w == 8)) return;
  }
  if (k < 9) {
    if (w >= 4) return;
    const int k2 = row + 8 > 10 ? 10 : row + 8;
    const int gb = WS_S3 + g * 11 * S3_STRIDE;
    const bool hd = w >= 2;
    int oa = row * S3_STRIDE + w * 256 + c4 * 16, oc = k2 * S3_STRIDE + w * 256 + c4 * 16;
    int ma_ = row * S3_STRIDE + 1024, mc_ = k2 * S3_STRIDE + 1024;
    pin(oa); pin(oc); pin(ma_); pin(mc_);
    v4u m0, m1, m2;
    v4u o0, o1, o2;
    for (unsigned spin = 0;; ++spin) {
      asm volatile("" ::: "memory");
      m0 = ldsc(ws, ma_, gb); m1 = ldsc(ws, ma_, gb + 4 * S3_STRIDE); m2 = ldsc(ws, mc_, gb);
      o0 = ldsc(ws, oa, gb); o1 = ldsc(ws, oa, gb + 4 * S3_STRIDE); o2 = ldsc(ws, oc, gb);
      __builtin_amdgcn_sched_barrier(0);
      m0 ^= T; m1 ^= T; m2 ^= T; o0 ^= T; o1 ^= T; o2 ^= T;
      if (allok(or4(m0) | or4(m1) | or4(m2) | or4(o0) | or4(o1) | or4(o2)) || spin > SPIN_MAX) break;
      __builtin_amdgcn_s_sleep(1);
    }
    const float ma = asf(hd ? m0.z : m0.x), mb = asf(hd ? m1.z : m1.x), mc = row == 3 ? -INFINITY : asf(hd ? m2.z : m2.x);
    const float MM = xrow_max(vmax(vmax(ma, mb), mc));
    float fa = __expf(ma - MM), fb = __expf(mb - MM), fc = __expf(mc - MM);
    const float inv = __builtin_amdgcn_rcpf(xrow_sum(fa * asf(hd ? m0.w : m0.y) + fb * asf(hd ? m1.w : m1.y) + fc * asf(hd ? m2.w : m2.y)));
    v4u h0, h1;
    shuf(ln.shw, ln.shr, tr4(asf4(o0) * fa + asf4(o1) * fb + asf4(o2) * fc) * inv, h0, h1);
    put_halves(ln, L_REDA + w * 128, gemv8(wt, h0, h1, ln.k));
    bar();
    if (w == 0) pub64(ws, sumN<8>(Lf + L_REDA + lane), T, lane, WS_S4 + (k * 16 + g) * 256);
    return;
  }
  if (!(light && g < 9) || w > 8) return;
  {
    const int j = g;
    float x1 = 0.f, gbv = 0.f;
    if (w == 0) {
      const float xv = bf(p.x[64 * j + col]);
      const unsigned Tl = row == 0 ? T.x : row == 1 ? T.y : row == 2 ? T.z : T.w;
      unsigned a2, b2;
      v4u q0, q1, q2, q3;
      for (unsigned spin = 0;; ++spin) {
        asm volatile("" ::: "memory");
        const int vo = (j * 16 + row) * 256 + c4 * 16;
        a2 = ldsc1(ws, col * 4, WS_SB + j * 256); b2 = ldsc1(ws, col * 4, WS_SB + (27 + j) * 256);
        q0 = ldsc(ws, vo, WS_S4); q1 = ldsc(ws, vo, WS_S4 + 1024); q2 = ldsc(ws, vo, WS_S4 + 2048); q3 = ldsc(ws, vo, WS_S4 + 3072);
        __builtin_amdgcn_sched_barrier(0);
        q0 ^= T; q1 ^= T; q2 ^= T; q3 ^= T; a2 ^= Tl; b2 ^= Tl;
        if (allok(a2 | b2 | or4(q0) | or4(q1) | or4(q2) | or4(q3)) || spin > SPIN_MAX) break;
        __builtin_amdgcn_s_sleep(8);
      }
      x1 = rb(xv + rb(asf(a2) * rb(tr4((asf4(q0) + asf4(q1)) + (asf4(q2) + asf4(q3))))));
      gbv = asf(b2);
      pin(x1); pin(gbv);
    }
    {
      v4u y0, y1;
      int vo = (8 * w + row) * 256 + c4 * 16;
      LAS float* rw = Lf + L_REDG + w * 64 + col;
      pin(vo); pin(rw);
      for (unsigned spin = 0;; ++spin) {
        asm volatile("" ::: "memory");
        y0 = ldsc(ws, vo, WS_S5 + j * 72 * 256); y1 = ldsc(ws, vo, WS_S5 + j * 72 * 256 + 1024);
        __builtin_amdgcn_sched_barrier(0);
        y0 ^= T; y1 ^= T;
        if (allok(or4(y0) | or4(y1)) || spin > SPIN_MAX) break;
        __builtin_amdgcn_s_sleep(1);
      }
      *rw = tr4(asf4(y0) + asf4(y1));
    }
    bar();
    if (w == 0) {
      LAS float* r = Lf + L_REDG + col;
      const float y = ((r[0] + r[64]) + (r[128] + r[192])) + ((r[256] + r[320]) + (r[384] + r[448])) + r[512];
      f32x2 o = {x1 + rb(gbv * rb(y)), 0.f};
      p.out[64 * j + col] = (u16)(__builtin_bit_cast(unsigned, __builtin_convertvector(o, bf16x2)) & 0xFFFFu);
      if (j == 0 && lane == 0) __builtin_amdgcn_raw_buffer_store_b32(E + 1u, ws, 0, WS_EPOCH, 16);
    }
  }
}

}

extern "C" void kernel_launch(void* const* d_in, const int* in_sizes, int n_in, void* d_out, int out_size, void* d_ws, size_t ws_size,
                              hipStream_t stream) {
  Params p;
  p.x = (const u16*)d_in[0]; p.kc = (const u16*)d_in[1]; p.vc = (const u16*)d_in[2]; p.wq = (const u16*)d_in[3]; p.wo = (const u16*)d_in[4];
  p.wg = (const u16*)d_in[6]; p.wu = (const u16*)d_in[7]; p.wd = (const u16*)d_in[8]; p.c = (const u16*)d_in[9]; p.wm = (const u16*)d_in[10];
  p.out = (u16*)d_out; p.ws = (unsigned char*)d_ws;
  fused_decode<<<256, NTHR, 0, stream>>>(p);
}
```

```cpp
#include <hip/hip_runtime.h>
#include <hip/hip_bf16.h>
#include <stdint.h>

namespace {

typedef unsigned short u16;
typedef unsigned v4u __attribute__((ext_vector_type(4)));
typedef unsigned v2u __attribute__((ext_vector_type(2)));
typedef float f32x4 __attribute__((ext_vector_type(4)));
typedef float f32x2 __attribute__((ext_vector_type(2)));
typedef __bf16 bf16x8 __attribute__((ext_vector_type(8)));
typedef __bf16 bf16x2 __attribute__((ext_vector_type(2)));
typedef __amdgpu_buffer_rsrc_t rsrc_t;
#define LAS __attribute__((address_space(3)))

constexpr int D = 576, SK = 65536, WIN = 4096, FF = 4608;
constexpr int NTHR = 768;
constexpr int WS_EPOCH = 0;
constexpr int WS_S1 = 4096;
constexpr int WS_SQ = WS_S1 + 72 * 256;
constexpr int WS_SB = WS_SQ + 64 * 256;
constexpr int WS_S4 = WS_SB + 36 * 256;
constexpr int WS_S5 = WS_S4 + 144 * 256;
constexpr int WS_S3 = WS_S5 + 648 * 256;
constexpr int S3_STRIDE = 1280;
constexpr int WS_END = WS_S3 + 176 * S3_STRIDE;
constexpr int WS_STAMP = 1 << 20;
constexpr unsigned SPIN_MAX = 1u << 19;
#define POLL_SLEEP 1
#define PRE_WAIT 2
#define WO_POS 2
#define SLEEP_F 0
#define DELAY_A 0
#define DELAY_F 650

struct Params {
  const u16 *x, *kc, *vc, *wq, *wo, *wg, *wu, *wd, *c, *wm;
  u16* out;
  unsigned char* ws;
};

constexpr int SH_STRIDE = 160;
constexpr int L_SH = 0;
constexpr int L_REDA = L_SH + 12 * SH_STRIDE;
constexpr int L_REDG = L_REDA + 1536;
constexpr int L_ST = L_REDG + 1152;
constexpr int L_Q = L_ST + 64;
constexpr int L_MW = L_Q + 128;
constexpr int L_LW = L_MW + 32;
constexpr int L_CNT = L_LW + 32;
constexpr int L_O = L_CNT + 16;
constexpr int L_END = L_O + 12 * 256;

__device__ __forceinline__ rsrc_t mk(const void* p, unsigned bytes) {
  return __builtin_amdgcn_make_buffer_rsrc((void*)p, 0, (int)bytes, 0x00020000);
}
#define AUX_W 2
__device__ __forceinline__ v4u ldnt(rsrc_t r, int v, int s) { return __builtin_amdgcn_raw_buffer_load_b128(r, v, s, AUX_W); }
__device__ __forceinline__ v4u ldsc(rsrc_t r, int v, int s) { return __builtin_amdgcn_raw_buffer_load_b128(r, v, s, 16); }
__device__ __forceinline__ unsigned ldsc1(rsrc_t r, int v, int s) { return __builtin_amdgcn_raw_buffer_load_b32(r, v, s, 16); }
__device__ __forceinline__ void stsc4(rsrc_t r, v4u x, int v, int s) { __builtin_amdgcn_raw_buffer_store_b128(x, r, v, s, 16); }
__device__ __forceinline__ float asf(unsigned x) { return __uint_as_float(x); }
__device__ __forceinline__ unsigned asu(float x) { return __float_as_uint(x); }
template <class V> __device__ __forceinline__ void pin(V& x) { asm volatile("" : "+v"(x)); }
__device__ __forceinline__ unsigned rep(float x) {
  f32x2 v = {x, x};
  return __builtin_bit_cast(unsigned, __builtin_convertvector(v, bf16x2));
}
__device__ __forceinline__ float rb(float x) { return asf(rep(x) & 0xFFFF0000u); }
__device__ __forceinline__ float bf(u16 x) { return asf((unsigned)x << 16); }
__device__ __forceinline__ unsigned tg(float x, unsigned T) { return (asu(x) & 0xFFFFFF00u) | T; }
__device__ __forceinline__ unsigned tgr(float x, unsigned T) { return ((asu(x) + 0x80u) & 0xFFFFFF00u) | T; }
__device__ __forceinline__ v4u tg4(f32x4 y, v4u T) { return v4u{tg(y.x, T.x), tg(y.y, T.y), tg(y.z, T.z), tg(y.w, T.w)}; }
__device__ __forceinline__ v4u make_tag(unsigned e, unsigned xw, unsigned cw) {
  const unsigned t = (e + 1u) * 0x01010101u + (xw ^ (cw * 0x9E3779B1u)) * 0x85EBCA6Bu;
  return v4u{t & 0xFFu, (t >> 8) & 0xFFu, (t >> 16) & 0xFFu, t >> 24};
}
template <int C> __device__ __forceinline__ float dpp(float x);
__device__ __forceinline__ void pub64(rsrc_t ws, float v, v4u T, int lane, int soff);
__device__ __forceinline__ unsigned or4(v4u d) { return (d.x | d.y) | (d.z | d.w); }
__device__ __forceinline__ bool allok(unsigned bad) { return __all((bad & 0xFFu) == 0u); }
template <int C> __device__ __forceinline__ float dpp(float x) {
  return asf((unsigned)__builtin_amdgcn_update_dpp(0, (int)asu(x), C, 0xF, 0xF, true));
}
__device__ __forceinline__ void pub64(rsrc_t ws, float v, v4u T, int lane, int soff) {
  const v4u d = {tg(v, T.x), tg(dpp<0x55>(v), T.y), tg(dpp<0xAA>(v), T.z), tg(dpp<0xFF>(v), T.w)};
  if ((lane & 3) == 0) __builtin_amdgcn_raw_buffer_store_b128(d, ws, lane * 4, soff, 16);
}
__device__ __forceinline__ float xrow_sum(float x) {
  auto s = __builtin_amdgcn_permlane16_swap(asu(x), asu(x), false, false);
  x = asf(s[0]) + asf(s[1]);
  auto t = __builtin_amdgcn_permlane32_swap(asu(x), asu(x), false, false);
  return asf(t[0]) + asf(t[1]);
}
__device__ __forceinline__ float xrow_max(float x) {
  auto s = __builtin_amdgcn_permlane16_swap(asu(x), asu(x), false, false);
  x = __builtin_amdgcn_fmed3f(asf(s[0]), asf(s[1]), INFINITY);
  auto t = __builtin_amdgcn_permlane32_swap(asu(x), asu(x), false, false);
  return __builtin_amdgcn_fmed3f(asf(t[0]), asf(t[1]), INFINITY);
}
__device__ __forceinline__ float tr4(f32x4 v) {
  auto a = __builtin_amdgcn_permlane16_swap(asu(v.x), asu(v.y), false, false);
  auto b = __builtin_amdgcn_permlane16_swap(asu(v.z), asu(v.w), false, false);
  const float s01 = asf(a[0]) + asf(a[1]), s23 = asf(b[0]) + asf(b[1]);
  auto c = __builtin_amdgcn_permlane32_swap(asu(s01), asu(s23), false, false);
  return asf(c[0]) + asf(c[1]);
}
__device__ __forceinline__ float wave_sum2(float x, float y) {
  x += dpp<0xB1>(x); y += dpp<0xB1>(y);
  x += dpp<0x4E>(x); y += dpp<0x4E>(y);
  x += dpp<0x141>(x); y += dpp<0x141>(y);
  x += dpp<0x140>(x); y += dpp<0x140>(y);
  auto a = __builtin_amdgcn_permlane16_swap(asu(x), asu(y), false, false);
  const float s = asf(a[0]) + asf(a[1]);
  auto c = __builtin_amdgcn_permlane32_swap(asu(s), asu(s), false, false);
  return asf(c[0]) + asf(c[1]);
}
__device__ __forceinline__ f32x4 asf4(v4u d) { return __builtin_bit_cast(f32x4, d); }
__device__ __forceinline__ f32x4 mfma(v4u a, v4u b, f32x4 c) {
  return __builtin_amdgcn_mfma_f32_16x16x32_bf16(__builtin_bit_cast(bf16x8, a), __builtin_bit_cast(bf16x8, b), c, 0, 0, 0);
}
struct Mask { unsigned m0, m1, m2, m3; };
__device__ __forceinline__ v4u diag(unsigned hh, const Mask& k) { return v4u{hh & k.m0, hh & k.m1, hh & k.m2, hh & k.m3}; }
__device__ __forceinline__ void bar() {
  asm volatile("s_waitcnt lgkmcnt(0)" ::: "memory");
  __builtin_amdgcn_s_barrier();
  asm volatile("" ::: "memory");
}
__device__ __forceinline__ void rawbar() {
  __builtin_amdgcn_sched_barrier(0);
  __builtin_amdgcn_s_barrier();
  __builtin_amdgcn_sched_barrier(0);
}
__device__ __forceinline__ float silu(float v) { return v * __builtin_amdgcn_rcpf(1.f + __expf(-v)); }
__device__ __forceinline__ float fold1(float x) {
  auto t = __builtin_amdgcn_permlane32_swap(asu(x), asu(x), false, false);
  return asf(t[0]) + dpp<0x128>(asf(t[1]));
}
__device__ __forceinline__ f32x4 fold(f32x4 a) { return f32x4{fold1(a.x), fold1(a.y), fold1(a.z), fold1(a.w)}; }
__device__ __forceinline__ float fold2_1(float a, float b) {
  auto t = __builtin_amdgcn_permlane32_swap(asu(a), asu(b), false, false);
  return asf(t[0]) + dpp<0x128>(asf(t[1]));
}

#define STAMP(i) do {} while (0)
#define FINISH() do {} while (0)

struct Lane {
  Mask k;
  LAS unsigned* shw;
  LAS unsigned* shr;
  LAS float* put;
  bool putv;
};
__device__ __forceinline__ void shuf(LAS unsigned* wr, LAS unsigned* rd, float v, v4u& h0, v4u& h1) {
  *wr = rep(v);
  asm volatile("" ::: "memory");
  h0 = *(LAS v4u*)rd;
  h1 = *(LAS v4u*)(rd + 4);
}
__device__ __forceinline__ f32x4 gemv8(const v4u (&w)[8], v4u h0, v4u h1, const Mask& k) {
  f32x4 a = {0.f, 0.f, 0.f, 0.f}, b = {0.f, 0.f, 0.f, 0.f};
  const v4u a0 = diag(h0.x, k), a1 = diag(h0.y, k), a2 = diag(h0.z, k), a3 = diag(h0.w, k);
  a = mfma(a0, w[0], a); b = mfma(a1, w[1], b);
  const v4u a4 = diag(h1.x, k), a5 = diag(h1.y, k);
  a = mfma(a2, w[2], a); b = mfma(a3, w[3], b);
  const v4u a6 = diag(h1.z, k), a7 = diag(h1.w, k);
  a = mfma(a4, w[4], a); b = mfma(a5, w[5], b);
  a = mfma(a6, w[6], a); b = mfma(a7, w[7], b);
  return a + b;
}
__device__ __forceinline__ f32x2 ln_sums(LAS float* st) {
  const f32x4 a = *(LAS f32x4*)(st), b = *(LAS f32x4*)(st + 4), c = *(LAS f32x4*)(st + 8), d = *(LAS f32x4*)(st + 12);
  const f32x2 e = *(LAS f32x2*)(st + 16);
  const f32x4 t = (a + b) + (c + d);
  return f32x2{t.x + t.z + e.x, t.y + t.w + e.y};
}
template <int N> __device__ __forceinline__ float sumN(LAS float* red) {
  float a = 0.f, b = 0.f;
#pragma unroll
  for (int i = 0; i < N; i += 2) { a += red[i * 64]; b += red[i * 64 + 64]; }
  return a + b;
}
__device__ __forceinline__ void loads8(v4u (&w)[8], rsrc_t r, int stride, int lane, int base) {
  const int vo = (lane >> 3) * stride + (lane & 7) * 16;
#pragma unroll
  for (int s = 0; s < 8; ++s) w[s] = ldnt(r, vo, base + 8 * s * stride);
}
struct Tile18 { v4u w[18]; v2u c; };
__device__ __forceinline__ void tile18_load(Tile18& t, const u16* c, rsrc_t rwm, int ks, int cb, int lane) {
  t.c = *(const v2u*)(c + 144 * ks + 4 * (lane < 36 ? lane : 35));
  const int vo = (lane >> 3) * 6912 + (lane & 7) * 16;
#pragma unroll
  for (int s = 0; s < 18; ++s) t.w[s] = ldnt(rwm, vo, (144 * ks + 8 * s) * 6912 + cb);
}
__device__ __forceinline__ f32x4 tile18_mac(const Tile18& t, LAS unsigned* sh, int lane, const Mask& k) {
  if (lane < 36) {
    LAS unsigned* d = sh + (4 * (lane & 1)) * 20 + (lane >> 1);
    d[0] = rep(silu(asf(t.c.x << 16))); d[20] = rep(silu(asf(t.c.x & 0xFFFF0000u)));
    d[40] = rep(silu(asf(t.c.y << 16))); d[60] = rep(silu(asf(t.c.y & 0xFFFF0000u)));
  }
  asm volatile("" ::: "memory");
  LAS unsigned* s = sh + (lane >> 3) * 20;
  const v4u h0 = *(LAS v4u*)s, h1 = *(LAS v4u*)(s + 4), h2 = *(LAS v4u*)(s + 8), h3 = *(LAS v4u*)(s + 12);
  const v2u h4 = *(LAS v2u*)(s + 16);
  f32x4 a = {0.f, 0.f, 0.f, 0.f}, b = {0.f, 0.f, 0.f, 0.f};
  a = mfma(diag(h0.x, k), t.w[0], a); b = mfma(diag(h0.y, k), t.w[1], b); a = mfma(diag(h0.z, k), t.w[2], a); b = mfma(diag(h0.w, k), t.w[3], b);
  a = mfma(diag(h1.x, k), t.w[4], a); b = mfma(diag(h1.y, k), t.w[5], b); a = mfma(diag(h1.z, k), t.w[6], a); b = mfma(diag(h1.w, k), t.w[7], b);
  a = mfma(diag(h2.x, k), t.w[8], a); b = mfma(diag(h2.y, k), t.w[9], b); a = mfma(diag(h2.z, k), t.w[10], a); b = mfma(diag(h2.w, k), t.w[11], b);
  a = mfma(diag(h3.x, k), t.w[12], a); b = mfma(diag(h3.y, k), t.w[13], b); a = mfma(diag(h3.z, k), t.w[14], a); b = mfma(diag(h3.w, k), t.w[15], b);
  a = mfma(diag(h4.x, k), t.w[16], a); b = mfma(diag(h4.y, k), t.w[17], b);
  return a + b;
}
__device__ __forceinline__ void lds_arrive(LAS unsigned* c) {
  if ((threadIdx.x & 63) == 0) asm volatile("ds_add_u32 %0, %1" ::"v"(c), "v"(1u) : "memory");
  asm volatile("" ::: "memory");
}
__device__ __forceinline__ float vmax(float a, float b) { return __builtin_amdgcn_fmed3f(a, b, INFINITY); }
__device__ __forceinline__ float max12(LAS float* m) {
  const f32x4 b0 = *(LAS f32x4*)m, b1 = *(LAS f32x4*)(m + 4), b2 = *(LAS f32x4*)(m + 8);
  return vmax(vmax(vmax(vmax(b0.x, b0.y), vmax(b0.z, b0.w)), vmax(vmax(b1.x, b1.y), vmax(b1.z, b1.w))), vmax(vmax(b2.x, b2.y), vmax(b2.z, b2.w)));
}
__device__ __forceinline__ unsigned lds_inc(LAS unsigned* c) {
  asm volatile("" ::: "memory");
  unsigned old = 0;
  if ((threadIdx.x & 63) == 0) old = __hip_atomic_fetch_add(c, 1u, __ATOMIC_RELAXED, __HIP_MEMORY_SCOPE_WORKGROUP);
  old = (unsigned)__builtin_amdgcn_readfirstlane((int)old);
  asm volatile("" ::: "memory");
  return old;
}
__device__ __forceinline__ void lds_wait(LAS unsigned* c, unsigned need) {
  for (unsigned spin = 0; *(volatile LAS unsigned*)c < need && spin < (1u << 28); ++spin) {}
  asm volatile("" ::: "memory");
}
__device__ __forceinline__ void make_lane(Lane& ln, int lane, LAS unsigned* sh) {
  const unsigned mm = (lane & 1) ? 0xFFFF0000u : 0x0000FFFFu;
  const int d = (lane & 7) >> 1, c = lane & 15, g = lane >> 4;
  ln.k.m0 = d == 0 ? mm : 0u; ln.k.m1 = d == 1 ? mm : 0u; ln.k.m2 = d == 2 ? mm : 0u; ln.k.m3 = d == 3 ? mm : 0u;
  ln.shw = sh + 32 * (c & 1) + 8 * g + (c >> 1);
  ln.shr = sh + (lane >> 3) * 8;
  ln.put = (LAS float*)0 + (c >> 3) * 64 + 8 * (c & 7) + 4 * (g & 1);
  ln.putv = (c >> 3) == (g >> 1);
  pin(ln.k.m0); pin(ln.k.m1); pin(ln.k.m2); pin(ln.k.m3); pin(ln.shw); pin(ln.shr); pin(ln.put);
}
__device__ __forceinline__ void put_halves(const Lane& ln, int base_dw, f32x4 a) {
  if (ln.putv) *(LAS f32x4*)(ln.put + base_dw) = a;
}
__device__ __forceinline__ void modb_finish(const Tile18& t, LAS unsigned* sh, const Lane& ln, LAS float* Lf, int red_dw, LAS unsigned* cnt, int ks, int lane,
                                            rsrc_t ws, int tile, v4u T) {
  put_halves(ln, red_dw + ks * 128, tile18_mac(t, sh, lane, ln.k));
  if (lds_inc(cnt) == 3u) pub64(ws, rb(sumN<8>(Lf + red_dw + lane)), T, lane, WS_SB + tile * 256);
}

__global__ void __launch_bounds__(NTHR, 3) fused_decode(Params p) {
  __shared__ __attribute__((aligned(16))) unsigned smem[L_END];
  LAS unsigned* L = (LAS unsigned*)smem;
  LAS float* Lf = (LAS float*)smem;
  const int b = blockIdx.x, xcd = b & 7, slot = b >> 3;
  const int lane = threadIdx.x & 63, w = __builtin_amdgcn_readfirstlane(threadIdx.x >> 6);
  const int c4 = lane & 15, row = lane >> 4;
  const int col = 4 * c4 + row;
  STAMP(0);
  const unsigned long long t_start = __builtin_amdgcn_s_memrealtime();
#define LOAD_EPOCH() __builtin_amdgcn_sched_barrier(0); const unsigned Ev = __builtin_amdgcn_raw_buffer_load_b32(ws, 0, WS_EPOCH, 16); \
  const unsigned Xv = __builtin_amdgcn_raw_buffer_load_b32(mk(p.x, 4), 0, 0, 0), Cv = __builtin_amdgcn_raw_buffer_load_b32(mk(p.c, 4), 0, 0, 0); __builtin_amdgcn_sched_barrier(0)
#define TAG_NOW() (E = (unsigned)__builtin_amdgcn_readfirstlane((int)Ev), T = make_tag(E, (unsigned)__builtin_amdgcn_readfirstlane((int)Xv), (unsigned)__builtin_amdgcn_readfirstlane((int)Cv)))
#define GET_TAG() unsigned E; v4u T; TAG_NOW(); (void)E
  LAS unsigned* sh = L + L_SH + w * SH_STRIDE;
  const int lbase = (int)(size_t)(LAS void*)smem;
  Lane ln;

  if (slot <= 9) {
  const int rgate = (int)((size_t)p.wg >> 7), xstar = (3 - rgate) & 7;
  const bool qonly = slot == 9 && xcd == xstar;
  if (slot < 9 || qonly) {
    if (w >= 10) return;
    const int f = xcd * 9 + slot;
    __builtin_amdgcn_sched_barrier(0);
    const rsrc_t ws = mk(p.ws, WS_END), rwm = mk(p.wm, D * 6 * D * 2);
    if (w == 9) {
      __builtin_amdgcn_s_setprio(3);
      if (lane == 0) L[L_CNT] = 0u;
      if (qonly) { bar(); return; }
      Tile18 t;
      tile18_load(t, p.c, rwm, f / 18, (f % 18) * 128, lane);
      LOAD_EPOCH();
      bar();
      make_lane(ln, lane, sh);
      GET_TAG();
      const f32x4 a = fold(tile18_mac(t, sh, lane, ln.k));
      if (c4 < 8 && row < 2) stsc4(ws, v4u{tgr(a.x, T.x), tgr(a.y, T.y), tgr(a.z, T.z), tgr(a.w, T.w)}, (8 * c4 + 4 * row) * 4, WS_S1 + f * 256);
      STAMP(1);
      FINISH();
      return;
    }
    const int j = w;
    const int s0 = (3 - rgate - xcd) & 7;
    const bool hasq = qonly || !(slot == s0 || (s0 == 0 && slot == 8));
    const int n = qonly ? xcd * 8 + 7 : xcd * 8 + slot - (slot > s0 ? 1 : 0);
    const u16 xr = p.x[64 * j + col], x0r = p.x[0];
    LOAD_EPOCH();
    rawbar();
    v4u wt[8], wg[8], wu[8], wd[8];
    const rsrc_t rg = mk(p.wg, D * FF * 2), ru = mk(p.wu, D * FF * 2), rd = mk(p.wd, FF * D * 2);
    loads8(wt, mk(p.wq, hasq ? D * 4096 * 2 : 0), 8192, lane, 64 * j * 8192 + n * 128);
    unsigned tg0 = __builtin_amdgcn_raw_buffer_load_b32(rg, 0, 64 * j * FF * 2 + f * 128, 2);
    unsigned tg1 = __builtin_amdgcn_raw_buffer_load_b32(ru, 0, 64 * j * FF * 2 + f * 128, 2);
    unsigned tg2 = __builtin_amdgcn_raw_buffer_load_b32(rd, 0, 64 * f * 1152 + j * 128, 2);
    unsigned tg3 = __builtin_amdgcn_raw_buffer_load_b32(rg, 0, (64 * j + 63) * FF * 2 + f * 128, 2);
    unsigned tg4_ = __builtin_amdgcn_raw_buffer_load_b32(ru, 0, (64 * j + 63) * FF * 2 + f * 128, 2);
    unsigned tg5 = __builtin_amdgcn_raw_buffer_load_b32(rd, 0, (64 * f + 63) * 1152 + j * 128, 2);
    __builtin_amdgcn_sched_barrier(0);
    make_lane(ln, lane, sh);
    ln.put += lbase / 4;
    GET_TAG();
    const float x0 = bf(x0r);
    float xv = bf(xr);
    {
      const float d = xv - x0;
      const float s = wave_sum2(d, d * d);
      if ((lane & 47) == 0) Lf[L_ST + 2 * j + (lane >> 4)] = s;
    }
    lds_arrive(L + L_CNT);
    lds_wait(L + L_CNT, 9u);
    float mean1, lnv;
    {
      const f32x2 s = ln_sums(Lf + L_ST);
      const float dm = s.x * (1.f / D);
      mean1 = x0 + dm;
      const float var = fmaxf(s.y * (1.f / D) - dm * dm, 0.f);
      const float den = rb(__builtin_amdgcn_sqrtf(rb(rb(var) + 1e-5f)));
      lnv = rb(rb(xv - rb(mean1)) / den);
      pin(lnv); pin(mean1); pin(xv);
    }
    STAMP(1);
    if (hasq) {
      int o1 = (row * 18 + j) * 256 + c4 * 16;
      pin(o1);
      __builtin_amdgcn_s_waitcnt(0x0F70 | (PRE_WAIT + 6));
      v4u sa, ca;
      for (unsigned spin = 0;; ++spin) {
        asm volatile("" ::: "memory");
        sa = ldsc(ws, o1, WS_S1); ca = ldsc(ws, o1, WS_S1 + 9 * 256);
        __builtin_amdgcn_sched_barrier(0);
        sa ^= T; ca ^= T;
        if (allok(or4(sa) | or4(ca)) || spin > SPIN_MAX) break;
        __builtin_amdgcn_s_sleep(2);
      }
      const float h = rb(lnv * rb(1.f + rb(tr4(asf4(ca))))) + rb(tr4(asf4(sa)));
      STAMP(2);
      v4u h0, h1;
      shuf(ln.shw, ln.shr, h, h0, h1);
      put_halves(ln, L_REDA + j * 128, gemv8(wt, h0, h1, ln.k));
      bar();
      if (j == 0) pub64(ws, rb(sumN<18>(Lf + L_REDA + lane)), T, lane, WS_SQ + n * 256);
      rawbar();
    }
    asm volatile("" ::"v"(tg0), "v"(tg1), "v"(tg2), "v"(tg3), "v"(tg4_), "v"(tg5));
    if (qonly) { FINISH(); return; }
    if (SLEEP_F && hasq) __builtin_amdgcn_s_sleep(SLEEP_F);
    if (hasq) while (__builtin_amdgcn_s_memrealtime() - t_start < DELAY_F) __builtin_amdgcn_s_sleep(1);
    STAMP(3);
    loads8(wg, rg, FF * 2, lane, 64 * j * FF * 2 + f * 128);
    loads8(wu, ru, FF * 2, lane, 64 * j * FF * 2 + f * 128);
    loads8(wd, rd, 1152, lane, 64 * f * 1152 + j * 128);
    __builtin_amdgcn_sched_barrier(0);
    int o4 = (j * 16 + row) * 256 + c4 * 16, ob = j * 256 + col * 4, o5 = (8 * c4 + 4 * row) * 4;
    LAS float* redg_w = Lf + L_REDG + j * 128 + (row >> 1) * 64 + 8 * c4 + 4 * (row & 1);
    LAS float* redg_r = Lf + L_REDG + lane;
    LAS unsigned* shw2 = L + L_Q + (lane & 7) * 8 + (lane >> 3);
    LAS unsigned* shr2 = L + L_Q + (lane >> 3) * 8;
    const unsigned Tl = row == 0 ? T.x : row == 1 ? T.y : row == 2 ? T.z : T.w;
    pin(o4); pin(ob); pin(o5); pin(redg_w); pin(redg_r); pin(shw2); pin(shr2);
    asm volatile("s_waitcnt vmcnt(0)" ::: "memory");
    STAMP(8);
    rawbar();
    STAMP(4);
    float x1, sb, cb;
    {
      unsigned a, s2, c2;
      v4u q0, q1, q2, q3;
      q0 = ldsc(ws, o4, WS_S4); q1 = ldsc(ws, o4, WS_S4 + 1024); q2 = ldsc(ws, o4, WS_S4 + 2048); q3 = ldsc(ws, o4, WS_S4 + 3072);
      a = ldsc1(ws, ob, WS_SB); s2 = ldsc1(ws, ob, WS_SB + 9 * 256); c2 = ldsc1(ws, ob, WS_SB + 18 * 256);
      for (unsigned spin = 0; spin <= SPIN_MAX; ++spin) {
        asm volatile("" ::: "memory");
        bool again = false;
        if (!allok(or4(q0 ^ T))) { q0 = ldsc(ws, o4, WS_S4); again = true; }
        if (!allok(or4(q1 ^ T))) { q1 = ldsc(ws, o4, WS_S4 + 1024); again = true; }
        if (!allok(or4(q2 ^ T))) { q2 = ldsc(ws, o4, WS_S4 + 2048); again = true; }
        if (!allok(or4(q3 ^ T))) { q3 = ldsc(ws, o4, WS_S4 + 3072); again = true; }
        if (!allok((a ^ Tl) | (s2 ^ Tl) | (c2 ^ Tl))) {
          a = ldsc1(ws, ob, WS_SB); s2 = ldsc1(ws, ob, WS_SB + 9 * 256); c2 = ldsc1(ws, ob, WS_SB + 18 * 256);
          again = true;
        }
        if (!again) break;
      }
      q0 ^= T; q1 ^= T; q2 ^= T; q3 ^= T; a ^= Tl; s2 ^= Tl; c2 ^= Tl;
      x1 = rb(xv + rb(asf(a) * rb(tr4((asf4(q0) + asf4(q1)) + (asf4(q2) + asf4(q3))))));
      sb = asf(s2); cb = rb(1.f + asf(c2));
    }
    STAMP(5);
    {
      const float d = x1 - mean1;
      const float s = wave_sum2(d, d * d);
      if ((lane & 47) == 0) Lf[L_ST + 32 + 2 * j + (lane >> 4)] = s;
    }
    bar();
    {
      const f32x2 s = ln_sums(Lf + L_ST + 32);
      const float dm = s.x * (1.f / D);
      const float var = fmaxf(s.y * (1.f / D) - dm * dm, 0.f);
      const float den = rb(__builtin_amdgcn_sqrtf(rb(rb(var) + 1e-5f)));
      const float h2 = rb(rb(rb(x1 - rb(mean1 + dm)) * __builtin_amdgcn_rcpf(den)) * cb) + sb;
      v4u h0, h1;
      shuf(ln.shw, ln.shr, h2, h0, h1);
      const f32x4 g = gemv8(wg, h0, h1, ln.k), u = gemv8(wu, h0, h1, ln.k);
      const f32x4 t = {fold2_1(g.x, u.x), fold2_1(g.y, u.y), fold2_1(g.z, u.z), fold2_1(g.w, u.w)};
      if (c4 < 8) *(LAS f32x4*)redg_w = t;
    }
    bar();
    STAMP(6);
    if (j == 1) {
      float g = 0.f, u = 0.f, g2 = 0.f, u2 = 0.f;
#pragma unroll
      for (int i = 0; i < 8; i += 2) {
        g += redg_r[i * 128]; u += redg_r[i * 128 + 64];
        g2 += redg_r[i * 128 + 128]; u2 += redg_r[i * 128 + 192];
      }
      g = rb((g + redg_r[1024]) + g2); u = rb((u + redg_r[1088]) + u2);
      const float z = 1.5957691216057308f * (g + 0.044715f * g * g * g);
      *shw2 = rep(rb(g * __builtin_amdgcn_rcpf(1.f + __expf(-z))) * u);
    }
    bar();
    {
      const v4u h0 = *(LAS v4u*)shr2, h1 = *(LAS v4u*)(shr2 + 4);
      const f32x4 y = fold(gemv8(wd, h0, h1, ln.k));
      if (c4 < 8 && row < 2) stsc4(ws, tg4(y, T), o5, WS_S5 + (j * 72 + f) * 256);
    }
    STAMP(7);
    FINISH();
    return;
  }

  {
    int tile = 16 + (w >> 2) * 8 + xcd;
    if (w >= 8 && xcd >= 4) {
      const int r = xcd - 4 - ((xstar >= 4 && xcd > xstar) ? 1 : 0);
      if (r >= (xstar < 4 ? 3 : 2)) return;
      tile = 16 + r * 8 + xstar;
    }
    if (threadIdx.x < 3) L[L_CNT + threadIdx.x] = 0u;
    const rsrc_t ws = mk(p.ws, WS_END), rwm = mk(p.wm, D * 6 * D * 2);
    Tile18 t;
    tile18_load(t, p.c, rwm, w & 3, 2304 + tile * 128, lane);
    LOAD_EPOCH();
    __builtin_amdgcn_sched_barrier(0);
    bar();
    make_lane(ln, lane, sh);
    GET_TAG();
    modb_finish(t, sh, ln, Lf, L_REDA + (w >> 2) * 512, L + L_CNT + (w >> 2), w & 3, lane, ws, tile, T);
    STAMP(7);
    FINISH();
    return;
  }

  }
  const int g1 = (slot + 11) >> 5, g = 2 * xcd + g1, k = slot - 10 - 11 * g1;
  const bool light = k == 10;
  const int s3 = WS_S3 + (g * 11 + k) * S3_STRIDE;
  __builtin_amdgcn_sched_barrier(0);
  const rsrc_t ws = mk(p.ws, WS_END), rwm = mk(p.wm, D * 6 * D * 2);
  unsigned E = 0;
  v4u T = {0u, 0u, 0u, 0u};
  v4u wt[8];
  const rsrc_t rwo = mk(p.wo, (k < 9 && w < 4) ? 4096 * D * 2 : 0);
  if (light && w >= 8) {
    Tile18 t;
    tile18_load(t, p.c, rwm, w - 8, 2304 + g * 128, lane);
    LOAD_EPOCH();
    if (w == 8 && lane < 32) Lf[L_MW + lane] = -INFINITY;
    if (w == 9 && lane < 32) Lf[L_LW + lane] = 0.f;
    if (w == 10 && lane < 4) L[L_CNT + lane] = 0u;
    bar();
    make_lane(ln, lane, sh);
    TAG_NOW();
    modb_finish(t, sh, ln, Lf, L_REDA, L + L_CNT + 1, w - 8, lane, ws, g, T);
    STAMP(1);
    rawbar();
    rawbar();
  } else {
    const size_t kvoff = ((size_t)g * SK + (SK - WIN) + k * 384 + w * 32) * 128;
    const rsrc_t rk = mk(p.kc + kvoff, 8192), rv = mk(p.vc + kvoff, 8192);
    v4u kk[8], vv[8];
    if (WO_POS == 0) loads8(wt, rwo, 1152, lane, (256 * g + 64 * (w & 3)) * 1152 + k * 128);
    const int kvo = (lane & 7) * 256 + ((lane >> 3) & 1) * 64 + row * 16, vvo = row * 2048 + c4 * 16;
#pragma unroll
    for (int t = 0; t < 4; ++t) {
      kk[2 * t] = ldnt(rk, kvo, t * 2048);
      kk[2 * t + 1] = ldnt(rk, kvo + 128, t * 2048);
    }
    const unsigned tv0 = __builtin_amdgcn_raw_buffer_load_b32(rv, 0, 0, 2);
    const unsigned tv1 = __builtin_amdgcn_raw_buffer_load_b32(mk(p.out, (light && w == 0) ? D * 2 : 0), 0, 0, 0);
    const unsigned tv2 = __builtin_amdgcn_raw_buffer_load_b32(rwo, 0, (256 * g + 64 * (w & 3)) * 1152 + k * 128, 2);
    const unsigned tv3 = __builtin_amdgcn_raw_buffer_load_b32(rwo, 0, (256 * g + 64 * (w & 3) + 63) * 1152 + k * 128, 2);
    LOAD_EPOCH();
    if (!light) {
      if (w == 8 && lane < 32) Lf[L_MW + lane] = -INFINITY;
      if (w == 9 && lane < 32) Lf[L_LW + lane] = 0.f;
      if (w == 10 && lane < 4) L[L_CNT + lane] = 0u;
    }
    __builtin_amdgcn_sched_barrier(0);
    bar();
    if (WO_POS == 1) loads8(wt, rwo, 1152, lane, (256 * g + 64 * (w & 3)) * 1152 + k * 128);
#pragma unroll
    for (int s = 0; s < 8; ++s) vv[s] = ldnt(rv, vvo + s * 256, 0);
    __builtin_amdgcn_sched_barrier(0);
    make_lane(ln, lane, sh);
    ln.put += lbase / 4;
    TAG_NOW();
    LAS unsigned* qrd = L + L_Q + (lane & 1) * 64 + ((lane >> 1) & 1) * 16 + row * 4;
    LAS float* mwr = Lf + L_MW + ((lane >> 3) & 1) * 16;
    LAS float* owr = Lf + L_O + w * 256 + (row >> 1) * 128 + 8 * c4 + 4 * (row & 1);
    pin(qrd); pin(mwr); pin(owr);
    asm volatile("" ::"v"(tv0), "v"(tv1), "v"(tv2), "v"(tv3));
    STAMP(1);
    if (w == 0) {
      const int oq = lane * 16, sq = WS_SQ + g * 1024;
      v4u q;
      for (unsigned spin = 0;; ++spin) {
        asm volatile("" ::: "memory");
        q = ldsc(ws, oq, sq) ^ T;
        if (allok(or4(q)) || spin > SPIN_MAX) break;
        __builtin_amdgcn_s_sleep(2);
      }
      *(LAS v2u*)(L + L_Q + lane * 2) = v2u{(q.x >> 16) | q.y, (q.z >> 16) | q.w};
    }
    bar();
    STAMP(2);
    float sc = 0.f;
    {
      const v4u q0 = *(LAS v4u*)qrd, q1 = *(LAS v4u*)(qrd + 32);
#pragma unroll
      for (int t = 0; t < 4; ++t) {
        f32x4 d = {0.f, 0.f, 0.f, 0.f};
        d = mfma(q0, kk[2 * t], d);
        d = mfma(q1, kk[2 * t + 1], d);
        const float h0 = d.x + dpp<0x128>(d.z), h1 = d.w + dpp<0x128>(d.y);
        const float v = c4 < 8 ? h0 : h1;
        sc = row == t ? v : sc;
      }
      sc = rb(rb(sc) * 0.08838834764831845f);
      float m = sc;
      m = vmax(m, dpp<0xB1>(m)); m = vmax(m, dpp<0x4E>(m)); m = vmax(m, dpp<0x141>(m));
      m = xrow_max(m);
      if ((lane & 55) == 0) mwr[w] = m;
    }
    bar();
    STAMP(3);
    {
      const float M = max12(mwr);
      const float pe = __expf(sc - M);
      v4u h0, h1;
      shuf(sh + lane, ln.shr, pe, h0, h1);
      float l = pe;
      l += dpp<0xB1>(l); l += dpp<0x4E>(l); l += dpp<0x141>(l);
      l = xrow_sum(l);
      if ((lane & 55) == 0) mwr[32 + w] = l;
      lds_arrive(L + L_CNT + 2);
      v4u A[8];
      A[0] = diag(h0.x, ln.k); A[1] = diag(h0.y, ln.k); A[2] = diag(h0.z, ln.k); A[3] = diag(h0.w, ln.k);
      A[4] = diag(h1.x, ln.k); A[5] = diag(h1.y, ln.k); A[6] = diag(h1.z, ln.k); A[7] = diag(h1.w, ln.k);
      __builtin_amdgcn_sched_barrier(0);
      f32x4 o0 = {0.f, 0.f, 0.f, 0.f}, o1 = {0.f, 0.f, 0.f, 0.f};
      o0 = mfma(A[0], vv[0], o0); o1 = mfma(A[1], vv[1], o1); o0 = mfma(A[2], vv[2], o0); o1 = mfma(A[3], vv[3], o1);
      o0 = mfma(A[4], vv[4], o0); o1 = mfma(A[5], vv[5], o1); o0 = mfma(A[6], vv[6], o0); o1 = mfma(A[7], vv[7], o1);
      *(LAS f32x4*)owr = o0 + o1;
      lds_arrive(L + L_CNT);
    }
    STAMP(4);
    if (w >= 7) __builtin_amdgcn_s_setprio(3);
    if (w == 7) {
      lds_wait(L + L_CNT + 2, light ? 8u : 12u);
      LAS float* lw = Lf + L_LW + (lane & 1) * 16;
      LAS float* mw = Lf + L_MW + (lane & 1) * 16;
      const f32x4 a0 = *(LAS f32x4*)lw, a1 = *(LAS f32x4*)(lw + 4), a2 = *(LAS f32x4*)(lw + 8);
      const float l = ((a0.x + a0.y) + (a0.z + a0.w)) + ((a1.x + a1.y) + (a1.z + a1.w)) + ((a2.x + a2.y) + (a2.z + a2.w));
      const float M = max12(mw);
      const v4u ml = {tg(M, T.x), tg(l, T.y), tg(dpp<0x55>(M), T.z), tg(dpp<0x55>(l), T.w)};
      if (lane == 0) stsc4(ws, ml, 0, s3 + 1024);
    }
  }
  if (w >= 8) {
    __builtin_amdgcn_s_setprio(3);
    const int wp = w - 8;
    LAS float* o = Lf + L_O + row * 256 + wp * 64 + c4 * 4;
    int so = wp * 256 + c4 * 16;
    pin(o); pin(so);
    lds_wait(L + L_CNT, light ? 8u : 12u);
    f32x4 v = *(LAS f32x4*)o + *(LAS f32x4*)(o + 1024);
    if (!light) v += *(LAS f32x4*)(o + 2048);
    const f32x4 vs = {xrow_sum(v.x), xrow_sum(v.y), xrow_sum(v.z), xrow_sum(v.w)};
    if (lane < 16) stsc4(ws, tg4(vs, T), so, s3);
    if (WO_POS == 2) lds_arrive(L + L_CNT + 3);
    STAMP(5);
    if (!(light && g < 9 && w == 8)) { FINISH(); return; }
  }
  if (k < 9) {
    if (w >= 4) { FINISH(); return; }
    if (WO_POS == 2) { lds_wait(L + L_CNT + 3, 4u); loads8(wt, rwo, 1152, lane, (256 * g + 64 * w) * 1152 + k * 128); }
    __builtin_amdgcn_sched_barrier(0);
    const int k2 = row + 8 > 10 ? 10 : row + 8;
    const int gb = WS_S3 + g * 11 * S3_STRIDE;
    const bool hd = w >= 2;
    int oa = row * S3_STRIDE + w * 256 + c4 * 16, oc = k2 * S3_STRIDE + w * 256 + c4 * 16;
    int ma_ = row * S3_STRIDE + 1024, mc_ = k2 * S3_STRIDE + 1024;
    pin(oa); pin(oc); pin(ma_); pin(mc_);
    v4u m0, m1, m2;
    v4u o0, o1, o2;
    for (unsigned spin = 0;; ++spin) {
      asm volatile("" ::: "memory");
      m0 = ldsc(ws, ma_, gb); m1 = ldsc(ws, ma_, gb + 4 * S3_STRIDE); m2 = ldsc(ws, mc_, gb);
      o0 = ldsc(ws, oa, gb); o1 = ldsc(ws, oa, gb + 4 * S3_STRIDE); o2 = ldsc(ws, oc, gb);
      __builtin_amdgcn_sched_barrier(0);
      m0 ^= T; m1 ^= T; m2 ^= T; o0 ^= T; o1 ^= T; o2 ^= T;
      if (allok(or4(m0) | or4(m1) | or4(m2) | or4(o0) | or4(o1) | or4(o2)) || spin > SPIN_MAX) break;
      __builtin_amdgcn_s_sleep(POLL_SLEEP);
    }
    const float ma = asf(hd ? m0.z : m0.x), mb = asf(hd ? m1.z : m1.x), mc = row == 3 ? -INFINITY : asf(hd ? m2.z : m2.x);
    const float MM = xrow_max(vmax(vmax(ma, mb), mc));
    float fa = __expf(ma - MM), fb = __expf(mb - MM), fc = __expf(mc - MM);
    const float inv = __builtin_amdgcn_rcpf(xrow_sum(fa * asf(hd ? m0.w : m0.y) + fb * asf(hd ? m1.w : m1.y) + fc * asf(hd ? m2.w : m2.y)));
    STAMP(6);
    v4u h0, h1;
    shuf(ln.shw, ln.shr, tr4(asf4(o0) * fa + asf4(o1) * fb + asf4(o2) * fc) * inv, h0, h1);
    put_halves(ln, L_REDA + w * 128, gemv8(wt, h0, h1, ln.k));
    bar();
    if (w == 0) pub64(ws, sumN<8>(Lf + L_REDA + lane), T, lane, WS_S4 + (k * 16 + g) * 256);
    STAMP(7);
    FINISH();
    return;
  }
  if (!(light && g < 9) || w > 8) { FINISH(); return; }
  {
    const int j = g;
    float x1 = 0.f, gbv = 0.f;
    if (w == 0) {
      const float xv = bf(p.x[64 * j + col]);
      const unsigned Tl = row == 0 ? T.x : row == 1 ? T.y : row == 2 ? T.z : T.w;
      unsigned a2, b2;
      v4u q0, q1, q2, q3;
      for (unsigned spin = 0;; ++spin) {
        asm volatile("" ::: "memory");
        const int vo = (j * 16 + row) * 256 + c4 * 16;
        a2 = ldsc1(ws, col * 4, WS_SB + j * 256); b2 = ldsc1(ws, col * 4, WS_SB + (27 + j) * 256);
        q0 = ldsc(ws, vo, WS_S4); q1 = ldsc(ws, vo, WS_S4 + 1024); q2 = ldsc(ws, vo, WS_S4 + 2048); q3 = ldsc(ws, vo, WS_S4 + 3072);
        __builtin_amdgcn_sched_barrier(0);
        q0 ^= T; q1 ^= T; q2 ^= T; q3 ^= T; a2 ^= Tl; b2 ^= Tl;
        if (allok(a2 | b2 | or4(q0) | or4(q1) | or4(q2) | or4(q3)) || spin > SPIN_MAX) break;
        __builtin_amdgcn_s_sleep(8);
      }
      x1 = rb(xv + rb(asf(a2) * rb(tr4((asf4(q0) + asf4(q1)) + (asf4(q2) + asf4(q3))))));
      gbv = asf(b2);
      pin(x1); pin(gbv);
    }
    STAMP(6);
    {
      v4u y0, y1;
      int vo = (8 * w + row) * 256 + c4 * 16;
      LAS float* rw = Lf + L_REDG + w * 64 + col;
      pin(vo); pin(rw);
      for (unsigned spin = 0;; ++spin) {
        asm volatile("" ::: "memory");
        y0 = ldsc(ws, vo, WS_S5 + j * 72 * 256); y1 = ldsc(ws, vo, WS_S5 + j * 72 * 256 + 1024);
        __builtin_amdgcn_sched_barrier(0);
        y0 ^= T; y1 ^= T;
        if (allok(or4(y0) | or4(y1)) || spin > SPIN_MAX) break;
        __builtin_amdgcn_s_sleep(1);
      }
      *rw = tr4(asf4(y0) + asf4(y1));
    }
    bar();
    if (w == 0) {
      LAS float* r = Lf + L_REDG + col;
      const float y = ((r[0] + r[64]) + (r[128] + r[192])) + ((r[256] + r[320]) + (r[384] + r[448])) + r[512];
      f32x2 o = {x1 + rb(gbv * rb(y)), 0.f};
      p.out[64 * j + col] = (u16)(__builtin_bit_cast(unsigned, __builtin_convertvector(o, bf16x2)) & 0xFFFFu);
      if (j == 0 && lane == 0) __builtin_amdgcn_raw_buffer_store_b32(E + 1u, ws, 0, WS_EPOCH, 16);
    }
    STAMP(7);
    FINISH();
  }
}

}

extern "C" void kernel_launch(void* const* d_in, const int* in_sizes, int n_in, void* d_out, int out_size, void* d_ws, size_t ws_size,
                              hipStream_t stream) {
  Params p;
  p.x = (const u16*)d_in[0]; p.kc = (const u16*)d_in[1]; p.vc = (const u16*)d_in[2]; p.wq = (const u16*)d_in[3]; p.wo = (const u16*)d_in[4];
  p.wg = (const u16*)d_in[6]; p.wu = (const u16*)d_in[7]; p.wd = (const u16*)d_in[8]; p.c = (const u16*)d_in[9]; p.wm = (const u16*)d_in[10];
  p.out = (u16*)d_out; p.ws = (unsigned char*)d_ws;
  fused_decode<<<256, NTHR, 0, stream>>>(p);
}
```

```cpp
#include <hip/hip_runtime.h>
#include <hip/hip_bf16.h>
#include <stdint.h>

namespace {

typedef unsigned short u16;
typedef unsigned v4u __attribute__((ext_vector_type(4)));
typedef unsigned v2u __attribute__((ext_vector_type(2)));
typedef float f32x4 __attribute__((ext_vector_type(4)));
typedef float f32x2 __attribute__((ext_vector_type(2)));
typedef __bf16 bf16x8 __attribute__((ext_vector_type(8)));
typedef __bf16 bf16x2 __attribute__((ext_vector_type(2)));
typedef __amdgpu_buffer_rsrc_t rsrc_t;
#define LAS __attribute__((address_space(3)))

constexpr int D = 576, SK = 65536, WIN = 4096, FF = 4608;
constexpr int NTHR = 768;
constexpr int WS_EPOCH = 0;
constexpr int WS_S1 = 4096;
constexpr int WS_SQ = WS_S1 + 72 * 256;
constexpr int WS_SB = WS_SQ + 64 * 256;
constexpr int WS_S4 = WS_SB + 36 * 256;
constexpr int WS_S5 = WS_S4 + 144 * 256;
constexpr int WS_S3 = WS_S5 + 648 * 256;
constexpr int S3_STRIDE = 1280;
constexpr int WS_END = WS_S3 + 176 * S3_STRIDE;
constexpr unsigned SPIN_MAX = 1u << 19;

struct Params {
  const u16 *x, *kc, *vc, *wq, *wo, *wg, *wu, *wd, *c, *wm;
  u16* out;
  unsigned char* ws;
};

constexpr int SH_STRIDE = 160;
constexpr int L_SH = 0;
constexpr int L_REDA = L_SH + 12 * SH_STRIDE;
constexpr int L_REDG = L_REDA + 1536;
constexpr int L_ST = L_REDG + 1152;
constexpr int L_Q = L_ST + 64;
constexpr int L_MW = L_Q + 128;
constexpr int L_LW = L_MW + 32;
constexpr int L_CNT = L_LW + 32;
constexpr int L_O = L_CNT + 16;
constexpr int L_END = L_O + 12 * 256;

__device__ __forceinline__ rsrc_t mk(const void* p, unsigned bytes) {
  return __builtin_amdgcn_make_buffer_rsrc((void*)p, 0, (int)bytes, 0x00020000);
}
__device__ __forceinline__ v4u ldnt(rsrc_t r, int v, int s) { return __builtin_amdgcn_raw_buffer_load_b128(r, v, s, 2); }
__device__ __forceinline__ v4u ldsc(rsrc_t r, int v, int s) { return __builtin_amdgcn_raw_buffer_load_b128(r, v, s, 16); }
__device__ __forceinline__ unsigned ldsc1(rsrc_t r, int v, int s) { return __builtin_amdgcn_raw_buffer_load_b32(r, v, s, 16); }
__device__ __forceinline__ void stsc4(rsrc_t r, v4u x, int v, int s) { __builtin_amdgcn_raw_buffer_store_b128(x, r, v, s, 16); }
__device__ __forceinline__ float asf(unsigned x) { return __uint_as_float(x); }
__device__ __forceinline__ unsigned asu(float x) { return __float_as_uint(x); }
template <class V> __device__ __forceinline__ void pin(V& x) { asm volatile("" : "+v"(x)); }
__device__ __forceinline__ unsigned rep(float x) {
  f32x2 v = {x, x};
  return __builtin_bit_cast(unsigned, __builtin_convertvector(v, bf16x2));
}
__device__ __forceinline__ float rb(float x) { return asf(rep(x) & 0xFFFF0000u); }
__device__ __forceinline__ float bf(u16 x) { return asf((unsigned)x << 16); }
__device__ __forceinline__ unsigned tg(float x, unsigned T) { return (asu(x) & 0xFFFFFF00u) | T; }
__device__ __forceinline__ unsigned tgr(float x, unsigned T) { return ((asu(x) + 0x8u) & 0xFFFFFFF0u) | (T & 0xFu); }
__device__ __forceinline__ v4u tg4(f32x4 y, v4u T) { return v4u{tg(y.x, T.x), tg(y.y, T.y), tg(y.z, T.z), tg(y.w, T.w)}; }
__device__ __forceinline__ v4u make_tag(unsigned e, unsigned xw, unsigned cw) {
  const unsigned t = (e + 1u) * 0x01010101u + (xw ^ (cw * 0x9E3779B1u)) * 0x85EBCA6Bu;
  return v4u{t & 0xFFu, (t >> 8) & 0xFFu, (t >> 16) & 0xFFu, t >> 24};
}
template <int C> __device__ __forceinline__ float dpp(float x);
__device__ __forceinline__ void pub64(rsrc_t ws, float v, v4u T, int lane, int soff);
__device__ __forceinline__ unsigned or4(v4u d) { return (d.x | d.y) | (d.z | d.w); }
__device__ __forceinline__ bool allok(unsigned bad) { return __all((bad & 0xFFu) == 0u); }
template <int C> __device__ __forceinline__ float dpp(float x) {
  return asf((unsigned)__builtin_amdgcn_update_dpp(0, (int)asu(x), C, 0xF, 0xF, true));
}
__device__ __forceinline__ void pub64(rsrc_t ws, float v, v4u T, int lane, int soff) {
  const v4u d = {tg(v, T.x), tg(dpp<0x55>(v), T.y), tg(dpp<0xAA>(v), T.z), tg(dpp<0xFF>(v), T.w)};
  if ((lane & 3) == 0) __builtin_amdgcn_raw_buffer_store_b128(d, ws, lane * 4, soff, 16);
}
__device__ __forceinline__ float xrow_sum(float x) {
  auto s = __builtin_amdgcn_permlane16_swap(asu(x), asu(x), false, false);
  x = asf(s[0]) + asf(s[1]);
  auto t = __builtin_amdgcn_permlane32_swap(asu(x), asu(x), false, false);
  return asf(t[0]) + asf(t[1]);
}
__device__ __forceinline__ float xrow_max(float x) {
  auto s = __builtin_amdgcn_permlane16_swap(asu(x), asu(x), false, false);
  x = __builtin_amdgcn_fmed3f(asf(s[0]), asf(s[1]), INFINITY);
  auto t = __builtin_amdgcn_permlane32_swap(asu(x), asu(x), false, false);
  return __builtin_amdgcn_fmed3f(asf(t[0]), asf(t[1]), INFINITY);
}
__device__ __forceinline__ float tr4(f32x4 v) {
  auto a = __builtin_amdgcn_permlane16_swap(asu(v.x), asu(v.y), false, false);
  auto b = __builtin_amdgcn_permlane16_swap(asu(v.z), asu(v.w), false, false);
  const float s01 = asf(a[0]) + asf(a[1]), s23 = asf(b[0]) + asf(b[1]);
  auto c = __builtin_amdgcn_permlane32_swap(asu(s01), asu(s23), false, false);
  return asf(c[0]) + asf(c[1]);
}
__device__ __forceinline__ float wave_sum2(float x, float y) {
  x += dpp<0xB1>(x); y += dpp<0xB1>(y);
  x += dpp<0x4E>(x); y += dpp<0x4E>(y);
  x += dpp<0x141>(x); y += dpp<0x141>(y);
  x += dpp<0x140>(x); y += dpp<0x140>(y);
  auto a = __builtin_amdgcn_permlane16_swap(asu(x), asu(y), false, false);
  const float s = asf(a[0]) + asf(a[1]);
  auto c = __builtin_amdgcn_permlane32_swap(asu(s), asu(s), false, false);
  return asf(c[0]) + asf(c[1]);
}
__device__ __forceinline__ f32x4 asf4(v4u d) { return __builtin_bit_cast(f32x4, d); }
__device__ __forceinline__ f32x4 mfma(v4u a, v4u b, f32x4 c) {
  return __builtin_amdgcn_mfma_f32_16x16x32_bf16(__builtin_bit_cast(bf16x8, a), __builtin_bit_cast(bf16x8, b), c, 0, 0, 0);
}
struct Mask { unsigned m0, m1, m2, m3; };
__device__ __forceinline__ v4u diag(unsigned hh, const Mask& k) { return v4u{hh & k.m0, hh & k.m1, hh & k.m2, hh & k.m3}; }
__device__ __forceinline__ void bar() {
  asm volatile("s_waitcnt lgkmcnt(0)" ::: "memory");
  __builtin_amdgcn_s_barrier();
  asm volatile("" ::: "memory");
}
__device__ __forceinline__ void rawbar() {
  __builtin_amdgcn_sched_barrier(0);
  __builtin_amdgcn_s_barrier();
  __builtin_amdgcn_sched_barrier(0);
}
__device__ __forceinline__ float silu(float v) { return v / (1.f + expf(-v)); }
__device__ __forceinline__ float fold1(float x) {
  auto t = __builtin_amdgcn_permlane32_swap(asu(x), asu(x), false, false);
  return asf(t[0]) + dpp<0x128>(asf(t[1]));
}
__device__ __forceinline__ f32x4 fold(f32x4 a) { return f32x4{fold1(a.x), fold1(a.y), fold1(a.z), fold1(a.w)}; }
__device__ __forceinline__ float fold2_1(float a, float b) {
  auto t = __builtin_amdgcn_permlane32_swap(asu(a), asu(b), false, false);
  return asf(t[0]) + dpp<0x128>(asf(t[1]));
}


struct Lane {
  Mask k;
  LAS unsigned* shw;
  LAS unsigned* shr;
  LAS float* put;
  bool putv;
};
__device__ __forceinline__ void shuf(LAS unsigned* wr, LAS unsigned* rd, float v, v4u& h0, v4u& h1) {
  *wr = rep(v);
  asm volatile("" ::: "memory");
  h0 = *(LAS v4u*)rd;
  h1 = *(LAS v4u*)(rd + 4);
}
__device__ __forceinline__ f32x4 gemv8(const v4u (&w)[8], v4u h0, v4u h1, const Mask& k) {
  f32x4 a = {0.f, 0.f, 0.f, 0.f}, b = {0.f, 0.f, 0.f, 0.f};
  const v4u a0 = diag(h0.x, k), a1 = diag(h0.y, k), a2 = diag(h0.z, k), a3 = diag(h0.w, k);
  a = mfma(a0, w[0], a); b = mfma(a1, w[1], b);
  const v4u a4 = diag(h1.x, k), a5 = diag(h1.y, k);
  a = mfma(a2, w[2], a); b = mfma(a3, w[3], b);
  const v4u a6 = diag(h1.z, k), a7 = diag(h1.w, k);
  a = mfma(a4, w[4], a); b = mfma(a5, w[5], b);
  a = mfma(a6, w[6], a); b = mfma(a7, w[7], b);
  return a + b;
}
__device__ __forceinline__ f32x2 ln_sums(LAS float* st) {
  const f32x4 a = *(LAS f32x4*)(st), b = *(LAS f32x4*)(st + 4), c = *(LAS f32x4*)(st + 8), d = *(LAS f32x4*)(st + 12);
  const f32x2 e = *(LAS f32x2*)(st + 16);
  const f32x4 t = (a + b) + (c + d);
  return f32x2{t.x + t.z + e.x, t.y + t.w + e.y};
}
template <int N> __device__ __forceinline__ float sumN(LAS float* red) {
  float a = 0.f, b = 0.f;
#pragma unroll
  for (int i = 0; i < N; i += 2) { a += red[i * 64]; b += red[i * 64 + 64]; }
  return a + b;
}
__device__ __forceinline__ void loads8(v4u (&w)[8], rsrc_t r, int stride, int lane, int base) {
  const int vo = (lane >> 3) * stride + (lane & 7) * 16;
#pragma unroll
  for (int s = 0; s < 8; ++s) w[s] = ldnt(r, vo, base + 8 * s * stride);
}
struct Tile18 { v4u w[18]; v2u c; };
__device__ __forceinline__ void tile18_load(Tile18& t, const u16* c, rsrc_t rwm, int ks, int cb, int lane) {
  t.c = *(const v2u*)(c + 144 * ks + 4 * (lane < 36 ? lane : 35));
  const int vo = (lane >> 3) * 6912 + (lane & 7) * 16;
#pragma unroll
  for (int s = 0; s < 18; ++s) t.w[s] = ldnt(rwm, vo, (144 * ks + 8 * s) * 6912 + cb);
}
__device__ __forceinline__ f32x4 tile18_mac(const Tile18& t, LAS unsigned* sh, int lane, const Mask& k) {
  if (lane < 36) {
    LAS unsigned* d = sh + (4 * (lane & 1)) * 20 + (lane >> 1);
    d[0] = rep(silu(asf(t.c.x << 16))); d[20] = rep(silu(asf(t.c.x & 0xFFFF0000u)));
    d[40] = rep(silu(asf(t.c.y << 16))); d[60] = rep(silu(asf(t.c.y & 0xFFFF0000u)));
  }
  asm volatile("" ::: "memory");
  LAS unsigned* s = sh + (lane >> 3) * 20;
  const v4u h0 = *(LAS v4u*)s, h1 = *(LAS v4u*)(s + 4), h2 = *(LAS v4u*)(s + 8), h3 = *(LAS v4u*)(s + 12);
  const v2u h4 = *(LAS v2u*)(s + 16);
  f32x4 a = {0.f, 0.f, 0.f, 0.f}, b = {0.f, 0.f, 0.f, 0.f};
  a = mfma(diag(h0.x, k), t.w[0], a); b = mfma(diag(h0.y, k), t.w[1], b); a = mfma(diag(h0.z, k), t.w[2], a); b = mfma(diag(h0.w, k), t.w[3], b);
  a = mfma(diag(h1.x, k), t.w[4], a); b = mfma(diag(h1.y, k), t.w[5], b); a = mfma(diag(h1.z, k), t.w[6], a); b = mfma(diag(h1.w, k), t.w[7], b);
  a = mfma(diag(h2.x, k), t.w[8], a); b = mfma(diag(h2.y, k), t.w[9], b); a = mfma(diag(h2.z, k), t.w[10], a); b = mfma(diag(h2.w, k), t.w[11], b);
  a = mfma(diag(h3.x, k), t.w[12], a); b = mfma(diag(h3.y, k), t.w[13], b); a = mfma(diag(h3.z, k), t.w[14], a); b = mfma(diag(h3.w, k), t.w[15], b);
  a = mfma(diag(h4.x, k), t.w[16], a); b = mfma(diag(h4.y, k), t.w[17], b);
  return a + b;
}
__device__ __forceinline__ void lds_arrive(LAS unsigned* c) {
  if ((threadIdx.x & 63) == 0) asm volatile("ds_add_u32 %0, %1" ::"v"(c), "v"(1u) : "memory");
  asm volatile("" ::: "memory");
}
__device__ __forceinline__ float vmax(float a, float b) { return __builtin_amdgcn_fmed3f(a, b, INFINITY); }
__device__ __forceinline__ float max12(LAS float* m) {
  const f32x4 b0 = *(LAS f32x4*)m, b1 = *(LAS f32x4*)(m + 4), b2 = *(LAS f32x4*)(m + 8);
  return vmax(vmax(vmax(vmax(b0.x, b0.y), vmax(b0.z, b0.w)), vmax(vmax(b1.x, b1.y), vmax(b1.z, b1.w))), vmax(vmax(b2.x, b2.y), vmax(b2.z, b2.w)));
}
__device__ __forceinline__ unsigned lds_inc(LAS unsigned* c) {
  asm volatile("" ::: "memory");
  unsigned old = 0;
  if ((threadIdx.x & 63) == 0) old = __hip_atomic_fetch_add(c, 1u, __ATOMIC_RELAXED, __HIP_MEMORY_SCOPE_WORKGROUP);
  old = (unsigned)__builtin_amdgcn_readfirstlane((int)old);
  asm volatile("" ::: "memory");
  return old;
}
__device__ __forceinline__ void lds_wait(LAS unsigned* c, unsigned need) {
  for (unsigned spin = 0; *(volatile LAS unsigned*)c < need && spin < (1u << 28); ++spin) {}
  asm volatile("" ::: "memory");
}
__device__ __forceinline__ void make_lane(Lane& ln, int lane, LAS unsigned* sh, LAS float* Lf) {
  const unsigned mm = (lane & 1) ? 0xFFFF0000u : 0x0000FFFFu;
  const int d = (lane & 7) >> 1, c = lane & 15, g = lane >> 4;
  ln.k.m0 = d == 0 ? mm : 0u; ln.k.m1 = d == 1 ? mm : 0u; ln.k.m2 = d == 2 ? mm : 0u; ln.k.m3 = d == 3 ? mm : 0u;
  ln.shw = sh + 32 * (c & 1) + 8 * g + (c >> 1);
  ln.shr = sh + (lane >> 3) * 8;
  ln.put = Lf + (c >> 3) * 64 + 8 * (c & 7) + 4 * (g & 1);
  ln.putv = (c >> 3) == (g >> 1);
  pin(ln.k.m0); pin(ln.k.m1); pin(ln.k.m2); pin(ln.k.m3); pin(ln.shw); pin(ln.shr); pin(ln.put);
}
__device__ __forceinline__ void put_halves(const Lane& ln, int base_dw, f32x4 a) {
  if (ln.putv) *(LAS f32x4*)(ln.put + base_dw) = a;
}
__device__ __forceinline__ void modb_finish(const Tile18& t, LAS unsigned* sh, const Lane& ln, LAS float* Lf, int red_dw, LAS unsigned* cnt, int ks, int lane,
                                            rsrc_t ws, int tile, v4u T) {
  put_halves(ln, red_dw + ks * 128, tile18_mac(t, sh, lane, ln.k));
  if (lds_inc(cnt) == 3u) pub64(ws, rb(sumN<8>(Lf + red_dw + lane)), T, lane, WS_SB + tile * 256);
}

__global__ void __launch_bounds__(NTHR, 3) fused_decode(Params p) {
  __shared__ __attribute__((aligned(16))) unsigned smem[L_END];
  LAS unsigned* L = (LAS unsigned*)smem;
  LAS float* Lf = (LAS float*)smem;
  const int b = blockIdx.x, xcd = b & 7, slot = b >> 3;
  const int lane = threadIdx.x & 63, w = __builtin_amdgcn_readfirstlane(threadIdx.x >> 6);
  const int c4 = lane & 15, row = lane >> 4;
  const int col = 4 * c4 + row;
#define LOAD_EPOCH() __builtin_amdgcn_sched_barrier(0); const unsigned Ev = __builtin_amdgcn_raw_buffer_load_b32(ws, 0, WS_EPOCH, 16); \
  const unsigned Xv = __builtin_amdgcn_raw_buffer_load_b32(mk(p.x, 4), 0, 0, 0), Cv = __builtin_amdgcn_raw_buffer_load_b32(mk(p.c, 4), 0, 0, 0); __builtin_amdgcn_sched_barrier(0)
#define TAG_NOW() (E = (unsigned)__builtin_amdgcn_readfirstlane((int)Ev), T = make_tag(E, (unsigned)__builtin_amdgcn_readfirstlane((int)Xv), (unsigned)__builtin_amdgcn_readfirstlane((int)Cv)))
#define GET_TAG() unsigned E; v4u T; TAG_NOW(); (void)E
  LAS unsigned* sh = L + L_SH + w * SH_STRIDE;
  Lane ln;

  if (slot <= 9) {
  const int rgate = (int)((size_t)p.wg >> 7), xstar = (3 - rgate) & 7;
  const bool qonly = slot == 9 && xcd == xstar;
  if (slot < 9 || qonly) {
    if (w >= 10) return;
    const int f = xcd * 9 + slot;
    __builtin_amdgcn_sched_barrier(0);
    const rsrc_t ws = mk(p.ws, WS_END), rwm = mk(p.wm, D * 6 * D * 2);
    if (w == 9) {
      __builtin_amdgcn_s_setprio(3);
      if (lane == 0) L[L_CNT] = 0u;
      if (qonly) { bar(); return; }
      Tile18 t;
      tile18_load(t, p.c, rwm, f / 18, (f % 18) * 128, lane);
      LOAD_EPOCH();
      bar();
      make_lane(ln, lane, sh, Lf);
      GET_TAG();
      const f32x4 a = fold(tile18_mac(t, sh, lane, ln.k));
      if (c4 < 8 && row < 2) stsc4(ws, v4u{tgr(a.x, T.x), tgr(a.y, T.y), tgr(a.z, T.z), tgr(a.w, T.w)}, (8 * c4 + 4 * row) * 4, WS_S1 + f * 256);
      return;
    }
    const int j = w;
    const int s0 = (3 - rgate - xcd) & 7;
    const bool hasq = qonly || !(slot == s0 || (s0 == 0 && slot == 8));
    const int n = qonly ? xcd * 8 + 7 : xcd * 8 + slot - (slot > s0 ? 1 : 0);
    const u16 xr = p.x[64 * j + col], x0r = p.x[0];
    LOAD_EPOCH();
    rawbar();
    v4u wt[8], wg[8], wu[8], wd[8];
    const rsrc_t rg = mk(p.wg, D * FF * 2), ru = mk(p.wu, D * FF * 2), rd = mk(p.wd, FF * D * 2);
    loads8(wt, mk(p.wq, hasq ? D * 4096 * 2 : 0), 8192, lane, 64 * j * 8192 + n * 128);
    unsigned tg0 = __builtin_amdgcn_raw_buffer_load_b32(rg, 0, 64 * j * FF * 2 + f * 128, 2);
    unsigned tg1 = __builtin_amdgcn_raw_buffer_load_b32(ru, 0, 64 * j * FF * 2 + f * 128, 2);
    unsigned tg2 = __builtin_amdgcn_raw_buffer_load_b32(rd, 0, 64 * f * 1152 + j * 128, 2);
    __builtin_amdgcn_sched_barrier(0);
    make_lane(ln, lane, sh, Lf);
    GET_TAG();
    const float x0 = bf(x0r);
    float xv = bf(xr);
    {
      const float d = xv - x0;
      const float s = wave_sum2(d, d * d);
      if ((lane & 47) == 0) Lf[L_ST + 2 * j + (lane >> 4)] = s;
    }
    lds_arrive(L + L_CNT);
    lds_wait(L + L_CNT, 9u);
    float mean1, lnv;
    {
      const f32x2 s = ln_sums(Lf + L_ST);
      const float dm = s.x * (1.f / D);
      mean1 = x0 + dm;
      const float var = fmaxf(s.y * (1.f / D) - dm * dm, 0.f);
      const float den = rb(__builtin_amdgcn_sqrtf(rb(rb(var) + 1e-5f)));
      lnv = rb(rb(xv - rb(mean1)) / den);
      pin(lnv); pin(mean1); pin(xv);
    }
    if (hasq) {
      int o1 = (row * 18 + j) * 256 + c4 * 16;
      pin(o1);
      __builtin_amdgcn_s_waitcnt(0x0F70 | 5);
      v4u sa, ca;
      for (unsigned spin = 0;; ++spin) {
        asm volatile("" ::: "memory");
        sa = ldsc(ws, o1, WS_S1); ca = ldsc(ws, o1, WS_S1 + 9 * 256);
        __builtin_amdgcn_sched_barrier(0);
        sa ^= T & 0xFu; ca ^= T & 0xFu;
        if (__all(((or4(sa) | or4(ca)) & 0xFu) == 0u) || spin > SPIN_MAX) break;
        __builtin_amdgcn_s_sleep(2);
      }
      const float h = rb(lnv * rb(1.f + rb(tr4(asf4(ca))))) + rb(tr4(asf4(sa)));
      v4u h0, h1;
      shuf(ln.shw, ln.shr, h, h0, h1);
      put_halves(ln, L_REDA + j * 128, gemv8(wt, h0, h1, ln.k));
      bar();
      if (j == 0) pub64(ws, rb(sumN<18>(Lf + L_REDA + lane)), T, lane, WS_SQ + n * 256);
      rawbar();
    }
    asm volatile("" ::"v"(tg0), "v"(tg1), "v"(tg2));
    if (qonly) return;
    loads8(wg, rg, FF * 2, lane, 64 * j * FF * 2 + f * 128);
    loads8(wu, ru, FF * 2, lane, 64 * j * FF * 2 + f * 128);
    loads8(wd, rd, 1152, lane, 64 * f * 1152 + j * 128);
    __builtin_amdgcn_sched_barrier(0);
    int o4 = (j * 16 + row) * 256 + c4 * 16, ob = j * 256 + col * 4, o5 = (8 * c4 + 4 * row) * 4;
    LAS float* redg_w = Lf + L_REDG + j * 128 + (row >> 1) * 64 + 8 * c4 + 4 * (row & 1);
    LAS float* redg_r = Lf + L_REDG + lane;
    LAS unsigned* shw2 = L + L_Q + (lane & 7) * 8 + (lane >> 3);
    LAS unsigned* shr2 = L + L_Q + (lane >> 3) * 8;
    const unsigned Tl = row == 0 ? T.x : row == 1 ? T.y : row == 2 ? T.z : T.w;
    pin(o4); pin(ob); pin(o5); pin(redg_w); pin(redg_r); pin(shw2); pin(shr2);
    asm volatile("s_waitcnt vmcnt(0)" ::: "memory");
    rawbar();
    float x1, sb, cb;
    {
      unsigned a, s2, c2;
      v4u q0, q1, q2, q3;
      q0 = ldsc(ws, o4, WS_S4); q1 = ldsc(ws, o4, WS_S4 + 1024); q2 = ldsc(ws, o4, WS_S4 + 2048); q3 = ldsc(ws, o4, WS_S4 + 3072);
      a = ldsc1(ws, ob, WS_SB); s2 = ldsc1(ws, ob, WS_SB + 9 * 256); c2 = ldsc1(ws, ob, WS_SB + 18 * 256);
      for (unsigned spin = 0; spin <= SPIN_MAX; ++spin) {
        asm volatile("" ::: "memory");
        bool again = false;
        if (!allok(or4(q0 ^ T))) { q0 = ldsc(ws, o4, WS_S4); again = true; }
        if (!allok(or4(q1 ^ T))) { q1 = ldsc(ws, o4, WS_S4 + 1024); again = true; }
        if (!allok(or4(q2 ^ T))) { q2 = ldsc(ws, o4, WS_S4 + 2048); again = true; }
        if (!allok(or4(q3 ^ T))) { q3 = ldsc(ws, o4, WS_S4 + 3072); again = true; }
        if (!allok((a ^ Tl) | (s2 ^ Tl) | (c2 ^ Tl))) {
          a = ldsc1(ws, ob, WS_SB); s2 = ldsc1(ws, ob, WS_SB + 9 * 256); c2 = ldsc1(ws, ob, WS_SB + 18 * 256);
          again = true;
        }
        if (!again) break;
      }
      q0 ^= T; q1 ^= T; q2 ^= T; q3 ^= T; a ^= Tl; s2 ^= Tl; c2 ^= Tl;
      x1 = rb(xv + rb(asf(a) * rb(tr4((asf4(q0) + asf4(q1)) + (asf4(q2) + asf4(q3))))));
      sb = asf(s2); cb = rb(1.f + asf(c2));
    }
    {
      const float d = x1 - mean1;
      const float s = wave_sum2(d, d * d);
      if ((lane & 47) == 0) Lf[L_ST + 32 + 2 * j + (lane >> 4)] = s;
    }
    bar();
    {
      const f32x2 s = ln_sums(Lf + L_ST + 32);
      const float dm = s.x * (1.f / D);
      const float var = fmaxf(s.y * (1.f / D) - dm * dm, 0.f);
      const float den = rb(__builtin_amdgcn_sqrtf(rb(rb(var) + 1e-5f)));
      const float h2 = rb(rb(rb(x1 - rb(mean1 + dm)) * __builtin_amdgcn_rcpf(den)) * cb) + sb;
      v4u h0, h1;
      shuf(ln.shw, ln.shr, h2, h0, h1);
      const f32x4 g = gemv8(wg, h0, h1, ln.k), u = gemv8(wu, h0, h1, ln.k);
      const f32x4 t = {fold2_1(g.x, u.x), fold2_1(g.y, u.y), fold2_1(g.z, u.z), fold2_1(g.w, u.w)};
      if (c4 < 8) *(LAS f32x4*)redg_w = t;
    }
    bar();
    if (j == 1) {
      float g = 0.f, u = 0.f, g2 = 0.f, u2 = 0.f;
#pragma unroll
      for (int i = 0; i < 8; i += 2) {
        g += redg_r[i * 128]; u += redg_r[i * 128 + 64];
        g2 += redg_r[i * 128 + 128]; u2 += redg_r[i * 128 + 192];
      }
      g = rb((g + redg_r[1024]) + g2); u = rb((u + redg_r[1088]) + u2);
      const float z = 1.5957691216057308f * (g + 0.044715f * g * g * g);
      *shw2 = rep(rb(g * __builtin_amdgcn_rcpf(1.f + __expf(-z))) * u);
    }
    bar();
    {
      const v4u h0 = *(LAS v4u*)shr2, h1 = *(LAS v4u*)(shr2 + 4);
      const f32x4 y = fold(gemv8(wd, h0, h1, ln.k));
      if (c4 < 8 && row < 2) stsc4(ws, tg4(y, T), o5, WS_S5 + (j * 72 + f) * 256);
    }
    return;
  }

  {
    int tile = 16 + (w >> 2) * 8 + xcd;
    if (w >= 8 && xcd >= 4) {
      const int r = xcd - 4 - ((xstar >= 4 && xcd > xstar) ? 1 : 0);
      if (r >= (xstar < 4 ? 3 : 2)) return;
      tile = 16 + r * 8 + xstar;
    }
    if (threadIdx.x < 3) L[L_CNT + threadIdx.x] = 0u;
    const rsrc_t ws = mk(p.ws, WS_END), rwm = mk(p.wm, D * 6 * D * 2);
    Tile18 t;
    tile18_load(t, p.c, rwm, w & 3, 2304 + tile * 128, lane);
    LOAD_EPOCH();
    __builtin_amdgcn_sched_barrier(0);
    bar();
    make_lane(ln, lane, sh, Lf);
    GET_TAG();
    modb_finish(t, sh, ln, Lf, L_REDA + (w >> 2) * 512, L + L_CNT + (w >> 2), w & 3, lane, ws, tile, T);
    return;
  }

  }
  const int g1 = (slot + 11) >> 5, g = 2 * xcd + g1, k = slot - 10 - 11 * g1;
  const bool light = k == 10;
  const int s3 = WS_S3 + (g * 11 + k) * S3_STRIDE;
  __builtin_amdgcn_sched_barrier(0);
  const rsrc_t ws = mk(p.ws, WS_END), rwm = mk(p.wm, D * 6 * D * 2);
  unsigned E = 0;
  v4u T = {0u, 0u, 0u, 0u};
  v4u wt[8];
  const rsrc_t rwo = mk(p.wo, (k < 9 && w < 4) ? 4096 * D * 2 : 0);
  if (light && w >= 8) {
    Tile18 t;
    tile18_load(t, p.c, rwm, w - 8, 2304 + g * 128, lane);
    LOAD_EPOCH();
    if (w == 8 && lane < 32) Lf[L_MW + lane] = -INFINITY;
    if (w == 9 && lane < 32) Lf[L_LW + lane] = 0.f;
    if (w == 10 && lane < 3) L[L_CNT + lane] = 0u;
    bar();
    make_lane(ln, lane, sh, Lf);
    TAG_NOW();
    modb_finish(t, sh, ln, Lf, L_REDA, L + L_CNT + 1, w - 8, lane, ws, g, T);
    rawbar();
    rawbar();
  } else {
    const size_t kvoff = ((size_t)g * SK + (SK - WIN) + k * 384 + w * 32) * 128;
    const rsrc_t rk = mk(p.kc + kvoff, 8192), rv = mk(p.vc + kvoff, 8192);
    v4u kk[8], vv[8];
    const int kvo = (lane & 7) * 256 + ((lane >> 3) & 1) * 64 + row * 16, vvo = row * 2048 + c4 * 16;
#pragma unroll
    for (int t = 0; t < 4; ++t) {
      kk[2 * t] = ldnt(rk, kvo, t * 2048);
      kk[2 * t + 1] = ldnt(rk, kvo + 128, t * 2048);
    }
    const unsigned tv0 = __builtin_amdgcn_raw_buffer_load_b32(rv, 0, 0, 2);
    const unsigned tv1 = __builtin_amdgcn_raw_buffer_load_b32(mk(p.out, (light && w == 0) ? D * 2 : 0), 0, 0, 0);
    const unsigned tv2 = __builtin_amdgcn_raw_buffer_load_b32(rwo, 0, (256 * g + 64 * (w & 3)) * 1152 + k * 128, 2);
    LOAD_EPOCH();
    if (!light) {
      if (w == 8 && lane < 32) Lf[L_MW + lane] = -INFINITY;
      if (w == 9 && lane < 32) Lf[L_LW + lane] = 0.f;
      if (w == 10 && lane < 3) L[L_CNT + lane] = 0u;
    }
    __builtin_amdgcn_sched_barrier(0);
    bar();
    loads8(wt, rwo, 1152, lane, (256 * g + 64 * (w & 3)) * 1152 + k * 128);
#pragma unroll
    for (int s = 0; s < 8; ++s) vv[s] = ldnt(rv, vvo + s * 256, 0);
    __builtin_amdgcn_sched_barrier(0);
    make_lane(ln, lane, sh, Lf);
    TAG_NOW();
    LAS unsigned* qrd = L + L_Q + (lane & 1) * 64 + ((lane >> 1) & 1) * 16 + row * 4;
    LAS float* mwr = Lf + L_MW + ((lane >> 3) & 1) * 16;
    LAS float* owr = Lf + L_O + w * 256 + (row >> 1) * 128 + 8 * c4 + 4 * (row & 1);
    pin(qrd); pin(mwr); pin(owr);
    asm volatile("" ::"v"(tv0), "v"(tv1), "v"(tv2));
    if (w == 0) {
      const int oq = lane * 16, sq = WS_SQ + g * 1024;
      v4u q;
      for (unsigned spin = 0;; ++spin) {
        asm volatile("" ::: "memory");
        q = ldsc(ws, oq, sq) ^ T;
        if (allok(or4(q)) || spin > SPIN_MAX) break;
        __builtin_amdgcn_s_sleep(2);
      }
      *(LAS v2u*)(L + L_Q + lane * 2) = v2u{(q.x >> 16) | q.y, (q.z >> 16) | q.w};
    }
    bar();
    float sc = 0.f;
    {
      const v4u q0 = *(LAS v4u*)qrd, q1 = *(LAS v4u*)(qrd + 32);
#pragma unroll
      for (int t = 0; t < 4; ++t) {
        f32x4 d = {0.f, 0.f, 0.f, 0.f};
        d = mfma(q0, kk[2 * t], d);
        d = mfma(q1, kk[2 * t + 1], d);
        const float h0 = d.x + dpp<0x128>(d.z), h1 = d.w + dpp<0x128>(d.y);
        const float v = c4 < 8 ? h0 : h1;
        sc = row == t ? v : sc;
      }
      sc = rb(rb(sc) * 0.08838834764831845f);
      float m = sc;
      m = vmax(m, dpp<0xB1>(m)); m = vmax(m, dpp<0x4E>(m)); m = vmax(m, dpp<0x141>(m));
      m = xrow_max(m);
      if ((lane & 55) == 0) mwr[w] = m;
    }
    bar();
    {
      const float M = max12(mwr);
      const float pe = __expf(sc - M);
      v4u h0, h1;
      shuf(sh + lane, ln.shr, pe, h0, h1);
      float l = pe;
      l += dpp<0xB1>(l); l += dpp<0x4E>(l); l += dpp<0x141>(l);
      l = xrow_sum(l);
      if ((lane & 55) == 0) mwr[32 + w] = l;
      lds_arrive(L + L_CNT + 2);
      v4u A[8];
      A[0] = diag(h0.x, ln.k); A[1] = diag(h0.y, ln.k); A[2] = diag(h0.z, ln.k); A[3] = diag(h0.w, ln.k);
      A[4] = diag(h1.x, ln.k); A[5] = diag(h1.y, ln.k); A[6] = diag(h1.z, ln.k); A[7] = diag(h1.w, ln.k);
      __builtin_amdgcn_sched_barrier(0);
      f32x4 o0 = {0.f, 0.f, 0.f, 0.f}, o1 = {0.f, 0.f, 0.f, 0.f};
      o0 = mfma(A[0], vv[0], o0); o1 = mfma(A[1], vv[1], o1); o0 = mfma(A[2], vv[2], o0); o1 = mfma(A[3], vv[3], o1);
      o0 = mfma(A[4], vv[4], o0); o1 = mfma(A[5], vv[5], o1); o0 = mfma(A[6], vv[6], o0); o1 = mfma(A[7], vv[7], o1);
      *(LAS f32x4*)owr = o0 + o1;
      lds_arrive(L + L_CNT);
    }
    if (w >= 7) __builtin_amdgcn_s_setprio(3);
    if (w == 7) {
      lds_wait(L + L_CNT + 2, light ? 8u : 12u);
      LAS float* lw = Lf + L_LW + (lane & 1) * 16;
      LAS float* mw = Lf + L_MW + (lane & 1) * 16;
      const f32x4 a0 = *(LAS f32x4*)lw, a1 = *(LAS f32x4*)(lw + 4), a2 = *(LAS f32x4*)(lw + 8);
      const float l = ((a0.x + a0.y) + (a0.z + a0.w)) + ((a1.x + a1.y) + (a1.z + a1.w)) + ((a2.x + a2.y) + (a2.z + a2.w));
      const float M = max12(mw);
      const v4u ml = {tg(M, T.x), tg(l, T.y), tg(dpp<0x55>(M), T.z), tg(dpp<0x55>(l), T.w)};
      if (lane == 0) stsc4(ws, ml, 0, s3 + 1024);
    }
  }
  if (w >= 8) {
    __builtin_amdgcn_s_setprio(3);
    const int wp = w - 8;
    LAS float* o = Lf + L_O + row * 256 + wp * 64 + c4 * 4;
    int so = wp * 256 + c4 * 16;
    pin(o); pin(so);
    lds_wait(L + L_CNT, light ? 8u : 12u);
    f32x4 v = *(LAS f32x4*)o + *(LAS f32x4*)(o + 1024);
    if (!light) v += *(LAS f32x4*)(o + 2048);
    const f32x4 vs = {xrow_sum(v.x), xrow_sum(v.y), xrow_sum(v.z), xrow_sum(v.w)};
    if (lane < 16) stsc4(ws, tg4(vs, T), so, s3);
    if (!(light && g < 9 && w == 8)) return;
  }
  if (k < 9) {
    if (w >= 4) return;
    const int k2 = row + 8 > 10 ? 10 : row + 8;
    const int gb = WS_S3 + g * 11 * S3_STRIDE;
    const bool hd = w >= 2;
    int oa = row * S3_STRIDE + w * 256 + c4 * 16, oc = k2 * S3_STRIDE + w * 256 + c4 * 16;
    int ma_ = row * S3_STRIDE + 1024, mc_ = k2 * S3_STRIDE + 1024;
    pin(oa); pin(oc); pin(ma_); pin(mc_);
    v4u m0, m1, m2;
    v4u o0, o1, o2;
    for (unsigned spin = 0;; ++spin) {
      asm volatile("" ::: "memory");
      m0 = ldsc(ws, ma_, gb); m1 = ldsc(ws, ma_, gb + 4 * S3_STRIDE); m2 = ldsc(ws, mc_, gb);
      o0 = ldsc(ws, oa, gb); o1 = ldsc(ws, oa, gb + 4 * S3_STRIDE); o2 = ldsc(ws, oc, gb);
      __builtin_amdgcn_sched_barrier(0);
      m0 ^= T; m1 ^= T; m2 ^= T; o0 ^= T; o1 ^= T; o2 ^= T;
      if (allok(or4(m0) | or4(m1) | or4(m2) | or4(o0) | or4(o1) | or4(o2)) || spin > SPIN_MAX) break;
      __builtin_amdgcn_s_sleep(1);
    }
    const float ma = asf(hd ? m0.z : m0.x), mb = asf(hd ? m1.z : m1.x), mc = row == 3 ? -INFINITY : asf(hd ? m2.z : m2.x);
    const float MM = xrow_max(vmax(vmax(ma, mb), mc));
    float fa = __expf(ma - MM), fb = __expf(mb - MM), fc = __expf(mc - MM);
    const float inv = __builtin_amdgcn_rcpf(xrow_sum(fa * asf(hd ? m0.w : m0.y) + fb * asf(hd ? m1.w : m1.y) + fc * asf(hd ? m2.w : m2.y)));
    v4u h0, h1;
    shuf(ln.shw, ln.shr, tr4(asf4(o0) * fa + asf4(o1) * fb + asf4(o2) * fc) * inv, h0, h1);
    put_halves(ln, L_REDA + w * 128, gemv8(wt, h0, h1, ln.k));
    bar();
    if (w == 0) pub64(ws, sumN<8>(Lf + L_REDA + lane), T, lane, WS_S4 + (k * 16 + g) * 256);
    return;
  }
  if (!(light && g < 9) || w > 8) return;
  {
    const int j = g;
    float x1 = 0.f, gbv = 0.f;
    if (w == 0) {
      const float xv = bf(p.x[64 * j + col]);
      const unsigned Tl = row == 0 ? T.x : row == 1 ? T.y : row == 2 ? T.z : T.w;
      unsigned a2, b2;
      v4u q0, q1, q2, q3;
      for (unsigned spin = 0;; ++spin) {
        asm volatile("" ::: "memory");
        const int vo = (j * 16 + row) * 256 + c4 * 16;
        a2 = ldsc1(ws, col * 4, WS_SB + j * 256); b2 = ldsc1(ws, col * 4, WS_SB + (27 + j) * 256);
        q0 = ldsc(ws, vo, WS_S4); q1 = ldsc(ws, vo, WS_S4 + 1024); q2 = ldsc(ws, vo, WS_S4 + 2048); q3 = ldsc(ws, vo, WS_S4 + 3072);
        __builtin_amdgcn_sched_barrier(0);
        q0 ^= T; q1 ^= T; q2 ^= T; q3 ^= T; a2 ^= Tl; b2 ^= Tl;
        if (allok(a2 | b2 | or4(q0) | or4(q1) | or4(q2) | or4(q3)) || spin > SPIN_MAX) break;
        __builtin_amdgcn_s_sleep(8);
      }
      x1 = rb(xv + rb(asf(a2) * rb(tr4((asf4(q0) + asf4(q1)) + (asf4(q2) + asf4(q3))))));
      gbv = asf(b2);
      pin(x1); pin(gbv);
    }
    {
      v4u y0, y1;
      int vo = (8 * w + row) * 256 + c4 * 16;
      LAS float* rw = Lf + L_REDG + w * 64 + col;
      pin(vo); pin(rw);
      for (unsigned spin = 0;; ++spin) {
        asm volatile("" ::: "memory");
        y0 = ldsc(ws, vo, WS_S5 + j * 72 * 256); y1 = ldsc(ws, vo, WS_S5 + j * 72 * 256 + 1024);
        __builtin_amdgcn_sched_barrier(0);
        y0 ^= T; y1 ^= T;
        if (allok(or4(y0) | or4(y1)) || spin > SPIN_MAX) break;
        __builtin_amdgcn_s_sleep(1);
      }
      *rw = tr4(asf4(y0) + asf4(y1));
    }
    bar();
    if (w == 0) {
      LAS float* r = Lf + L_REDG + col;
      const float y = ((r[0] + r[64]) + (r[128] + r[192])) + ((r[256] + r[320]) + (r[384] + r[448])) + r[512];
      f32x2 o = {x1 + rb(gbv * rb(y)), 0.f};
      p.out[64 * j + col] = (u16)(__builtin_bit_cast(unsigned, __builtin_convertvector(o, bf16x2)) & 0xFFFFu);
      if (j == 0 && lane == 0) __builtin_amdgcn_raw_buffer_store_b32(E + 1u, ws, 0, WS_EPOCH, 16);
    }
  }
}

}

extern "C" void kernel_launch(void* const* d_in, const int* in_sizes, int n_in, void* d_out, int out_size, void* d_ws, size_t ws_size,
                              hipStream_t stream) {
  Params p;
  p.x = (const u16*)d_in[0]; p.kc = (const u16*)d_in[1]; p.vc = (const u16*)d_in[2]; p.wq = (const u16*)d_in[3]; p.wo = (const u16*)d_in[4];
  p.wg = (const u16*)d_in[6]; p.wu = (const u16*)d_in[7]; p.wd = (const u16*)d_in[8]; p.c = (const u16*)d_in[9]; p.wm = (const u16*)d_in[10];
  p.out = (u16*)d_out; p.ws = (unsigned char*)d_ws;
  fused_decode<<<256, NTHR, 0, stream>>>(p);
}
```
